```python
import torch

def _softmax(x, axis=-1):
    return torch.softmax(x, dim=axis)
def _gelu(x):
    return torch.nn.functional.gelu(x, approximate="tanh")
def _silu(x):
    return torch.nn.functional.silu(x)
def _sigmoid(x):
    return torch.sigmoid(x)
def _layernorm(x, axis=-1, eps=1e-5):
    m = x.mean(dim=axis, keepdim=True)
    v = x.var(dim=axis, keepdim=True, unbiased=False)
    return (x - m) / torch.sqrt(v + eps)
def _scatter_add(tgt, idx, src, axis=0):
    return tgt.clone().index_add_(axis, idx, src)
def _segment_sum(src, seg, n):
    out = torch.zeros((n,) + src.shape[1:], dtype=src.dtype)
    return out.index_add_(0, seg, src)
def _pad(x, pw):
    flat = tuple(v for pair in reversed(pw) for v in pair)
    return torch.nn.functional.pad(x, flat)

kernel_name = 'parallel_sum__linear_kernel_hedgehog__glu_gated_glu__prompt'

def setup_inputs():

    _w = (lambda a: (lambda t: t.to(torch.bfloat16) if t.is_floating_point() else t)(torch.as_tensor(a)))
    def _A_setup():
        B, S, D, H = 1, 2048, 1024, 32
        Dh = D // H
        return {
            "x": _w((lambda *s: torch.randn(*s, dtype=torch.bfloat16) * 0.05)(B, S, D)),
            "wqkv": _w((lambda *s: torch.randn(*s, dtype=torch.bfloat16) * 0.4)(D, 3 * D)),
            "w_phi": _w((lambda *s: torch.randn(*s, dtype=torch.bfloat16) * 0.4)(H, Dh, Dh) * (Dh ** -0.5)),
            "wo": _w((lambda *s: torch.randn(*s, dtype=torch.bfloat16) * 0.1)(D, D)),
        }
    _A_in = _A_setup()

    def _B_setup():
        B, S, D, F = 1, 2048, 1024, 3584
        return {
            "x": _w((lambda *s: torch.randn(*s, dtype=torch.bfloat16) * 0.05)(B, S, D)),
            "w_gate": _w((lambda *s: torch.randn(*s, dtype=torch.bfloat16) * 0.05)(D, F)),
            "w_up": _w((lambda *s: torch.randn(*s, dtype=torch.bfloat16) * 0.05)(D, F)),
            "w_down": _w((lambda *s: torch.randn(*s, dtype=torch.bfloat16) * 0.05)(F, D)),
        }
    _B_in = _B_setup()

    return {
        "A_x": _A_in["x"],
        "A_wqkv": _A_in["wqkv"],
        "A_w_phi": _A_in["w_phi"],
        "A_wo": _A_in["wo"],
        "B_x": _B_in["x"],
        "B_w_gate": _B_in["w_gate"],
        "B_w_up": _B_in["w_up"],
        "B_w_down": _B_in["w_down"],
    }

def reference(A_x, A_wqkv, A_w_phi, A_wo, B_x, B_w_gate, B_w_up, B_w_down):
    def _A_ref(x, wqkv, w_phi, wo):
        B, S, D = x.shape
        H = 32; Dh = D // H
        qkv = (lambda x, *d: x.permute(*d))((x @ wqkv).reshape(B, S, 3, H, Dh), 2, 0, 3, 1, 4)
        q, k, v = qkv[0], qkv[1], qkv[2]
        pq = torch.einsum("bhsd,hdf->bhsf", q, w_phi)
        pk = torch.einsum("bhsd,hdf->bhsf", k, w_phi)
        phi_q = torch.concatenate([_softmax(pq, axis=-1), _softmax(-pq, axis=-1)], axis=-1)
        phi_k = torch.concatenate([_softmax(pk, axis=-1), _softmax(-pk, axis=-1)], axis=-1)
        kv = torch.einsum("bhsf,bhsd->bhfd", phi_k, v)
        z  = torch.einsum("bhsf->bhf", phi_k)
        num = torch.einsum("bhqf,bhfd->bhqd", phi_q, kv)
        den = torch.einsum("bhqf,bhf->bhq", phi_q, z)[..., None] + 1e-6
        o = num / den
        return (lambda x, *d: x.permute(*d))(o, 0, 2, 1, 3).reshape(B, S, D) @ wo

    def _B_ref(x, w_gate, w_up, w_down):
        g = torch.einsum("bsd,df->bsf", x, w_gate)
        u = torch.einsum("bsd,df->bsf", x, w_up)
        h = _sigmoid(g) * u
        return torch.einsum("bsf,fd->bsd", h, w_down)

    x = A_x
    xn = _layernorm(x, axis=-1)
    return x + _A_ref(xn, A_wqkv, A_w_phi, A_wo) + _B_ref(xn, B_w_gate, B_w_up, B_w_down)
```

```cpp
#include <hip/hip_runtime.h>
#include <hip/hip_bf16.h>
#include <stdint.h>

#define LAS __attribute__((address_space(3)))
#define GAS __attribute__((address_space(1)))
#define DI __device__ __forceinline__
typedef __bf16 bf16x8 __attribute__((ext_vector_type(8)));
typedef __bf16 bf16x2 __attribute__((ext_vector_type(2)));
typedef short s16x4 __attribute__((ext_vector_type(4)));
typedef short s16x8 __attribute__((ext_vector_type(8)));
typedef float f32x4 __attribute__((ext_vector_type(4)));
typedef float f32x2 __attribute__((ext_vector_type(2)));
typedef unsigned u32x4 __attribute__((ext_vector_type(4)));
typedef unsigned u32x2 __attribute__((ext_vector_type(2)));

constexpr int S_ = 2048, D_ = 1024, F_ = 3584, H_ = 32;
constexpr int NMB = S_ / 16;
constexpr size_t WS_XN = 0;
constexpr size_t WS_A2 = WS_XN + (size_t)32 * NMB * 1024;
constexpr size_t WS_PHIQ = WS_A2 + (size_t)144 * NMB * 1024;
constexpr size_t WS_KVP = WS_PHIQ + (size_t)32 * NMB * 2048;
constexpr size_t WS_END = WS_KVP + (size_t)8 * 32 * 12 * 1024;

#define MFMA16(a, b, c) __builtin_amdgcn_mfma_f32_16x16x32_bf16((a), (b), (c), 0, 0, 0)
#define WAIT_V(n) asm volatile("s_waitcnt vmcnt(" #n ")" ::: "memory")
#define WAIT_L(n) asm volatile("s_waitcnt lgkmcnt(" #n ")" ::: "memory")
#define WAIT_L0() __builtin_amdgcn_s_waitcnt(0xC07F)
#define MFMA16_ACC(acc, a, b) asm volatile("v_mfma_f32_16x16x32_bf16 %0, %1, %2, %0" : "+v"(acc) : "v"(a), "v"(b))
#define BAR() __builtin_amdgcn_s_barrier()
#define SCHED() __builtin_amdgcn_sched_barrier(0)

DI unsigned pk2(float lo, float hi) {
    f32x2 v = {lo, hi};
    return __builtin_bit_cast(unsigned, __builtin_convertvector(v, bf16x2));
}
DI float bflo(unsigned u) { return __uint_as_float(u << 16); }
DI float bfhi(unsigned u) { return __uint_as_float(u & 0xffff0000u); }
DI float bfr(float x) { return bflo(pk2(x, x)); }
DI bf16x8 pack8(const f32x4& a, const f32x4& b) {
    u32x4 r = {pk2(a[0], a[1]), pk2(a[2], a[3]), pk2(b[0], b[1]), pk2(b[2], b[3])};
    return __builtin_bit_cast(bf16x8, r);
}
DI bf16x8 tr_frag(LAS const char* p0, LAS const char* p1) {
    s16x4 a = __builtin_amdgcn_ds_read_tr16_b64_v4i16((LAS s16x4*)p0);
    s16x4 b = __builtin_amdgcn_ds_read_tr16_b64_v4i16((LAS s16x4*)p1);
    s16x8 c = __builtin_shufflevector(a, b, 0, 1, 2, 3, 4, 5, 6, 7);
    return __builtin_bit_cast(bf16x8, c);
}
template <int OFF> DI void dma1(const char* g, unsigned voff, unsigned ldsdst) {
    asm volatile("s_mov_b32 m0, %2\n\ts_nop 0\n\tglobal_load_lds_dwordx4 %0, %1 offset:%3" :: "v"(voff), "s"(g), "s"(ldsdst), "i"(OFF) : "memory");
}
DI void dma2(const char* g, unsigned voff, unsigned ldsdst) {
    asm volatile("s_mov_b32 m0, %2\n\ts_nop 0\n\tglobal_load_lds_dwordx4 %0, %1\n\tglobal_load_lds_dwordx4 %0, %1 offset:1024"
                 :: "v"(voff), "s"(g), "s"(ldsdst) : "memory");
}
DI void dma4(const char* g, unsigned voff, unsigned ldsdst) {
    asm volatile("s_mov_b32 m0, %2\n\ts_nop 0\n\tglobal_load_lds_dwordx4 %0, %1\n\tglobal_load_lds_dwordx4 %0, %1 offset:1024\n\t"
                 "global_load_lds_dwordx4 %0, %1 offset:2048\n\tglobal_load_lds_dwordx4 %0, %1 offset:3072"
                 :: "v"(voff), "s"(g), "s"(ldsdst) : "memory");
}
#define OPAQUE(x) asm volatile("" : "+v"(x))
#define OPAQUE_S(x) asm volatile("" : "+s"(x))
DI float xmax(float v) {
    auto a = __builtin_amdgcn_permlane16_swap(__float_as_uint(v), __float_as_uint(v), false, false);
    v = fmaxf(__uint_as_float(a[0]), __uint_as_float(a[1]));
    auto b = __builtin_amdgcn_permlane32_swap(__float_as_uint(v), __float_as_uint(v), false, false);
    return fmaxf(__uint_as_float(b[0]), __uint_as_float(b[1]));
}
DI float xsum(float v) {
    auto a = __builtin_amdgcn_permlane16_swap(__float_as_uint(v), __float_as_uint(v), false, false);
    v = __uint_as_float(a[0]) + __uint_as_float(a[1]);
    auto b = __builtin_amdgcn_permlane32_swap(__float_as_uint(v), __float_as_uint(v), false, false);
    return __uint_as_float(b[0]) + __uint_as_float(b[1]);
}

__global__ __launch_bounds__(256) void k_ln(const uint16_t* __restrict__ x, char* __restrict__ ws) {
    __shared__ float red[2][4][8];
    const int tid = threadIdx.x, w = tid >> 6, l = tid & 63, c = l & 7, r8 = l >> 3;
    const int row = blockIdx.x * 8 + r8;
    const char* xr = (const char*)x + (size_t)row * 2048 + w * 512 + c * 16;
    u32x4 v[4];
#pragma unroll
    for (int i = 0; i < 4; ++i) v[i] = *(const u32x4*)(xr + i * 128);
    float f[32];
#pragma unroll
    for (int i = 0; i < 4; ++i)
#pragma unroll
        for (int j = 0; j < 4; ++j) { f[i * 8 + 2 * j] = bflo(v[i][j]); f[i * 8 + 2 * j + 1] = bfhi(v[i][j]); }
    float s = 0.f;
#pragma unroll
    for (int i = 0; i < 32; ++i) s += f[i];
    s += __shfl_xor(s, 1); s += __shfl_xor(s, 2); s += __shfl_xor(s, 4);
    if (c == 0) red[0][w][r8] = s;
    __syncthreads();
    const float mean = (red[0][0][r8] + red[0][1][r8] + red[0][2][r8] + red[0][3][r8]) * (1.0f / 1024.0f);
    float q = 0.f;
#pragma unroll
    for (int i = 0; i < 32; ++i) { const float d = f[i] - mean; q += d * d; }
    q += __shfl_xor(q, 1); q += __shfl_xor(q, 2); q += __shfl_xor(q, 4);
    if (c == 0) red[1][w][r8] = q;
    __syncthreads();
    const float var = (red[1][0][r8] + red[1][1][r8] + red[1][2][r8] + red[1][3][r8]) * (1.0f / 1024.0f);
    const float mb = bfr(mean);
    const float sd = bfr(sqrtf(bfr(bfr(var) + 1e-5f)));
    const float rs = 1.0f / sd;
    const int mbk = row >> 4, fr = row & 15;
#pragma unroll
    for (int i = 0; i < 4; ++i) {
        u32x4 o;
#pragma unroll
        for (int j = 0; j < 4; ++j) {
            const unsigned d2 = pk2(f[i * 8 + 2 * j] - mb, f[i * 8 + 2 * j + 1] - mb);
            o[j] = pk2(bflo(d2) * rs, bfhi(d2) * rs);
        }
        const int kb = (w * 4 + i) * 2 + (c >> 2), fq = c & 3;
        *(u32x4*)(ws + WS_XN + ((size_t)(kb * NMB + mbk)) * 1024 + (fq * 16 + fr) * 16) = o;
    }
}

constexpr int G1_SLOT = 36864;
constexpr int G1_WPHI = 4 * G1_SLOT;
constexpr int G1_LDS = G1_WPHI + 4096;

DI void hedgehog(const f32x4& p0, const f32x4& p1, bf16x8& pos, bf16x8& neg) {
    float x[8];
#pragma unroll
    for (int i = 0; i < 2; ++i) {
        const unsigned a = pk2(p0[2 * i], p0[2 * i + 1]), b = pk2(p1[2 * i], p1[2 * i + 1]);
        x[2 * i] = bflo(a); x[2 * i + 1] = bfhi(a); x[4 + 2 * i] = bflo(b); x[4 + 2 * i + 1] = bfhi(b);
    }
    float mx = x[0], mn = x[0];
#pragma unroll
    for (int i = 1; i < 8; ++i) { mx = fmaxf(mx, x[i]); mn = fminf(mn, x[i]); }
    mx = xmax(mx); mn = -xmax(-mn);
    const float L2E = 1.44269504088896340736f;
    float ep[8], en[8], sp = 0.f, sn = 0.f;
#pragma unroll
    for (int i = 0; i < 8; ++i) {
        ep[i] = __builtin_amdgcn_exp2f((x[i] - mx) * L2E); sp += ep[i];
        en[i] = __builtin_amdgcn_exp2f((mn - x[i]) * L2E); sn += en[i];
    }
    sp = xsum(sp); sn = xsum(sn);
    const float rp = __builtin_amdgcn_rcpf(sp), rn = __builtin_amdgcn_rcpf(sn);
    u32x4 a, b;
#pragma unroll
    for (int i = 0; i < 4; ++i) { a[i] = pk2(ep[2 * i] * rp, ep[2 * i + 1] * rp); b[i] = pk2(en[2 * i] * rn, en[2 * i + 1] * rn); }
    pos = __builtin_bit_cast(bf16x8, a); neg = __builtin_bit_cast(bf16x8, b);
}
DI f32x4 glu4(const f32x4& g, const f32x4& u) {
    f32x4 h;
#pragma unroll
    for (int i = 0; i < 4; ++i) {
        const float e = __builtin_amdgcn_exp2f(g[i] * -1.44269504088896340736f);
        h[i] = u[i] * __builtin_amdgcn_rcpf(1.0f + e);
    }
    return h;
}

__global__ __launch_bounds__(512, 2) void k_gemm1(const uint16_t* __restrict__ wqkv, const uint16_t* __restrict__ wphi,
                                                 const uint16_t* __restrict__ wgate, const uint16_t* __restrict__ wup,
                                                 char* __restrict__ ws) {
    __shared__ __attribute__((aligned(16))) char smem_[G1_LDS];
    LAS char* lds = (LAS char*)smem_;
    const int tid = threadIdx.x, l = tid & 63;
    const int w = __builtin_amdgcn_readfirstlane(tid >> 6);
    const int grp = w >> 2;
    const int wr = w & 3, wc = grp;
    const int bid = blockIdx.x, idx = bid >> 3;
    const int ct = (bid & 7) * 4 + (idx & 3), pm = idx >> 2;
    const bool kvt = (ct & 1) != 0;
    const int j = ct >> 1;
    const int F0 = kvt ? 2048 + 96 * j : 128 * j;

    const char* aptr = ws + WS_XN + (size_t)(pm * 16 + 2 * w) * 1024;
    const unsigned avoff = l * 16;
    const char* bptr[3]; unsigned bstep[3], bvoff[3], bdst[3];
#pragma unroll
    for (int i = 0; i < 3; ++i) {
        int t = grp == 0 ? 3 * w + i : 12 + 2 * (w - 4) + (i < 2 ? i : 0);
        const int u = t >> 1, hf = t & 1, uc = u / 5, r = u % 5;
        const uint16_t* base; int col, stride;
        if (!kvt) {
            if (r == 0) { base = wqkv; col = (2 * j + uc) * 32; stride = 3 * D_; }
            else { base = (r & 1) ? wgate : wup; col = F0 + 64 * uc + (r >= 3 ? 32 : 0); stride = F_; }
        } else {
            if (r < 2) { base = wqkv; col = D_ * (1 + r) + (2 * j + uc) * 32; stride = 3 * D_; }
            else if (r < 4) { base = (r == 2) ? wgate : wup; col = F0 + 32 * uc; stride = F_; }
            else { base = uc == 0 ? wgate : wup; col = F0 + 64; stride = F_; }
        }
        bptr[i] = (const char*)(base + (size_t)(16 * hf) * stride + col);
        bstep[i] = 64u * stride;
        bvoff[i] = (l >> 2) * stride * 2 + ((((l >> 1) & 1) ^ ((l >> 5) & 1)) * 32) + (l & 1) * 16;
        bdst[i] = (unsigned)(uintptr_t)lds + 16384 + t * 1024;
    }
    const unsigned lbase = (unsigned)(uintptr_t)lds;
    const unsigned adst = lbase + 2 * w * 1024;
#define G1_STAGE(slot) do { \
        dma2(aptr, avoff, adst + (slot) * G1_SLOT); \
        dma1<0>(bptr[0], bvoff[0], bdst[0] + (slot) * G1_SLOT); dma1<0>(bptr[1], bvoff[1], bdst[1] + (slot) * G1_SLOT); \
        if (grp == 0) dma1<0>(bptr[2], bvoff[2], bdst[2] + (slot) * G1_SLOT); \
        aptr += NMB * 1024; bptr[0] += bstep[0]; bptr[1] += bstep[1]; bptr[2] += bstep[2]; \
        OPAQUE_S(aptr); OPAQUE_S(bptr[0]); OPAQUE_S(bptr[1]); OPAQUE_S(bptr[2]); } while (0)

    const int G = l >> 4, q4 = (l >> 2) & 3, p4 = l & 3;
    const unsigned lp0 = G * 512 + q4 * 64 + ((G & 1) * 32) + p4 * 8;
    const unsigned lp1 = lp0 ^ 32;
    unsigned rA = lbase + wr * 4096 + l * 16;
    unsigned rB0 = lbase + 16384 + 5 * wc * 2048 + lp0;
    unsigned rB1 = lbase + 16384 + 5 * wc * 2048 + lp1;
    unsigned rB8 = kvt ? lbase + 16384 + 4 * 2048 + (wc ? lp1 : lp0) : rB0 + 4 * 2048;
    unsigned rB9 = kvt ? lbase + 16384 + 9 * 2048 + (wc ? lp1 : lp0) : rB1 + 4 * 2048;
#define G1_MOVE(d) do { rA += (d); rB0 += (d); rB1 += (d); rB8 += (d); rB9 += (d); \
        OPAQUE(rA); OPAQUE(rB0); OPAQUE(rB1); OPAQUE(rB8); OPAQUE(rB9); } while (0)
#define LP(a) ((LAS const char*)(uintptr_t)(a))

    f32x4 acc[4][10];
#pragma unroll
    for (int m = 0; m < 4; ++m)
#pragma unroll
        for (int n = 0; n < 10; ++n) acc[m][n] = (f32x4){0.f, 0.f, 0.f, 0.f};
    bf16x8 fa[4], fb[10];

#define G1_READ(so) do { \
        _Pragma("unroll") for (int m = 0; m < 4; ++m) fa[m] = *(LAS const bf16x8*)(LP(rA) + (so) + m * 1024); \
        _Pragma("unroll") for (int u = 0; u < 4; ++u) { \
            fb[2 * u] = tr_frag(LP(rB0) + (so) + u * 2048, LP(rB0) + (so) + u * 2048 + 256); \
            fb[2 * u + 1] = tr_frag(LP(rB1) + (so) + u * 2048, LP(rB1) + (so) + u * 2048 + 256); } \
        fb[8] = tr_frag(LP(rB8) + (so), LP(rB8) + (so) + 256); \
        fb[9] = tr_frag(LP(rB9) + (so), LP(rB9) + (so) + 256); } while (0)
#define G1_MMA() do { __builtin_amdgcn_s_setprio(1); \
        _Pragma("unroll") for (int n = 0; n < 10; ++n) _Pragma("unroll") for (int m = 0; m < 4; ++m) MFMA16_ACC(acc[m][n], fb[n], fa[m]); \
        __builtin_amdgcn_s_setprio(0); } while (0)
#define G1_PHASE(so, STG, VW) do { G1_READ(so); STG; WAIT_L0(); VW; SCHED(); BAR(); SCHED(); G1_MMA(); SCHED(); BAR(); SCHED(); } while (0)

    if (grp == 0) dma1<0>((const char*)wphi + (size_t)(2 * j) * 2048 + w * 1024, avoff, lbase + G1_WPHI + w * 1024);
    G1_STAGE(0); G1_STAGE(1); G1_STAGE(2);
    WAIT_V(8); BAR();
    if (grp == 1) BAR();
    G1_MOVE(0);
    SCHED();
    for (int it = 0; it < 7; ++it) {
        G1_PHASE(0, G1_STAGE(3), WAIT_V(8));
        G1_PHASE(G1_SLOT, G1_STAGE(0), WAIT_V(8));
        G1_MOVE(2 * G1_SLOT);
        G1_PHASE(0, G1_STAGE(1), WAIT_V(8));
        G1_PHASE(G1_SLOT, G1_STAGE(2), WAIT_V(8));
        G1_MOVE(-2 * G1_SLOT);
    }
    G1_PHASE(0, G1_STAGE(3), WAIT_V(8));
    G1_PHASE(G1_SLOT, (void)0, WAIT_V(4));
    G1_MOVE(2 * G1_SLOT);
    G1_PHASE(0, (void)0, WAIT_V(0));
    G1_PHASE(G1_SLOT, (void)0, (void)0);
    if (grp == 0) BAR();
    asm volatile("s_nop 7\n\ts_nop 7" ::: "memory");
    SCHED();

    const int fq = l >> 4, fr = l & 15;
    const int hh = 2 * j + wc;
    const int mb0 = pm * 16 + wr * 4;
    bf16x8 wp[2];
#pragma unroll
    for (int c = 0; c < 2; ++c) {
        LAS const char* p = lds + G1_WPHI + wc * 2048 + (4 * G + q4) * 64 + c * 32 + p4 * 8;
        wp[c] = tr_frag(p, p + 1024);
    }
    const f32x4 z4 = {0.f, 0.f, 0.f, 0.f};
    char* a2 = ws + WS_A2 + (size_t)l * 16;
    if (!kvt) {
        char* pq = ws + WS_PHIQ + ((size_t)hh * NMB + mb0) * 2048 + l * 16;
        const int kbA = (F0 + 64 * wc) >> 5;
#pragma unroll
        for (int m = 0; m < 4; ++m) {
            const bf16x8 qf = pack8(acc[m][0], acc[m][1]);
            const f32x4 p0 = MFMA16(wp[0], qf, z4), p1 = MFMA16(wp[1], qf, z4);
            bf16x8 pos, neg;
            hedgehog(p0, p1, pos, neg);
            *(bf16x8*)(pq + m * 2048) = pos;
            *(bf16x8*)(pq + m * 2048 + 1024) = neg;
            *(bf16x8*)(a2 + ((size_t)kbA * NMB + mb0 + m) * 1024) = pack8(glu4(acc[m][2], acc[m][4]), glu4(acc[m][3], acc[m][5]));
            *(bf16x8*)(a2 + ((size_t)(kbA + 1) * NMB + mb0 + m) * 1024) = pack8(glu4(acc[m][6], acc[m][8]), glu4(acc[m][7], acc[m][9]));
        }
    } else {
        u32x4 i0 = {0u, 0u, 0u, 0u}, i1 = {0u, 0u, 0u, 0u};
        {
            const int d = fr - 4 * fq;
            const unsigned lo = (d == 0) ? 0x3f80u : (d == 1) ? 0x3f800000u : 0u;
            const unsigned hi = (d == 2) ? 0x3f80u : (d == 3) ? 0x3f800000u : 0u;
            i0[0] = lo; i0[1] = hi; i1[2] = lo; i1[3] = hi;
        }
        const bf16x8 I0 = __builtin_bit_cast(bf16x8, i0), I1 = __builtin_bit_cast(bf16x8, i1);
        const u32x4 o4 = {0x3f803f80u, 0x3f803f80u, 0x3f803f80u, 0x3f803f80u};
        const bf16x8 ONES = __builtin_bit_cast(bf16x8, o4);
        f32x4 kv[2][3][2];
#pragma unroll
        for (int s = 0; s < 2; ++s)
#pragma unroll
            for (int c = 0; c < 3; ++c) { kv[s][c][0] = z4; kv[s][c][1] = z4; }
        const int kbA = (F0 + 32 * wc) >> 5, kbH = (F0 + 64) >> 5;
#pragma unroll
        for (int mp = 0; mp < 2; ++mp) {
            f32x4 T[2][2][2], TV[2][2];
#pragma unroll
            for (int mi = 0; mi < 2; ++mi) {
                const int m = 2 * mp + mi;
                const bf16x8 kf = pack8(acc[m][0], acc[m][1]);
                const f32x4 p0 = MFMA16(wp[0], kf, z4), p1 = MFMA16(wp[1], kf, z4);
                bf16x8 pos, neg;
                hedgehog(p0, p1, pos, neg);
                T[mi][0][0] = MFMA16(pos, I0, z4); T[mi][0][1] = MFMA16(pos, I1, z4);
                T[mi][1][0] = MFMA16(neg, I0, z4); T[mi][1][1] = MFMA16(neg, I1, z4);
                const bf16x8 vf = pack8(acc[m][2], acc[m][3]);
                TV[mi][0] = MFMA16(vf, I0, z4); TV[mi][1] = MFMA16(vf, I1, z4);
                *(bf16x8*)(a2 + ((size_t)kbA * NMB + mb0 + m) * 1024) = pack8(glu4(acc[m][4], acc[m][6]), glu4(acc[m][5], acc[m][7]));
                const f32x4 hh4 = glu4(acc[m][8], acc[m][9]);
                u32x2 hv = {pk2(hh4[0], hh4[1]), pk2(hh4[2], hh4[3])};
                *(u32x2*)(a2 + ((size_t)kbH * NMB + mb0 + m) * 1024 + 8 * wc) = hv;
            }
            const bf16x8 V0 = pack8(TV[0][0], TV[1][0]), V1 = pack8(TV[0][1], TV[1][1]);
#pragma unroll
            for (int s = 0; s < 2; ++s)
#pragma unroll
                for (int cc = 0; cc < 2; ++cc) {
                    const bf16x8 PK = pack8(T[0][s][cc], T[1][s][cc]);
                    kv[s][0][cc] = MFMA16(PK, V0, kv[s][0][cc]);
                    kv[s][1][cc] = MFMA16(PK, V1, kv[s][1][cc]);
                    kv[s][2][cc] = MFMA16(PK, ONES, kv[s][2][cc]);
                }
        }
        LAS char* red = lds + (wc * 4 + wr) * 12288 + l * 16;
#pragma unroll
        for (int s = 0; s < 2; ++s)
#pragma unroll
            for (int c = 0; c < 3; ++c)
#pragma unroll
                for (int cc = 0; cc < 2; ++cc) *(LAS f32x4*)(red + ((s * 3 + c) * 2 + cc) * 1024) = kv[s][c][cc];
        __syncthreads();
        char* kvp = ws + WS_KVP + ((size_t)(pm * 32 + hh) * 12 + 3 * wr) * 1024 + l * 16;
#pragma unroll
        for (int i = 0; i < 3; ++i) {
            LAS const char* p = lds + wc * 4 * 12288 + (3 * wr + i) * 1024 + l * 16;
            const f32x4 sum = (*(LAS const f32x4*)p + *(LAS const f32x4*)(p + 12288)) + (*(LAS const f32x4*)(p + 2 * 12288) + *(LAS const f32x4*)(p + 3 * 12288));
            *(f32x4*)(kvp + i * 1024) = sum;
        }
    }
}

__global__ __launch_bounds__(256) void k_o(char* __restrict__ ws) {
    __shared__ __attribute__((aligned(16))) char smem_[6 * 1024];
    LAS char* lds = (LAS char*)smem_;
    const int tid = threadIdx.x, l = tid & 63, w = tid >> 6;
    const int hh = blockIdx.x & 31, pm = blockIdx.x >> 5;
    const int mb0 = pm * 16 + w * 4;
    bf16x8 ph[4][2];
    const char* pq = ws + WS_PHIQ + ((size_t)hh * NMB + mb0) * 2048 + l * 16;
#pragma unroll
    for (int m = 0; m < 4; ++m) { ph[m][0] = *(const bf16x8*)(pq + m * 2048); ph[m][1] = *(const bf16x8*)(pq + m * 2048 + 1024); }
#pragma unroll
    for (int i = 0; i < 3; ++i) {
        const int id = 3 * w + i;
        const char* p = ws + WS_KVP + ((size_t)hh * 12 + id) * 1024 + l * 16;
        f32x4 s[8];
#pragma unroll
        for (int k = 0; k < 8; ++k) s[k] = *(const f32x4*)(p + (size_t)k * 32 * 12 * 1024);
        const f32x4 t = ((s[0] + s[1]) + (s[2] + s[3])) + ((s[4] + s[5]) + (s[6] + s[7]));
        u32x2 v = {pk2(t[0], t[1]), pk2(t[2], t[3])};
        *(LAS u32x2*)(lds + (id >> 1) * 1024 + l * 16 + (id & 1) * 8) = v;
    }
    __syncthreads();
    bf16x8 kf[2][3];
#pragma unroll
    for (int s = 0; s < 2; ++s)
#pragma unroll
        for (int c = 0; c < 3; ++c) kf[s][c] = *(LAS const bf16x8*)(lds + (s * 3 + c) * 1024 + l * 16);
    const f32x4 z4 = {0.f, 0.f, 0.f, 0.f};
    char* o = ws + WS_A2 + ((size_t)(112 + hh) * NMB + mb0) * 1024 + l * 16;
#pragma unroll
    for (int m = 0; m < 4; ++m) {
        f32x4 n0 = MFMA16(kf[0][0], ph[m][0], z4); n0 = MFMA16(kf[1][0], ph[m][1], n0);
        f32x4 n1 = MFMA16(kf[0][1], ph[m][0], z4); n1 = MFMA16(kf[1][1], ph[m][1], n1);
        f32x4 dn = MFMA16(kf[0][2], ph[m][0], z4); dn = MFMA16(kf[1][2], ph[m][1], dn);
        const float den = bfr(bfr(dn[0]) + 1e-6f);
        const float rd = 1.0f / den;
#pragma unroll
        for (int i = 0; i < 4; ++i) { n0[i] = bfr(n0[i]) * rd; n1[i] = bfr(n1[i]) * rd; }
        *(bf16x8*)(o + m * 1024) = pack8(n0, n1);
    }
}

constexpr int G2_SLOT = 24576;
constexpr int G2_NS = 6;
__global__ __launch_bounds__(256) void k_gemm2(const uint16_t* __restrict__ x, const uint16_t* __restrict__ wdown, const uint16_t* __restrict__ wo,
                                              const char* __restrict__ ws, uint16_t* __restrict__ out) {
    __shared__ __attribute__((aligned(16))) char smem_[G2_NS * G2_SLOT];
    LAS char* lds = (LAS char*)smem_;
    const int tid = threadIdx.x, l = tid & 63;
    const int w = __builtin_amdgcn_readfirstlane(tid >> 6);
    const int wr = w >> 1, wc = w & 1;
    const int bid = blockIdx.x, xcd = bid & 7, idx = bid >> 3;
    const int pm = (xcd >> 1) * 4 + (idx & 3), pn = (xcd & 1) * 8 + (idx >> 2);

    const char* aptr = ws + WS_A2 + ((size_t)(w >> 1) * NMB + pm * 8 + (w & 1) * 4) * 1024;
    const unsigned avoff = l * 16;
    const unsigned lbase = (unsigned)(uintptr_t)lds;
    const unsigned adst = lbase + w * 4096;
    const char* bptr = (const char*)(wdown + (size_t)(16 * w) * D_ + pn * 64);
    const char* bptr_o = (const char*)(wo + (size_t)(16 * w) * D_ + pn * 64);
    const unsigned bvoff = (l >> 3) * (D_ * 2) + ((((l & 7) >> 1) ^ ((l >> 4) & 3)) * 32) + (l & 1) * 16;
    const unsigned bvoff2 = bvoff + 8 * D_ * 2 - 1024;
    const unsigned bdst = lbase + 16384 + w * 2048;
    int tld = 0;
#define G2_STAGE(slot) do { \
        dma4(aptr, avoff, adst + (slot) * G2_SLOT); \
        dma1<0>(bptr, bvoff, bdst + (slot) * G2_SLOT); dma1<1024>(bptr, bvoff2, bdst + (slot) * G2_SLOT); \
        aptr += 2 * NMB * 1024; ++tld; bptr = (tld == 56) ? bptr_o : bptr + 64 * D_ * 2; OPAQUE_S(aptr); OPAQUE_S(bptr); } while (0)

    const int G = l >> 4, q4 = (l >> 2) & 3, p4 = l & 3;
    const int r8 = 4 * (G & 1) + q4;
    unsigned rA = lbase + wr * 4096 + l * 16;
    unsigned rB[2];
#pragma unroll
    for (int n = 0; n < 2; ++n) rB[n] = lbase + 16384 + (G >> 1) * 1024 + r8 * 128 + (((2 * wc + n) ^ (r8 >> 1)) * 32) + p4 * 8;
#define G2_MOVE(d) do { rA += (d); rB[0] += (d); rB[1] += (d); OPAQUE(rA); OPAQUE(rB[0]); OPAQUE(rB[1]); } while (0)

    f32x4 acc[4][2];
#pragma unroll
    for (int m = 0; m < 4; ++m) { acc[m][0] = (f32x4){0.f, 0.f, 0.f, 0.f}; acc[m][1] = acc[m][0]; }

#define G2_COMP(so) do { \
        _Pragma("unroll") for (int kk = 0; kk < 2; ++kk) { bf16x8 fa[4], fb[2]; \
            _Pragma("unroll") for (int n = 0; n < 2; ++n) fb[n] = tr_frag(LP(rB[n]) + (so) + kk * 4096, LP(rB[n]) + (so) + kk * 4096 + 2048); \
            _Pragma("unroll") for (int m = 0; m < 4; ++m) fa[m] = *(LAS const bf16x8*)(LP(rA) + (so) + kk * 8192 + m * 1024); \
            _Pragma("unroll") for (int m = 0; m < 4; ++m) _Pragma("unroll") for (int n = 0; n < 2; ++n) acc[m][n] = MFMA16(fb[n], fa[m], acc[m][n]); } } while (0)
#define G2_STEP(so, VW, STG) do { VW; WAIT_L(0); BAR(); SCHED(); STG; G2_COMP(so); SCHED(); } while (0)

    G2_STAGE(0); G2_STAGE(1); G2_STAGE(2); G2_STAGE(3); G2_STAGE(4);
    G2_MOVE(0);
    for (int it = 0; it < 11; ++it) {
        G2_STEP(0, WAIT_V(24), G2_STAGE(5)); G2_STEP(G2_SLOT, WAIT_V(24), G2_STAGE(0)); G2_MOVE(2 * G2_SLOT);
        G2_STEP(0, WAIT_V(24), G2_STAGE(1)); G2_STEP(G2_SLOT, WAIT_V(24), G2_STAGE(2)); G2_MOVE(2 * G2_SLOT);
        G2_STEP(0, WAIT_V(24), G2_STAGE(3)); G2_STEP(G2_SLOT, WAIT_V(24), G2_STAGE(4)); G2_MOVE(-4 * G2_SLOT);
    }
    G2_STEP(0, WAIT_V(24), G2_STAGE(5)); G2_STEP(G2_SLOT, WAIT_V(24), (void)0); G2_MOVE(2 * G2_SLOT);
    G2_STEP(0, WAIT_V(18), (void)0); G2_STEP(G2_SLOT, WAIT_V(12), (void)0); G2_MOVE(2 * G2_SLOT);
    G2_STEP(0, WAIT_V(6), (void)0); G2_STEP(G2_SLOT, WAIT_V(0), (void)0);

    const int fq = l >> 4, fr = l & 15;
#pragma unroll
    for (int m = 0; m < 4; ++m) {
        const size_t off = (size_t)(pm * 128 + wr * 64 + m * 16 + fr) * D_ + pn * 64 + wc * 32 + 4 * fq;
#pragma unroll
        for (int n = 0; n < 2; ++n) {
            const u32x2 xv = *(const u32x2*)(x + off + n * 16);
            u32x2 o = {pk2(bflo(xv[0]) + acc[m][n][0], bfhi(xv[0]) + acc[m][n][1]), pk2(bflo(xv[1]) + acc[m][n][2], bfhi(xv[1]) + acc[m][n][3])};
            *(u32x2*)(out + off + n * 16) = o;
        }
    }
}

extern "C" void kernel_launch(void* const* d_in, const int* in_sizes, int n_in, void* d_out, int out_size, void* d_ws, size_t ws_size,
                              hipStream_t stream) {
    const uint16_t* x = (const uint16_t*)d_in[0];
    const uint16_t* wqkv = (const uint16_t*)d_in[1];
    const uint16_t* wphi = (const uint16_t*)d_in[2];
    const uint16_t* wo = (const uint16_t*)d_in[3];
    const uint16_t* wgate = (const uint16_t*)d_in[5];
    const uint16_t* wup = (const uint16_t*)d_in[6];
    const uint16_t* wdown = (const uint16_t*)d_in[7];
    char* ws = (char*)d_ws;
    k_ln<<<256, 256, 0, stream>>>(x, ws);
    k_gemm1<<<256, 512, 0, stream>>>(wqkv, wphi, wgate, wup, ws);
    k_o<<<256, 256, 0, stream>>>(ws);
    k_gemm2<<<256, 256, 0, stream>>>(x, wdown, wo, ws, (uint16_t*)d_out);
}
```

```cpp
#include <hip/hip_runtime.h>
#include <hip/hip_bf16.h>
#include <stdint.h>

#define LAS __attribute__((address_space(3)))
#define GAS __attribute__((address_space(1)))
#define DI __device__ __forceinline__
typedef __bf16 bf16x8 __attribute__((ext_vector_type(8)));
typedef __bf16 bf16x2 __attribute__((ext_vector_type(2)));
typedef short s16x4 __attribute__((ext_vector_type(4)));
typedef short s16x8 __attribute__((ext_vector_type(8)));
typedef float f32x4 __attribute__((ext_vector_type(4)));
typedef float f32x2 __attribute__((ext_vector_type(2)));
typedef unsigned u32x4 __attribute__((ext_vector_type(4)));
typedef unsigned u32x2 __attribute__((ext_vector_type(2)));

constexpr int S_ = 2048, D_ = 1024, F_ = 3584, H_ = 32;
constexpr int NMB = S_ / 16;
constexpr size_t WS_XN = 0;
constexpr size_t WS_A2 = WS_XN + (size_t)32 * NMB * 1024;
constexpr size_t WS_PHIQ = WS_A2 + (size_t)144 * NMB * 1024;
constexpr size_t WS_KVP = WS_PHIQ + (size_t)32 * NMB * 2048;
constexpr size_t WS_PART = WS_KVP + (size_t)8 * 32 * 12 * 1024;
constexpr size_t WS_CNT = WS_PART + (size_t)128 * 65536;
constexpr size_t WS_END = WS_CNT + 132 * 256;

#define STAMP(kid, i) do {} while (0)
#define MFMA16(a, b, c) __builtin_amdgcn_mfma_f32_16x16x32_bf16((a), (b), (c), 0, 0, 0)
#define WAIT_V(n) asm volatile("s_waitcnt vmcnt(" #n ")" ::: "memory")
#define WAIT_L(n) asm volatile("s_waitcnt lgkmcnt(" #n ")" ::: "memory")
#define WAIT_L0() __builtin_amdgcn_s_waitcnt(0xC07F)
#define MFMA16_ACC(acc, a, b) asm volatile("v_mfma_f32_16x16x32_bf16 %0, %1, %2, %0" : "+v"(acc) : "v"(a), "v"(b))
#define BAR() __builtin_amdgcn_s_barrier()
#define SCHED() __builtin_amdgcn_sched_barrier(0)

DI unsigned pk2(float lo, float hi) {
    f32x2 v = {lo, hi};
    return __builtin_bit_cast(unsigned, __builtin_convertvector(v, bf16x2));
}
DI float bflo(unsigned u) { return __uint_as_float(u << 16); }
DI float bfhi(unsigned u) { return __uint_as_float(u & 0xffff0000u); }
DI float bfr(float x) { return bflo(pk2(x, x)); }
DI bf16x8 pack8(const f32x4& a, const f32x4& b) {
    u32x4 r = {pk2(a[0], a[1]), pk2(a[2], a[3]), pk2(b[0], b[1]), pk2(b[2], b[3])};
    return __builtin_bit_cast(bf16x8, r);
}
DI bf16x8 tr_frag(LAS const char* p0, LAS const char* p1) {
    s16x4 a = __builtin_amdgcn_ds_read_tr16_b64_v4i16((LAS s16x4*)p0);
    s16x4 b = __builtin_amdgcn_ds_read_tr16_b64_v4i16((LAS s16x4*)p1);
    s16x8 c = __builtin_shufflevector(a, b, 0, 1, 2, 3, 4, 5, 6, 7);
    return __builtin_bit_cast(bf16x8, c);
}
template <int OFF> DI void dma1(const char* g, unsigned voff, unsigned ldsdst) {
    asm volatile("s_mov_b32 m0, %2\n\ts_nop 0\n\tglobal_load_lds_dwordx4 %0, %1 offset:%3" :: "v"(voff), "s"(g), "s"(ldsdst), "i"(OFF) : "memory");
}
DI void dma2(const char* g, unsigned voff, unsigned ldsdst) {
    asm volatile("s_mov_b32 m0, %2\n\ts_nop 0\n\tglobal_load_lds_dwordx4 %0, %1\n\tglobal_load_lds_dwordx4 %0, %1 offset:1024"
                 :: "v"(voff), "s"(g), "s"(ldsdst) : "memory");
}
DI void dma4(const char* g, unsigned voff, unsigned ldsdst) {
    asm volatile("s_mov_b32 m0, %2\n\ts_nop 0\n\tglobal_load_lds_dwordx4 %0, %1\n\tglobal_load_lds_dwordx4 %0, %1 offset:1024\n\t"
                 "global_load_lds_dwordx4 %0, %1 offset:2048\n\tglobal_load_lds_dwordx4 %0, %1 offset:3072"
                 :: "v"(voff), "s"(g), "s"(ldsdst) : "memory");
}
#define OPAQUE(x) asm volatile("" : "+v"(x))
#define OPAQUE_S(x) asm volatile("" : "+s"(x))
DI float xmax(float v) {
    auto a = __builtin_amdgcn_permlane16_swap(__float_as_uint(v), __float_as_uint(v), false, false);
    v = fmaxf(__uint_as_float(a[0]), __uint_as_float(a[1]));
    auto b = __builtin_amdgcn_permlane32_swap(__float_as_uint(v), __float_as_uint(v), false, false);
    return fmaxf(__uint_as_float(b[0]), __uint_as_float(b[1]));
}
DI float xsum(float v) {
    auto a = __builtin_amdgcn_permlane16_swap(__float_as_uint(v), __float_as_uint(v), false, false);
    v = __uint_as_float(a[0]) + __uint_as_float(a[1]);
    auto b = __builtin_amdgcn_permlane32_swap(__float_as_uint(v), __float_as_uint(v), false, false);
    return __uint_as_float(b[0]) + __uint_as_float(b[1]);
}

__global__ __launch_bounds__(256) void k_ln(const uint16_t* __restrict__ x, char* __restrict__ ws) {
    __shared__ float red[2][4][8];
    const int tid = threadIdx.x, w = tid >> 6, l = tid & 63, c = l & 7, r8 = l >> 3;
    const int row = blockIdx.x * 8 + r8;
    STAMP(0, 0);
    const char* xr = (const char*)x + (size_t)row * 2048 + w * 512 + c * 16;
    u32x4 v[4];
#pragma unroll
    for (int i = 0; i < 4; ++i) v[i] = *(const u32x4*)(xr + i * 128);
    float f[32];
#pragma unroll
    for (int i = 0; i < 4; ++i)
#pragma unroll
        for (int j = 0; j < 4; ++j) { f[i * 8 + 2 * j] = bflo(v[i][j]); f[i * 8 + 2 * j + 1] = bfhi(v[i][j]); }
    float s = 0.f;
#pragma unroll
    for (int i = 0; i < 32; ++i) s += f[i];
    s += __shfl_xor(s, 1); s += __shfl_xor(s, 2); s += __shfl_xor(s, 4);
    if (c == 0) red[0][w][r8] = s;
    __syncthreads();
    const float mean = (red[0][0][r8] + red[0][1][r8] + red[0][2][r8] + red[0][3][r8]) * (1.0f / 1024.0f);
    float q = 0.f;
#pragma unroll
    for (int i = 0; i < 32; ++i) { const float d = f[i] - mean; q += d * d; }
    q += __shfl_xor(q, 1); q += __shfl_xor(q, 2); q += __shfl_xor(q, 4);
    if (c == 0) red[1][w][r8] = q;
    __syncthreads();
    const float var = (red[1][0][r8] + red[1][1][r8] + red[1][2][r8] + red[1][3][r8]) * (1.0f / 1024.0f);
    const float mb = bfr(mean);
    const float sd = bfr(sqrtf(bfr(bfr(var) + 1e-5f)));
    const float rs = 1.0f / sd;
    const int mbk = row >> 4, fr = row & 15;
#pragma unroll
    for (int i = 0; i < 4; ++i) {
        u32x4 o;
#pragma unroll
        for (int j = 0; j < 4; ++j) {
            const unsigned d2 = pk2(f[i * 8 + 2 * j] - mb, f[i * 8 + 2 * j + 1] - mb);
            o[j] = pk2(bflo(d2) * rs, bfhi(d2) * rs);
        }
        const int kb = (w * 4 + i) * 2 + (c >> 2), fq = c & 3;
        *(u32x4*)(ws + WS_XN + ((size_t)(kb * NMB + mbk)) * 1024 + (fq * 16 + fr) * 16) = o;
    }
    STAMP(0, 7);
}

constexpr int G1_SLOT = 36864;
constexpr int G1_WPHI = 4 * G1_SLOT;
constexpr int G1_LDS = G1_WPHI + 4096;

DI void hedgehog(const f32x4& p0, const f32x4& p1, bf16x8& pos, bf16x8& neg) {
    float x[8];
#pragma unroll
    for (int i = 0; i < 2; ++i) {
        const unsigned a = pk2(p0[2 * i], p0[2 * i + 1]), b = pk2(p1[2 * i], p1[2 * i + 1]);
        x[2 * i] = bflo(a); x[2 * i + 1] = bfhi(a); x[4 + 2 * i] = bflo(b); x[4 + 2 * i + 1] = bfhi(b);
    }
    float mx = x[0], mn = x[0];
#pragma unroll
    for (int i = 1; i < 8; ++i) { mx = fmaxf(mx, x[i]); mn = fminf(mn, x[i]); }
    mx = xmax(mx); mn = -xmax(-mn);
    const float L2E = 1.44269504088896340736f;
    float ep[8], en[8], sp = 0.f, sn = 0.f;
#pragma unroll
    for (int i = 0; i < 8; ++i) {
        ep[i] = __builtin_amdgcn_exp2f((x[i] - mx) * L2E); sp += ep[i];
        en[i] = __builtin_amdgcn_exp2f((mn - x[i]) * L2E); sn += en[i];
    }
    sp = xsum(sp); sn = xsum(sn);
    const float rp = __builtin_amdgcn_rcpf(sp), rn = __builtin_amdgcn_rcpf(sn);
    u32x4 a, b;
#pragma unroll
    for (int i = 0; i < 4; ++i) { a[i] = pk2(ep[2 * i] * rp, ep[2 * i + 1] * rp); b[i] = pk2(en[2 * i] * rn, en[2 * i + 1] * rn); }
    pos = __builtin_bit_cast(bf16x8, a); neg = __builtin_bit_cast(bf16x8, b);
}
DI f32x4 glu4(const f32x4& g, const f32x4& u) {
    f32x4 h;
#pragma unroll
    for (int i = 0; i < 4; ++i) {
        const float e = __builtin_amdgcn_exp2f(g[i] * -1.44269504088896340736f);
        h[i] = u[i] * __builtin_amdgcn_rcpf(1.0f + e);
    }
    return h;
}

__global__ __launch_bounds__(512, 2) void k_gemm1(const uint16_t* __restrict__ wqkv, const uint16_t* __restrict__ wphi,
                                                 const uint16_t* __restrict__ wgate, const uint16_t* __restrict__ wup,
                                                 char* __restrict__ ws) {
    __shared__ __attribute__((aligned(16))) char smem_[G1_LDS];
    LAS char* lds = (LAS char*)smem_;
    const int tid = threadIdx.x, l = tid & 63;
    const int w = __builtin_amdgcn_readfirstlane(tid >> 6);
    const int grp = w >> 2;
    const int wr = w & 3, wc = grp;
    const int bid = blockIdx.x, idx = bid >> 3;
    const int ct = (bid & 7) * 4 + (idx & 3), pm = idx >> 2;
    const bool kvt = (ct & 1) != 0;
    const int j = ct >> 1;
    const int F0 = kvt ? 2048 + 96 * j : 128 * j;

    const char* aptr = ws + WS_XN + (size_t)(pm * 16 + 2 * w) * 1024;
    const unsigned avoff = l * 16;
    const char* bptr[3]; unsigned bstep[3], bvoff[3], bdst[3];
#pragma unroll
    for (int i = 0; i < 3; ++i) {
        int t = grp == 0 ? 3 * w + i : 12 + 2 * (w - 4) + (i < 2 ? i : 0);
        const int u = t >> 1, hf = t & 1, uc = u / 5, r = u % 5;
        const uint16_t* base; int col, stride;
        if (!kvt) {
            if (r == 0) { base = wqkv; col = (2 * j + uc) * 32; stride = 3 * D_; }
            else { base = (r & 1) ? wgate : wup; col = F0 + 64 * uc + (r >= 3 ? 32 : 0); stride = F_; }
        } else {
            if (r < 2) { base = wqkv; col = D_ * (1 + r) + (2 * j + uc) * 32; stride = 3 * D_; }
            else if (r < 4) { base = (r == 2) ? wgate : wup; col = F0 + 32 * uc; stride = F_; }
            else { base = uc == 0 ? wgate : wup; col = F0 + 64; stride = F_; }
        }
        bptr[i] = (const char*)(base + (size_t)(16 * hf) * stride + col);
        bstep[i] = 64u * stride;
        bvoff[i] = (l >> 2) * stride * 2 + ((((l >> 1) & 1) ^ ((l >> 5) & 1)) * 32) + (l & 1) * 16;
        bdst[i] = (unsigned)(uintptr_t)lds + 16384 + t * 1024;
    }
    const unsigned lbase = (unsigned)(uintptr_t)lds;
    const unsigned adst = lbase + 2 * w * 1024;
#define G1_STAGE(slot) do { \
        dma2(aptr, avoff, adst + (slot) * G1_SLOT); \
        dma1<0>(bptr[0], bvoff[0], bdst[0] + (slot) * G1_SLOT); dma1<0>(bptr[1], bvoff[1], bdst[1] + (slot) * G1_SLOT); \
        if (grp == 0) dma1<0>(bptr[2], bvoff[2], bdst[2] + (slot) * G1_SLOT); \
        aptr += NMB * 1024; bptr[0] += bstep[0]; bptr[1] += bstep[1]; bptr[2] += bstep[2]; \
        OPAQUE_S(aptr); OPAQUE_S(bptr[0]); OPAQUE_S(bptr[1]); OPAQUE_S(bptr[2]); } while (0)

    const int G = l >> 4, q4 = (l >> 2) & 3, p4 = l & 3;
    const unsigned lp0 = G * 512 + q4 * 64 + ((G & 1) * 32) + p4 * 8;
    const unsigned lp1 = lp0 ^ 32;
    unsigned rA = lbase + wr * 4096 + l * 16;
    unsigned rB0 = lbase + 16384 + 5 * wc * 2048 + lp0;
    unsigned rB1 = lbase + 16384 + 5 * wc * 2048 + lp1;
    unsigned rB8 = kvt ? lbase + 16384 + 4 * 2048 + (wc ? lp1 : lp0) : rB0 + 4 * 2048;
    unsigned rB9 = kvt ? lbase + 16384 + 9 * 2048 + (wc ? lp1 : lp0) : rB1 + 4 * 2048;
#define G1_MOVE(d) do { rA += (d); rB0 += (d); rB1 += (d); rB8 += (d); rB9 += (d); \
        OPAQUE(rA); OPAQUE(rB0); OPAQUE(rB1); OPAQUE(rB8); OPAQUE(rB9); } while (0)
#define LP(a) ((LAS const char*)(uintptr_t)(a))

    f32x4 acc[4][10];
#pragma unroll
    for (int m = 0; m < 4; ++m)
#pragma unroll
        for (int n = 0; n < 10; ++n) acc[m][n] = (f32x4){0.f, 0.f, 0.f, 0.f};
    bf16x8 fa[4], fb[10];

#define G1_READ(so) do { \
        _Pragma("unroll") for (int m = 0; m < 4; ++m) fa[m] = *(LAS const bf16x8*)(LP(rA) + (so) + m * 1024); \
        _Pragma("unroll") for (int u = 0; u < 4; ++u) { \
            fb[2 * u] = tr_frag(LP(rB0) + (so) + u * 2048, LP(rB0) + (so) + u * 2048 + 256); \
            fb[2 * u + 1] = tr_frag(LP(rB1) + (so) + u * 2048, LP(rB1) + (so) + u * 2048 + 256); } \
        fb[8] = tr_frag(LP(rB8) + (so), LP(rB8) + (so) + 256); \
        fb[9] = tr_frag(LP(rB9) + (so), LP(rB9) + (so) + 256); } while (0)
#define G1_MMA() do { __builtin_amdgcn_s_setprio(1); \
        _Pragma("unroll") for (int n = 0; n < 10; ++n) _Pragma("unroll") for (int m = 0; m < 4; ++m) MFMA16_ACC(acc[m][n], fb[n], fa[m]); \
        __builtin_amdgcn_s_setprio(0); } while (0)
#define G1_PHASE(so, STG, VW) do { G1_READ(so); STG; WAIT_L0(); VW; SCHED(); BAR(); SCHED(); G1_MMA(); SCHED(); BAR(); SCHED(); } while (0)

    STAMP(1, 0);
    if (bid == 0 && tid < 132) *(unsigned*)(ws + WS_CNT + tid * 256) = 0u;
    if (grp == 0) dma1<0>((const char*)wphi + (size_t)(2 * j) * 2048 + w * 1024, avoff, lbase + G1_WPHI + w * 1024);
    G1_STAGE(0); G1_STAGE(1); G1_STAGE(2);
    WAIT_V(8); BAR();
    STAMP(1, 1);
    if (grp == 1) BAR();
    G1_MOVE(0);
    SCHED();
    for (int it = 0; it < 7; ++it) {
        G1_PHASE(0, G1_STAGE(3), WAIT_V(8));
        G1_PHASE(G1_SLOT, G1_STAGE(0), WAIT_V(8));
        G1_MOVE(2 * G1_SLOT);
        G1_PHASE(0, G1_STAGE(1), WAIT_V(8));
        G1_PHASE(G1_SLOT, G1_STAGE(2), WAIT_V(8));
        G1_MOVE(-2 * G1_SLOT);
    }
    G1_PHASE(0, G1_STAGE(3), WAIT_V(8));
    G1_PHASE(G1_SLOT, (void)0, WAIT_V(4));
    G1_MOVE(2 * G1_SLOT);
    G1_PHASE(0, (void)0, WAIT_V(0));
    G1_PHASE(G1_SLOT, (void)0, (void)0);
    if (grp == 0) BAR();
    asm volatile("s_nop 7\n\ts_nop 7" ::: "memory");
    SCHED();
    STAMP(1, 2);

    const int fq = l >> 4, fr = l & 15;
    const int hh = 2 * j + wc;
    const int mb0 = pm * 16 + wr * 4;
    bf16x8 wp[2];
#pragma unroll
    for (int c = 0; c < 2; ++c) {
        LAS const char* p = lds + G1_WPHI + wc * 2048 + (4 * G + q4) * 64 + c * 32 + p4 * 8;
        wp[c] = tr_frag(p, p + 1024);
    }
    const f32x4 z4 = {0.f, 0.f, 0.f, 0.f};
    char* a2 = ws + WS_A2 + (size_t)l * 16;
    if (!kvt) {
        char* pq = ws + WS_PHIQ + ((size_t)hh * NMB + mb0) * 2048 + l * 16;
        const int kbA = (F0 + 64 * wc) >> 5;
#pragma unroll
        for (int m = 0; m < 4; ++m) {
            const bf16x8 qf = pack8(acc[m][0], acc[m][1]);
            const f32x4 p0 = MFMA16(wp[0], qf, z4), p1 = MFMA16(wp[1], qf, z4);
            bf16x8 pos, neg;
            hedgehog(p0, p1, pos, neg);
            *(bf16x8*)(pq + m * 2048) = pos;
            *(bf16x8*)(pq + m * 2048 + 1024) = neg;
            *(bf16x8*)(a2 + ((size_t)kbA * NMB + mb0 + m) * 1024) = pack8(glu4(acc[m][2], acc[m][4]), glu4(acc[m][3], acc[m][5]));
            *(bf16x8*)(a2 + ((size_t)(kbA + 1) * NMB + mb0 + m) * 1024) = pack8(glu4(acc[m][6], acc[m][8]), glu4(acc[m][7], acc[m][9]));
        }
    } else {
        u32x4 i0 = {0u, 0u, 0u, 0u}, i1 = {0u, 0u, 0u, 0u};
        {
            const int d = fr - 4 * fq;
            const unsigned lo = (d == 0) ? 0x3f80u : (d == 1) ? 0x3f800000u : 0u;
            const unsigned hi = (d == 2) ? 0x3f80u : (d == 3) ? 0x3f800000u : 0u;
            i0[0] = lo; i0[1] = hi; i1[2] = lo; i1[3] = hi;
        }
        const bf16x8 I0 = __builtin_bit_cast(bf16x8, i0), I1 = __builtin_bit_cast(bf16x8, i1);
        const u32x4 o4 = {0x3f803f80u, 0x3f803f80u, 0x3f803f80u, 0x3f803f80u};
        const bf16x8 ONES = __builtin_bit_cast(bf16x8, o4);
        f32x4 kv[2][3][2];
#pragma unroll
        for (int s = 0; s < 2; ++s)
#pragma unroll
            for (int c = 0; c < 3; ++c) { kv[s][c][0] = z4; kv[s][c][1] = z4; }
        const int kbA = (F0 + 32 * wc) >> 5, kbH = (F0 + 64) >> 5;
#pragma unroll
        for (int mp = 0; mp < 2; ++mp) {
            f32x4 T[2][2][2], TV[2][2];
#pragma unroll
            for (int mi = 0; mi < 2; ++mi) {
                const int m = 2 * mp + mi;
                const bf16x8 kf = pack8(acc[m][0], acc[m][1]);
                const f32x4 p0 = MFMA16(wp[0], kf, z4), p1 = MFMA16(wp[1], kf, z4);
                bf16x8 pos, neg;
                hedgehog(p0, p1, pos, neg);
                T[mi][0][0] = MFMA16(pos, I0, z4); T[mi][0][1] = MFMA16(pos, I1, z4);
                T[mi][1][0] = MFMA16(neg, I0, z4); T[mi][1][1] = MFMA16(neg, I1, z4);
                const bf16x8 vf = pack8(acc[m][2], acc[m][3]);
                TV[mi][0] = MFMA16(vf, I0, z4); TV[mi][1] = MFMA16(vf, I1, z4);
                *(bf16x8*)(a2 + ((size_t)kbA * NMB + mb0 + m) * 1024) = pack8(glu4(acc[m][4], acc[m][6]), glu4(acc[m][5], acc[m][7]));
                const f32x4 hh4 = glu4(acc[m][8], acc[m][9]);
                u32x2 hv = {pk2(hh4[0], hh4[1]), pk2(hh4[2], hh4[3])};
                *(u32x2*)(a2 + ((size_t)kbH * NMB + mb0 + m) * 1024 + 8 * wc) = hv;
            }
            const bf16x8 V0 = pack8(TV[0][0], TV[1][0]), V1 = pack8(TV[0][1], TV[1][1]);
#pragma unroll
            for (int s = 0; s < 2; ++s)
#pragma unroll
                for (int cc = 0; cc < 2; ++cc) {
                    const bf16x8 PK = pack8(T[0][s][cc], T[1][s][cc]);
                    kv[s][0][cc] = MFMA16(PK, V0, kv[s][0][cc]);
                    kv[s][1][cc] = MFMA16(PK, V1, kv[s][1][cc]);
                    kv[s][2][cc] = MFMA16(PK, ONES, kv[s][2][cc]);
                }
        }
        LAS char* red = lds + (wc * 4 + wr) * 12288 + l * 16;
#pragma unroll
        for (int s = 0; s < 2; ++s)
#pragma unroll
            for (int c = 0; c < 3; ++c)
#pragma unroll
                for (int cc = 0; cc < 2; ++cc) *(LAS f32x4*)(red + ((s * 3 + c) * 2 + cc) * 1024) = kv[s][c][cc];
        __syncthreads();
        char* kvp = ws + WS_KVP + ((size_t)(pm * 32 + hh) * 12 + 3 * wr) * 1024 + l * 16;
#pragma unroll
        for (int i = 0; i < 3; ++i) {
            LAS const char* p = lds + wc * 4 * 12288 + (3 * wr + i) * 1024 + l * 16;
            const f32x4 sum = (*(LAS const f32x4*)p + *(LAS const f32x4*)(p + 12288)) + (*(LAS const f32x4*)(p + 2 * 12288) + *(LAS const f32x4*)(p + 3 * 12288));
            *(f32x4*)(kvp + i * 1024) = sum;
        }
    }
    STAMP(1, 6);
    WAIT_V(0);
    STAMP(1, 7);
}

constexpr int G2_SLOT = 32768;
constexpr int G2_NS = 4;
constexpr int G2_KV = G2_NS * G2_SLOT;
constexpr int G2_LDS = G2_KV + 6144;
#define G2_NA 28
constexpr int G2_NB = 72 - G2_NA;
static_assert(G2_NA % 4 == 0 && G2_NA >= 12 && G2_NA <= 40, "");
typedef GAS unsigned gu32;
DI void wait_word(gu32* p, unsigned seen, unsigned want) {
    if (__builtin_amdgcn_readfirstlane(seen) < want) {
        unsigned spins = 0;
        while (__builtin_amdgcn_readfirstlane(__hip_atomic_load(p, __ATOMIC_RELAXED, __HIP_MEMORY_SCOPE_AGENT)) < want && ++spins < (1u << 22))
            __builtin_amdgcn_s_sleep(2);
    }
    asm volatile("buffer_inv sc1" ::: "memory");
}
__global__ __launch_bounds__(512, 2) void k_gemm2(const uint16_t* __restrict__ x, const uint16_t* __restrict__ wdown, const uint16_t* __restrict__ wo,
                                                 char* __restrict__ ws, uint16_t* __restrict__ out) {
    __shared__ __attribute__((aligned(16))) char smem_[G2_LDS];
    LAS char* lds = (LAS char*)smem_;
    const int tid = threadIdx.x, l = tid & 63;
    const int w8 = __builtin_amdgcn_readfirstlane(tid >> 6), w = w8 & 3;
    const int bid = blockIdx.x, xcd = bid & 7, idx = bid >> 3, t16 = idx >> 1;
    const bool tb = (idx & 1) != 0;
    const int pm = (xcd >> 1) * 4 + (t16 & 3), pn = (xcd & 1) * 4 + (t16 >> 2);
    const int tile = pm * 8 + pn;
    const unsigned lbase = (unsigned)(uintptr_t)lds;
    gu32* cnt = (gu32*)(ws + WS_CNT);
    char* part = ws + WS_PART + (size_t)tile * 65536;
    STAMP(3, 0);
    if (w8 >= 4) {
        const bool isb = w >= 2;
        const int k0 = tb ? G2_NA : 0;
        const char* gptr = isb ? (const char*)(wdown + (size_t)(64 * k0 + 32 * (w - 2)) * D_ + pn * 128) : ws + WS_A2 + ((size_t)(2 * k0 + w) * NMB + pm * 8) * 1024;
        const char* gptr_o = (const char*)(wo + (size_t)(32 * (w & 1)) * D_ + pn * 128);
        const unsigned gstep = isb ? 64 * D_ * 2 : 2 * NMB * 1024;
        unsigned voff[4];
#pragma unroll
        for (int i = 0; i < 4; ++i) {
            const int r = 4 * i + (l >> 4);
            voff[i] = isb ? r * (D_ * 2) + ((((l & 15) >> 1) ^ (r & 7)) * 32) + (l & 1) * 16 - 1024 * i : l * 16;
        }
        const unsigned ghalf = isb ? 16 * D_ * 2 : 4096;
        unsigned dst = lbase + w * 8192;
        int tld = k0, slot = 0;
#define G2_DMA4(g, d) asm volatile("s_mov_b32 m0, %5\n\ts_nop 0\n\tglobal_load_lds_dwordx4 %0, %4\n\tglobal_load_lds_dwordx4 %1, %4 offset:1024\n\t" \
            "global_load_lds_dwordx4 %2, %4 offset:2048\n\tglobal_load_lds_dwordx4 %3, %4 offset:3072" \
            :: "v"(voff[0]), "v"(voff[1]), "v"(voff[2]), "v"(voff[3]), "s"(g), "s"(d) : "memory")
#define G2_NEXTSLOT() do { ++slot; const int wrap = slot == G2_NS; slot = wrap ? 0 : slot; dst += wrap ? (unsigned)(-(G2_NS - 1) * G2_SLOT) : (unsigned)G2_SLOT; } while (0)
#define G2_STAGE() do { G2_DMA4(gptr, dst); G2_DMA4(gptr + ghalf, dst + 4096); \
        ++tld; gptr = (isb && tld == 56) ? gptr_o : gptr + gstep; G2_NEXTSLOT(); } while (0)
#define G2_PRUN(n) do { _Pragma("unroll 1") for (int t_ = 0; t_ < (n); ++t_) { WAIT_V(16); BAR(); G2_STAGE(); } } while (0)
#pragma unroll 1
        for (int t = 0; t < G2_NS; ++t) G2_STAGE();
        if (!tb) {
            BAR();
            WAIT_V(24); BAR();
            G2_PRUN(G2_NA - 4);
            WAIT_V(16); BAR(); WAIT_V(8); BAR(); WAIT_V(0); BAR(); BAR();
            BAR();
        } else {
            WAIT_V(24); BAR();
            G2_PRUN(G2_NB - 28);
            gu32* f0 = cnt + 64 * (pm >> 2);
            unsigned seen = 0;
            if (w == 3) asm volatile("global_load_dword %0, %1, off sc1" : "=v"(seen) : "v"(f0) : "memory");
            G2_PRUN(4);
            asm volatile("" : "+v"(seen));
            if (w == 3) wait_word(f0, seen, 32u);
            G2_PRUN(4);
            G2_PRUN(14);
            if (w != 3) {
                G2_PRUN(2);
                WAIT_V(16); BAR(); WAIT_V(8); BAR(); WAIT_V(0); BAR(); BAR();
                BAR();
            } else {
                gu32* f1 = cnt + 64 * (4 + tile);
                asm volatile("global_load_dword %0, %1, off sc1" : "=v"(seen) : "v"(f1) : "memory");
                G2_PRUN(2);
                asm volatile("s_waitcnt vmcnt(16)" : "+v"(seen) :: "memory");
                wait_word(f1, seen, 1u);
                const unsigned pv = l * 16;
                BAR();
#pragma unroll 1
                for (int i = 0; i < 8; ++i) dma4(part + i * 4096, pv, lbase + i * 4096);
                WAIT_V(41); BAR();
#pragma unroll 1
                for (int i = 8; i < 16; ++i) dma4(part + i * 4096, pv, lbase + i * 4096);
                WAIT_V(63); BAR(); BAR();
                WAIT_V(0); BAR();
            }
        }
        STAMP(3, 7);
        return;
    }
    const int wr = w >> 1, wc = w & 1;
    const int G = l >> 4, q4 = (l >> 2) & 3, p4 = l & 3;
    const f32x4 z4 = {0.f, 0.f, 0.f, 0.f};
    const size_t off0 = (size_t)(pm * 128 + wr * 64 + (l & 15)) * D_ + pn * 128 + wc * 64 + 4 * G;
    f32x4 acc[4][4];
#pragma unroll
    for (int m = 0; m < 4; ++m)
#pragma unroll
        for (int n = 0; n < 4; ++n) acc[m][n] = z4;
    if (!tb) {
        u32x2 xv[4][4];
#pragma unroll
        for (int m = 0; m < 4; ++m)
#pragma unroll
            for (int n = 0; n < 4; ++n) xv[m][n] = *(const u32x2*)(x + off0 + m * 16 * D_ + n * 16);
        const int hh = xcd * 4 + (t16 & 3), g4 = t16 >> 2, mbo = g4 * 32 + w * 8;
        const char* kvsrc = ws + WS_KVP + ((size_t)hh * 12 + 3 * w) * 1024 + l * 16;
        const char* pq = ws + WS_PHIQ + ((size_t)hh * NMB + mbo) * 2048 + l * 16;
        f32x4 tmp[24]; bf16x8 ph[16];
#pragma unroll
        for (int k = 0; k < 24; ++k) tmp[k] = *(const f32x4*)(kvsrc + (size_t)(k & 7) * 32 * 12 * 1024 + (k >> 3) * 1024);
#pragma unroll
        for (int k = 0; k < 16; ++k) ph[k] = *(const bf16x8*)(pq + k * 1024);
#pragma unroll
        for (int i = 0; i < 3; ++i) {
            const f32x4 t = ((tmp[8 * i] + tmp[8 * i + 1]) + (tmp[8 * i + 2] + tmp[8 * i + 3])) + ((tmp[8 * i + 4] + tmp[8 * i + 5]) + (tmp[8 * i + 6] + tmp[8 * i + 7]));
            const int id = 3 * w + i;
            u32x2 v = {pk2(t[0], t[1]), pk2(t[2], t[3])};
            *(LAS u32x2*)(lds + G2_KV + (id >> 1) * 1024 + l * 16 + (id & 1) * 8) = v;
        }
        WAIT_L0(); SCHED(); BAR(); SCHED();
        bf16x8 kf[2][3];
#pragma unroll
        for (int s = 0; s < 2; ++s)
#pragma unroll
            for (int c = 0; c < 3; ++c) kf[s][c] = *(LAS const bf16x8*)(lds + G2_KV + (s * 3 + c) * 1024 + l * 16);
        const __amdgpu_buffer_rsrc_t ro = __builtin_amdgcn_make_buffer_rsrc(ws + WS_A2 + ((size_t)(112 + hh) * NMB + mbo) * 1024, 0, 8192, 0x00020000);
#pragma unroll
        for (int m = 0; m < 8; ++m) {
            f32x4 n0 = MFMA16(kf[0][0], ph[2 * m], z4); n0 = MFMA16(kf[1][0], ph[2 * m + 1], n0);
            f32x4 n1 = MFMA16(kf[0][1], ph[2 * m], z4); n1 = MFMA16(kf[1][1], ph[2 * m + 1], n1);
            f32x4 dn = MFMA16(kf[0][2], ph[2 * m], z4); dn = MFMA16(kf[1][2], ph[2 * m + 1], dn);
            const float den = bfr(bfr(dn[0]) + 1e-6f);
            const float rd = 1.0f / den;
#pragma unroll
            for (int i = 0; i < 4; ++i) { n0[i] = bfr(n0[i]) * rd; n1[i] = bfr(n1[i]) * rd; }
            __builtin_amdgcn_raw_buffer_store_b128(__builtin_bit_cast(u32x4, pack8(n0, n1)), ro, m * 1024 + l * 16, 0, 16);
        }
#pragma unroll
        for (int m = 0; m < 4; ++m)
#pragma unroll
            for (int n = 0; n < 4; ++n) acc[m][n] = (f32x4){bflo(xv[m][n][0]), bfhi(xv[m][n][0]), bflo(xv[m][n][1]), bfhi(xv[m][n][1])};
        SCHED();
    }
    const int r8 = 4 * (G & 1) + q4;
    unsigned rA = lbase + wr * 4096 + l * 16;
    unsigned rB[4];
#pragma unroll
    for (int n = 0; n < 4; ++n) rB[n] = lbase + 16384 + (4 * G + q4) * 256 + (((4 * wc + n) ^ r8) * 32) + p4 * 8;
#define G2_MOVE(d) do { rA += (d); rB[0] += (d); rB[1] += (d); rB[2] += (d); rB[3] += (d); OPAQUE(rA); OPAQUE(rB[0]); OPAQUE(rB[1]); OPAQUE(rB[2]); OPAQUE(rB[3]); } while (0)
    bf16x8 fa[2][2][4], fb[2][2][4];
#define G2_RD_A(buf, so, kk, m) fa[buf][kk][m] = *(LAS const bf16x8*)(LP(rA) + (so) + (kk) * 8192 + (m) * 1024)
#define G2_RD_B(buf, so, kk, n) fb[buf][kk][n] = tr_frag(LP(rB[n]) + (so) + (kk) * 8192, LP(rB[n]) + (so) + (kk) * 8192 + 4096)
#define G2_READ(buf, so) do { _Pragma("unroll") for (int kk = 0; kk < 2; ++kk) _Pragma("unroll") for (int i = 0; i < 4; ++i) { G2_RD_B(buf, so, kk, i); G2_RD_A(buf, so, kk, i); } } while (0)
#define G2_STEP(cur, so, RD) do { BAR(); SCHED(); \
        _Pragma("unroll") for (int kk = 0; kk < 2; ++kk) _Pragma("unroll") for (int m = 0; m < 4; ++m) _Pragma("unroll") for (int n = 0; n < 4; ++n) { \
            MFMA16_ACC(acc[m][n], fb[cur][kk][n], fa[cur][kk][m]); \
            if ((RD) && kk == 0) { const int i_ = m * 4 + n; if (i_ & 1) G2_RD_A((cur) ^ 1, so, i_ >> 3, (i_ >> 1) & 3); else G2_RD_B((cur) ^ 1, so, i_ >> 3, (i_ >> 1) & 3); } \
            SCHED(); } \
        WAIT_L0(); SCHED(); } while (0)
    unsigned mv = 2 * G2_SLOT;
#define G2_RUN(n) do { _Pragma("unroll 1") for (int i_ = 0; i_ < (n); ++i_) { \
        G2_STEP(0, G2_SLOT, 1); G2_MOVE(mv); mv = -mv; G2_STEP(1, 0, 1); } } while (0)

    G2_MOVE(0);
    BAR(); SCHED();
    G2_READ(0, 0);
    WAIT_L0(); SCHED();
    STAMP(3, 1);
    G2_RUN(2);
    WAIT_V(0); SCHED();
    G2_RUN(1);
    if (!tb && tid == 0) __hip_atomic_fetch_add(cnt + 64 * (t16 >> 2), 1u, __ATOMIC_RELAXED, __HIP_MEMORY_SCOPE_AGENT);
    SCHED();
    const int nrest = (tb ? G2_NB : G2_NA) / 2 - 4;
    G2_RUN(nrest);
    G2_STEP(0, G2_SLOT, 1);
    G2_STEP(1, 0, 0);
    asm volatile("s_nop 7\n\ts_nop 7" ::: "memory");
    SCHED();
    if (!tb) {
        STAMP(3, 2);
        const __amdgpu_buffer_rsrc_t rp = __builtin_amdgcn_make_buffer_rsrc(part + w * 16384, 0, 16384, 0x00020000);
#pragma unroll
        for (int m = 0; m < 4; ++m)
#pragma unroll
            for (int n = 0; n < 4; ++n) __builtin_amdgcn_raw_buffer_store_b128(__builtin_bit_cast(u32x4, acc[m][n]), rp, (m * 4 + n) * 1024 + l * 16, 0, 16);
        WAIT_V(0); BAR();
        if (tid == 0) __hip_atomic_store(cnt + 64 * (4 + tile), 1u, __ATOMIC_RELAXED, __HIP_MEMORY_SCOPE_AGENT);
        STAMP(3, 6);
        return;
    }
    BAR(); SCHED();
    STAMP(3, 3);
#pragma unroll
    for (int m = 0; m < 4; ++m)
#pragma unroll
        for (int n = 0; n < 4; ++n) {
            const f32x4 r = acc[m][n] + *(LAS const f32x4*)(lds + w * 16384 + (m * 4 + n) * 1024 + l * 16);
            u32x2 o = {pk2(r[0], r[1]), pk2(r[2], r[3])};
            *(u32x2*)(out + off0 + m * 16 * D_ + n * 16) = o;
        }
    STAMP(3, 5);
}

extern "C" void kernel_launch(void* const* d_in, const int* in_sizes, int n_in, void* d_out, int out_size, void* d_ws, size_t ws_size,
                              hipStream_t stream) {
    const uint16_t* x = (const uint16_t*)d_in[0];
    const uint16_t* wqkv = (const uint16_t*)d_in[1];
    const uint16_t* wphi = (const uint16_t*)d_in[2];
    const uint16_t* wo = (const uint16_t*)d_in[3];
    const uint16_t* wgate = (const uint16_t*)d_in[5];
    const uint16_t* wup = (const uint16_t*)d_in[6];
    const uint16_t* wdown = (const uint16_t*)d_in[7];
    char* ws = (char*)d_ws;
    k_ln<<<256, 256, 0, stream>>>(x, ws);
    k_gemm1<<<256, 512, 0, stream>>>(wqkv, wphi, wgate, wup, ws);
    k_gemm2<<<256, 512, 0, stream>>>(x, wdown, wo, ws, (uint16_t*)d_out);
}
```

```cpp
#include <hip/hip_runtime.h>
#include <hip/hip_bf16.h>
#include <stdint.h>

#define LAS __attribute__((address_space(3)))
#define GAS __attribute__((address_space(1)))
#define DI __device__ __forceinline__
typedef __bf16 bf16x8 __attribute__((ext_vector_type(8)));
typedef __bf16 bf16x2 __attribute__((ext_vector_type(2)));
typedef short s16x4 __attribute__((ext_vector_type(4)));
typedef short s16x8 __attribute__((ext_vector_type(8)));
typedef float f32x4 __attribute__((ext_vector_type(4)));
typedef float f32x2 __attribute__((ext_vector_type(2)));
typedef unsigned u32x4 __attribute__((ext_vector_type(4)));
typedef unsigned u32x2 __attribute__((ext_vector_type(2)));

constexpr int S_ = 2048, D_ = 1024, F_ = 3584, H_ = 32;
constexpr int NMB = S_ / 16;
constexpr size_t WS_XN = 0;
constexpr size_t WS_A2 = WS_XN + (size_t)32 * NMB * 1024;
constexpr size_t WS_PHIQ = WS_A2 + (size_t)144 * NMB * 1024;
constexpr size_t WS_KVP = WS_PHIQ + (size_t)32 * NMB * 2048;
constexpr size_t WS_CNT = WS_KVP + (size_t)8 * 32 * 12 * 1024;
constexpr size_t WS_END = WS_CNT + 2048;

#define STAMP(kid, i) do {} while (0)
#define MFMA16(a, b, c) __builtin_amdgcn_mfma_f32_16x16x32_bf16((a), (b), (c), 0, 0, 0)
#define WAIT_V(n) asm volatile("s_waitcnt vmcnt(" #n ")" ::: "memory")
#define WAIT_L(n) asm volatile("s_waitcnt lgkmcnt(" #n ")" ::: "memory")
#define WAIT_L0() __builtin_amdgcn_s_waitcnt(0xC07F)
#define MFMA16_ACC(acc, a, b) asm volatile("v_mfma_f32_16x16x32_bf16 %0, %1, %2, %0" : "+v"(acc) : "v"(a), "v"(b))
#define BAR() __builtin_amdgcn_s_barrier()
#define SCHED() __builtin_amdgcn_sched_barrier(0)

DI unsigned pk2(float lo, float hi) {
    f32x2 v = {lo, hi};
    return __builtin_bit_cast(unsigned, __builtin_convertvector(v, bf16x2));
}
DI float bflo(unsigned u) { return __uint_as_float(u << 16); }
DI float bfhi(unsigned u) { return __uint_as_float(u & 0xffff0000u); }
DI float bfr(float x) { return bflo(pk2(x, x)); }
DI bf16x8 pack8(const f32x4& a, const f32x4& b) {
    u32x4 r = {pk2(a[0], a[1]), pk2(a[2], a[3]), pk2(b[0], b[1]), pk2(b[2], b[3])};
    return __builtin_bit_cast(bf16x8, r);
}
DI bf16x8 tr_frag(LAS const char* p0, LAS const char* p1) {
    s16x4 a = __builtin_amdgcn_ds_read_tr16_b64_v4i16((LAS s16x4*)p0);
    s16x4 b = __builtin_amdgcn_ds_read_tr16_b64_v4i16((LAS s16x4*)p1);
    s16x8 c = __builtin_shufflevector(a, b, 0, 1, 2, 3, 4, 5, 6, 7);
    return __builtin_bit_cast(bf16x8, c);
}
template <int OFF> DI void dma1(const char* g, unsigned voff, unsigned ldsdst) {
    asm volatile("s_mov_b32 m0, %2\n\ts_nop 0\n\tglobal_load_lds_dwordx4 %0, %1 offset:%3" :: "v"(voff), "s"(g), "s"(ldsdst), "i"(OFF) : "memory");
}
DI void dma2(const char* g, unsigned voff, unsigned ldsdst) {
    asm volatile("s_mov_b32 m0, %2\n\ts_nop 0\n\tglobal_load_lds_dwordx4 %0, %1\n\tglobal_load_lds_dwordx4 %0, %1 offset:1024"
                 :: "v"(voff), "s"(g), "s"(ldsdst) : "memory");
}
DI void dma4(const char* g, unsigned voff, unsigned ldsdst) {
    asm volatile("s_mov_b32 m0, %2\n\ts_nop 0\n\tglobal_load_lds_dwordx4 %0, %1\n\tglobal_load_lds_dwordx4 %0, %1 offset:1024\n\t"
                 "global_load_lds_dwordx4 %0, %1 offset:2048\n\tglobal_load_lds_dwordx4 %0, %1 offset:3072"
                 :: "v"(voff), "s"(g), "s"(ldsdst) : "memory");
}
#define OPAQUE(x) asm volatile("" : "+v"(x))
#define OPAQUE_S(x) asm volatile("" : "+s"(x))
DI float xmax(float v) {
    auto a = __builtin_amdgcn_permlane16_swap(__float_as_uint(v), __float_as_uint(v), false, false);
    v = fmaxf(__uint_as_float(a[0]), __uint_as_float(a[1]));
    auto b = __builtin_amdgcn_permlane32_swap(__float_as_uint(v), __float_as_uint(v), false, false);
    return fmaxf(__uint_as_float(b[0]), __uint_as_float(b[1]));
}
DI float xsum(float v) {
    auto a = __builtin_amdgcn_permlane16_swap(__float_as_uint(v), __float_as_uint(v), false, false);
    v = __uint_as_float(a[0]) + __uint_as_float(a[1]);
    auto b = __builtin_amdgcn_permlane32_swap(__float_as_uint(v), __float_as_uint(v), false, false);
    return __uint_as_float(b[0]) + __uint_as_float(b[1]);
}

__global__ __launch_bounds__(256) void k_ln(const uint16_t* __restrict__ x, char* __restrict__ ws) {
    __shared__ float red[2][4][8];
    const int tid = threadIdx.x, w = tid >> 6, l = tid & 63, c = l & 7, r8 = l >> 3;
    const int row = blockIdx.x * 8 + r8;
    STAMP(0, 0);
    const char* xr = (const char*)x + (size_t)row * 2048 + w * 512 + c * 16;
    u32x4 v[4];
#pragma unroll
    for (int i = 0; i < 4; ++i) v[i] = *(const u32x4*)(xr + i * 128);
    float f[32];
#pragma unroll
    for (int i = 0; i < 4; ++i)
#pragma unroll
        for (int j = 0; j < 4; ++j) { f[i * 8 + 2 * j] = bflo(v[i][j]); f[i * 8 + 2 * j + 1] = bfhi(v[i][j]); }
    float s = 0.f;
#pragma unroll
    for (int i = 0; i < 32; ++i) s += f[i];
    s += __shfl_xor(s, 1); s += __shfl_xor(s, 2); s += __shfl_xor(s, 4);
    if (c == 0) red[0][w][r8] = s;
    __syncthreads();
    const float mean = (red[0][0][r8] + red[0][1][r8] + red[0][2][r8] + red[0][3][r8]) * (1.0f / 1024.0f);
    float q = 0.f;
#pragma unroll
    for (int i = 0; i < 32; ++i) { const float d = f[i] - mean; q += d * d; }
    q += __shfl_xor(q, 1); q += __shfl_xor(q, 2); q += __shfl_xor(q, 4);
    if (c == 0) red[1][w][r8] = q;
    __syncthreads();
    const float var = (red[1][0][r8] + red[1][1][r8] + red[1][2][r8] + red[1][3][r8]) * (1.0f / 1024.0f);
    const float mb = bfr(mean);
    const float sd = bfr(sqrtf(bfr(bfr(var) + 1e-5f)));
    const float rs = 1.0f / sd;
    const int mbk = row >> 4, fr = row & 15;
#pragma unroll
    for (int i = 0; i < 4; ++i) {
        u32x4 o;
#pragma unroll
        for (int j = 0; j < 4; ++j) {
            const unsigned d2 = pk2(f[i * 8 + 2 * j] - mb, f[i * 8 + 2 * j + 1] - mb);
            o[j] = pk2(bflo(d2) * rs, bfhi(d2) * rs);
        }
        const int kb = (w * 4 + i) * 2 + (c >> 2), fq = c & 3;
        *(u32x4*)(ws + WS_XN + ((size_t)(kb * NMB + mbk)) * 1024 + (fq * 16 + fr) * 16) = o;
    }
    STAMP(0, 7);
}

constexpr int G1_SLOT = 36864;
constexpr int G1_WPHI = 4 * G1_SLOT;
constexpr int G1_LDS = G1_WPHI + 4096;

DI void hedgehog(const f32x4& p0, const f32x4& p1, bf16x8& pos, bf16x8& neg) {
    float x[8];
#pragma unroll
    for (int i = 0; i < 2; ++i) {
        const unsigned a = pk2(p0[2 * i], p0[2 * i + 1]), b = pk2(p1[2 * i], p1[2 * i + 1]);
        x[2 * i] = bflo(a); x[2 * i + 1] = bfhi(a); x[4 + 2 * i] = bflo(b); x[4 + 2 * i + 1] = bfhi(b);
    }
    float mx = x[0], mn = x[0];
#pragma unroll
    for (int i = 1; i < 8; ++i) { mx = fmaxf(mx, x[i]); mn = fminf(mn, x[i]); }
    mx = xmax(mx); mn = -xmax(-mn);
    const float L2E = 1.44269504088896340736f;
    float ep[8], en[8], sp = 0.f, sn = 0.f;
#pragma unroll
    for (int i = 0; i < 8; ++i) {
        ep[i] = __builtin_amdgcn_exp2f((x[i] - mx) * L2E); sp += ep[i];
        en[i] = __builtin_amdgcn_exp2f((mn - x[i]) * L2E); sn += en[i];
    }
    sp = xsum(sp); sn = xsum(sn);
    const float rp = __builtin_amdgcn_rcpf(sp), rn = __builtin_amdgcn_rcpf(sn);
    u32x4 a, b;
#pragma unroll
    for (int i = 0; i < 4; ++i) { a[i] = pk2(ep[2 * i] * rp, ep[2 * i + 1] * rp); b[i] = pk2(en[2 * i] * rn, en[2 * i + 1] * rn); }
    pos = __builtin_bit_cast(bf16x8, a); neg = __builtin_bit_cast(bf16x8, b);
}
DI f32x4 glu4(const f32x4& g, const f32x4& u) {
    f32x4 h;
#pragma unroll
    for (int i = 0; i < 4; ++i) {
        const float e = __builtin_amdgcn_exp2f(g[i] * -1.44269504088896340736f);
        h[i] = u[i] * __builtin_amdgcn_rcpf(1.0f + e);
    }
    return h;
}

__global__ __launch_bounds__(512, 2) void k_gemm1(const uint16_t* __restrict__ wqkv, const uint16_t* __restrict__ wphi,
                                                 const uint16_t* __restrict__ wgate, const uint16_t* __restrict__ wup,
                                                 char* __restrict__ ws) {
    __shared__ __attribute__((aligned(16))) char smem_[G1_LDS];
    LAS char* lds = (LAS char*)smem_;
    const int tid = threadIdx.x, l = tid & 63;
    const int w = __builtin_amdgcn_readfirstlane(tid >> 6);
    const int grp = w >> 2;
    const int wr = w & 3, wc = grp;
    const int bid = blockIdx.x, idx = bid >> 3;
    const int ct = (bid & 7) * 4 + (idx & 3), pm = idx >> 2;
    const bool kvt = (ct & 1) != 0;
    const int j = ct >> 1;
    const int F0 = kvt ? 2048 + 96 * j : 128 * j;

    const char* aptr = ws + WS_XN + (size_t)(pm * 16 + 2 * w) * 1024;
    const unsigned avoff = l * 16;
    const char* bptr[3]; unsigned bstep[3], bvoff[3], bdst[3];
#pragma unroll
    for (int i = 0; i < 3; ++i) {
        int t = grp == 0 ? 3 * w + i : 12 + 2 * (w - 4) + (i < 2 ? i : 0);
        const int u = t >> 1, hf = t & 1, uc = u / 5, r = u % 5;
        const uint16_t* base; int col, stride;
        if (!kvt) {
            if (r == 0) { base = wqkv; col = (2 * j + uc) * 32; stride = 3 * D_; }
            else { base = (r & 1) ? wgate : wup; col = F0 + 64 * uc + (r >= 3 ? 32 : 0); stride = F_; }
        } else {
            if (r < 2) { base = wqkv; col = D_ * (1 + r) + (2 * j + uc) * 32; stride = 3 * D_; }
            else if (r < 4) { base = (r == 2) ? wgate : wup; col = F0 + 32 * uc; stride = F_; }
            else { base = uc == 0 ? wgate : wup; col = F0 + 64; stride = F_; }
        }
        bptr[i] = (const char*)(base + (size_t)(16 * hf) * stride + col);
        bstep[i] = 64u * stride;
        bvoff[i] = (l >> 2) * stride * 2 + ((((l >> 1) & 1) ^ ((l >> 5) & 1)) * 32) + (l & 1) * 16;
        bdst[i] = (unsigned)(uintptr_t)lds + 16384 + t * 1024;
    }
    const unsigned lbase = (unsigned)(uintptr_t)lds;
    const unsigned adst = lbase + 2 * w * 1024;
#define G1_STAGE(slot) do { \
        dma2(aptr, avoff, adst + (slot) * G1_SLOT); \
        dma1<0>(bptr[0], bvoff[0], bdst[0] + (slot) * G1_SLOT); dma1<0>(bptr[1], bvoff[1], bdst[1] + (slot) * G1_SLOT); \
        if (grp == 0) dma1<0>(bptr[2], bvoff[2], bdst[2] + (slot) * G1_SLOT); \
        aptr += NMB * 1024; bptr[0] += bstep[0]; bptr[1] += bstep[1]; bptr[2] += bstep[2]; \
        OPAQUE_S(aptr); OPAQUE_S(bptr[0]); OPAQUE_S(bptr[1]); OPAQUE_S(bptr[2]); } while (0)

    const int G = l >> 4, q4 = (l >> 2) & 3, p4 = l & 3;
    const unsigned lp0 = G * 512 + q4 * 64 + ((G & 1) * 32) + p4 * 8;
    const unsigned lp1 = lp0 ^ 32;
    unsigned rA = lbase + wr * 4096 + l * 16;
    unsigned rB0 = lbase + 16384 + 5 * wc * 2048 + lp0;
    unsigned rB1 = lbase + 16384 + 5 * wc * 2048 + lp1;
    unsigned rB8 = kvt ? lbase + 16384 + 4 * 2048 + (wc ? lp1 : lp0) : rB0 + 4 * 2048;
    unsigned rB9 = kvt ? lbase + 16384 + 9 * 2048 + (wc ? lp1 : lp0) : rB1 + 4 * 2048;
#define G1_MOVE(d) do { rA += (d); rB0 += (d); rB1 += (d); rB8 += (d); rB9 += (d); \
        OPAQUE(rA); OPAQUE(rB0); OPAQUE(rB1); OPAQUE(rB8); OPAQUE(rB9); } while (0)
#define LP(a) ((LAS const char*)(uintptr_t)(a))

    f32x4 acc[4][10];
#pragma unroll
    for (int m = 0; m < 4; ++m)
#pragma unroll
        for (int n = 0; n < 10; ++n) acc[m][n] = (f32x4){0.f, 0.f, 0.f, 0.f};
    bf16x8 fa[4], fb[10];

#define G1_READ(so) do { \
        _Pragma("unroll") for (int m = 0; m < 4; ++m) fa[m] = *(LAS const bf16x8*)(LP(rA) + (so) + m * 1024); \
        _Pragma("unroll") for (int u = 0; u < 4; ++u) { \
            fb[2 * u] = tr_frag(LP(rB0) + (so) + u * 2048, LP(rB0) + (so) + u * 2048 + 256); \
            fb[2 * u + 1] = tr_frag(LP(rB1) + (so) + u * 2048, LP(rB1) + (so) + u * 2048 + 256); } \
        fb[8] = tr_frag(LP(rB8) + (so), LP(rB8) + (so) + 256); \
        fb[9] = tr_frag(LP(rB9) + (so), LP(rB9) + (so) + 256); } while (0)
#define G1_MMA() do { __builtin_amdgcn_s_setprio(1); \
        _Pragma("unroll") for (int n = 0; n < 10; ++n) _Pragma("unroll") for (int m = 0; m < 4; ++m) MFMA16_ACC(acc[m][n], fb[n], fa[m]); \
        __builtin_amdgcn_s_setprio(0); } while (0)
#define G1_PHASE(so, STG, VW) do { G1_READ(so); STG; WAIT_L0(); VW; SCHED(); BAR(); SCHED(); G1_MMA(); SCHED(); BAR(); SCHED(); } while (0)

    STAMP(1, 0);
    if (bid == 0 && tid < 8) *(unsigned*)(ws + WS_CNT + tid * 256) = 0u;
    if (grp == 0) dma1<0>((const char*)wphi + (size_t)(2 * j) * 2048 + w * 1024, avoff, lbase + G1_WPHI + w * 1024);
    G1_STAGE(0); G1_STAGE(1); G1_STAGE(2);
    WAIT_V(8); BAR();
    STAMP(1, 1);
    if (grp == 1) BAR();
    G1_MOVE(0);
    SCHED();
    for (int it = 0; it < 7; ++it) {
        G1_PHASE(0, G1_STAGE(3), WAIT_V(8));
        G1_PHASE(G1_SLOT, G1_STAGE(0), WAIT_V(8));
        G1_MOVE(2 * G1_SLOT);
        G1_PHASE(0, G1_STAGE(1), WAIT_V(8));
        G1_PHASE(G1_SLOT, G1_STAGE(2), WAIT_V(8));
        G1_MOVE(-2 * G1_SLOT);
    }
    G1_PHASE(0, G1_STAGE(3), WAIT_V(8));
    G1_PHASE(G1_SLOT, (void)0, WAIT_V(4));
    G1_MOVE(2 * G1_SLOT);
    G1_PHASE(0, (void)0, WAIT_V(0));
    G1_PHASE(G1_SLOT, (void)0, (void)0);
    if (grp == 0) BAR();
    asm volatile("s_nop 7\n\ts_nop 7" ::: "memory");
    SCHED();
    STAMP(1, 2);

    const int fq = l >> 4, fr = l & 15;
    const int hh = 2 * j + wc;
    const int mb0 = pm * 16 + wr * 4;
    bf16x8 wp[2];
#pragma unroll
    for (int c = 0; c < 2; ++c) {
        LAS const char* p = lds + G1_WPHI + wc * 2048 + (4 * G + q4) * 64 + c * 32 + p4 * 8;
        wp[c] = tr_frag(p, p + 1024);
    }
    const f32x4 z4 = {0.f, 0.f, 0.f, 0.f};
    char* a2 = ws + WS_A2 + (size_t)l * 16;
    if (!kvt) {
        char* pq = ws + WS_PHIQ + ((size_t)hh * NMB + mb0) * 2048 + l * 16;
        const int kbA = (F0 + 64 * wc) >> 5;
#pragma unroll
        for (int m = 0; m < 4; ++m) {
            const bf16x8 qf = pack8(acc[m][0], acc[m][1]);
            const f32x4 p0 = MFMA16(wp[0], qf, z4), p1 = MFMA16(wp[1], qf, z4);
            bf16x8 pos, neg;
            hedgehog(p0, p1, pos, neg);
            *(bf16x8*)(pq + m * 2048) = pos;
            *(bf16x8*)(pq + m * 2048 + 1024) = neg;
            *(bf16x8*)(a2 + ((size_t)kbA * NMB + mb0 + m) * 1024) = pack8(glu4(acc[m][2], acc[m][4]), glu4(acc[m][3], acc[m][5]));
            *(bf16x8*)(a2 + ((size_t)(kbA + 1) * NMB + mb0 + m) * 1024) = pack8(glu4(acc[m][6], acc[m][8]), glu4(acc[m][7], acc[m][9]));
        }
    } else {
        u32x4 i0 = {0u, 0u, 0u, 0u}, i1 = {0u, 0u, 0u, 0u};
        {
            const int d = fr - 4 * fq;
            const unsigned lo = (d == 0) ? 0x3f80u : (d == 1) ? 0x3f800000u : 0u;
            const unsigned hi = (d == 2) ? 0x3f80u : (d == 3) ? 0x3f800000u : 0u;
            i0[0] = lo; i0[1] = hi; i1[2] = lo; i1[3] = hi;
        }
        const bf16x8 I0 = __builtin_bit_cast(bf16x8, i0), I1 = __builtin_bit_cast(bf16x8, i1);
        const u32x4 o4 = {0x3f803f80u, 0x3f803f80u, 0x3f803f80u, 0x3f803f80u};
        const bf16x8 ONES = __builtin_bit_cast(bf16x8, o4);
        f32x4 kv[2][3][2];
#pragma unroll
        for (int s = 0; s < 2; ++s)
#pragma unroll
            for (int c = 0; c < 3; ++c) { kv[s][c][0] = z4; kv[s][c][1] = z4; }
        const int kbA = (F0 + 32 * wc) >> 5, kbH = (F0 + 64) >> 5;
#pragma unroll
        for (int mp = 0; mp < 2; ++mp) {
            f32x4 T[2][2][2], TV[2][2];
#pragma unroll
            for (int mi = 0; mi < 2; ++mi) {
                const int m = 2 * mp + mi;
                const bf16x8 kf = pack8(acc[m][0], acc[m][1]);
                const f32x4 p0 = MFMA16(wp[0], kf, z4), p1 = MFMA16(wp[1], kf, z4);
                bf16x8 pos, neg;
                hedgehog(p0, p1, pos, neg);
                T[mi][0][0] = MFMA16(pos, I0, z4); T[mi][0][1] = MFMA16(pos, I1, z4);
                T[mi][1][0] = MFMA16(neg, I0, z4); T[mi][1][1] = MFMA16(neg, I1, z4);
                const bf16x8 vf = pack8(acc[m][2], acc[m][3]);
                TV[mi][0] = MFMA16(vf, I0, z4); TV[mi][1] = MFMA16(vf, I1, z4);
                *(bf16x8*)(a2 + ((size_t)kbA * NMB + mb0 + m) * 1024) = pack8(glu4(acc[m][4], acc[m][6]), glu4(acc[m][5], acc[m][7]));
                const f32x4 hh4 = glu4(acc[m][8], acc[m][9]);
                u32x2 hv = {pk2(hh4[0], hh4[1]), pk2(hh4[2], hh4[3])};
                *(u32x2*)(a2 + ((size_t)kbH * NMB + mb0 + m) * 1024 + 8 * wc) = hv;
            }
            const bf16x8 V0 = pack8(TV[0][0], TV[1][0]), V1 = pack8(TV[0][1], TV[1][1]);
#pragma unroll
            for (int s = 0; s < 2; ++s)
#pragma unroll
                for (int cc = 0; cc < 2; ++cc) {
                    const bf16x8 PK = pack8(T[0][s][cc], T[1][s][cc]);
                    kv[s][0][cc] = MFMA16(PK, V0, kv[s][0][cc]);
                    kv[s][1][cc] = MFMA16(PK, V1, kv[s][1][cc]);
                    kv[s][2][cc] = MFMA16(PK, ONES, kv[s][2][cc]);
                }
        }
        LAS char* red = lds + (wc * 4 + wr) * 12288 + l * 16;
#pragma unroll
        for (int s = 0; s < 2; ++s)
#pragma unroll
            for (int c = 0; c < 3; ++c)
#pragma unroll
                for (int cc = 0; cc < 2; ++cc) *(LAS f32x4*)(red + ((s * 3 + c) * 2 + cc) * 1024) = kv[s][c][cc];
        __syncthreads();
        char* kvp = ws + WS_KVP + ((size_t)(pm * 32 + hh) * 12 + 3 * wr) * 1024 + l * 16;
#pragma unroll
        for (int i = 0; i < 3; ++i) {
            LAS const char* p = lds + wc * 4 * 12288 + (3 * wr + i) * 1024 + l * 16;
            const f32x4 sum = (*(LAS const f32x4*)p + *(LAS const f32x4*)(p + 12288)) + (*(LAS const f32x4*)(p + 2 * 12288) + *(LAS const f32x4*)(p + 3 * 12288));
            *(f32x4*)(kvp + i * 1024) = sum;
        }
    }
    STAMP(1, 6);
    WAIT_V(0);
    STAMP(1, 7);
}

constexpr int G2_SLOT = 24576;
constexpr int G2_NS = 6;
constexpr int G2_KV = G2_NS * G2_SLOT;
constexpr int G2_PF = G2_KV + 6144;
constexpr int G2_LDS = G2_PF + 256;
#define G2_EXP 0
#define G2_PFD 8
typedef GAS unsigned gu32;
__global__ __launch_bounds__(512, 2) void k_gemm2(const uint16_t* __restrict__ x, const uint16_t* __restrict__ wdown, const uint16_t* __restrict__ wo,
                                                 char* __restrict__ ws, uint16_t* __restrict__ out) {
    __shared__ __attribute__((aligned(16))) char smem_[G2_LDS];
    LAS char* lds = (LAS char*)smem_;
    const int tid = threadIdx.x, l = tid & 63;
    const int w8 = __builtin_amdgcn_readfirstlane(tid >> 6), w = w8 & 3;
    const int bid = blockIdx.x, xcd = bid & 7, idx = bid >> 3;
    const int pm = (xcd >> 1) * 4 + (idx & 3), pn = (xcd & 1) * 8 + (idx >> 2);
    const unsigned lbase = (unsigned)(uintptr_t)lds;
    STAMP(3, 0);
    gu32* cnt = (gu32*)(ws + WS_CNT);
    if (w8 == 7) {
        const bool pfa = idx < 16;
        const char* pfp = pfa ? ws + WS_A2 + ((size_t)(idx >> 2) * NMB + pm * 8) * 1024 : (const char*)(wdown + (size_t)(8 * (idx - 16)) * D_ + (xcd & 1) * 512);
        const char* pfo = (const char*)(wo + (size_t)(8 * (idx & 15)) * D_ + (xcd & 1) * 512);
        const unsigned pfs = pfa ? 4 * NMB * 1024 : 128 * D_ * 2;
        const unsigned pfv = pfa ? l * 128 : (l >> 3) * (D_ * 2) + (l & 7) * 128;
        int pfn = 4;
        pfp += 4 * pfs;
        const int pfend = pfa ? 28 : 36;
#define G2_PREFETCH() do { asm volatile("s_mov_b32 m0, %2\n\ts_nop 0\n\tglobal_load_lds_dword %0, %1" :: "v"(pfv), "s"(pfp), "s"(lbase + G2_PF) : "memory"); \
        ++pfn; pfp = (G2_EXP & 8) || pfn >= pfend ? pfp : (!pfa && pfn == 28) ? pfo : pfp + pfs; } while (0)
#define G2_PFRUN(n) do { _Pragma("unroll 1") for (int i_ = 0; i_ < (n); ++i_) { G2_PREFETCH(); BAR(); BAR(); } } while (0)
        BAR();
#pragma unroll 1
        for (int i = 0; i < G2_PFD - 4; ++i) { G2_PREFETCH(); BAR(); G2_PREFETCH(); BAR(); }
        G2_PFRUN(20 - (G2_PFD - 4));
        gu32* flag = cnt + 64 * (pm >> 1);
        unsigned seen;
        asm volatile("global_load_dword %0, %1, off sc1" : "=v"(seen) : "v"(flag) : "memory");
        G2_PFRUN(2);
        asm volatile("s_waitcnt vmcnt(2)" : "+v"(seen) :: "memory");
        if (__builtin_amdgcn_readfirstlane(seen) < 32u) {
            unsigned spins = 0;
            while (__builtin_amdgcn_readfirstlane(__hip_atomic_load(flag, __ATOMIC_RELAXED, __HIP_MEMORY_SCOPE_AGENT)) < 32u && ++spins < (1u << 22))
                __builtin_amdgcn_s_sleep(2);
        }
        if (!(G2_EXP & 16)) asm volatile("buffer_inv sc1" ::: "memory");
        G2_PFRUN(3);
        WAIT_V(3);
        G2_PFRUN(11 - G2_PFD);
#pragma unroll 1
        for (int i = 0; i < G2_PFD; ++i) { BAR(); BAR(); }
        WAIT_V(0);
        STAMP(3, 7);
        return;
    }
    if (w8 >= 4) {
        const bool isb = w == 2;
        const char* gptr = isb ? (const char*)(wdown + pn * 64) : ws + WS_A2 + ((size_t)w * NMB + pm * 8) * 1024;
        const char* gptr_o = (const char*)(wo + pn * 64);
        const unsigned gstep = isb ? 64 * D_ * 2 : 2 * NMB * 1024;
        unsigned voff[4];
#pragma unroll
        for (int i = 0; i < 4; ++i)
            voff[i] = isb ? ((l >> 3) + 8 * i) * (D_ * 2) + ((((l & 7) >> 1) ^ ((l >> 4) & 3)) * 32) + (l & 1) * 16 - 1024 * i : l * 16;
        const unsigned ghalf = isb ? 32 * D_ * 2 : 4096;
        unsigned dst = lbase + w * 8192;
        int tld = 0, slot = 0;
#define G2_DMA4(g, d) asm volatile("s_mov_b32 m0, %5\n\ts_nop 0\n\tglobal_load_lds_dwordx4 %0, %4\n\tglobal_load_lds_dwordx4 %1, %4 offset:1024\n\t" \
            "global_load_lds_dwordx4 %2, %4 offset:2048\n\tglobal_load_lds_dwordx4 %3, %4 offset:3072" \
            :: "v"(voff[0]), "v"(voff[1]), "v"(voff[2]), "v"(voff[3]), "s"(g), "s"(d) : "memory")
#define G2_STAGE() do { \
        if (!((G2_EXP & 1) && isb) && !((G2_EXP & 2) && !isb)) { G2_DMA4(gptr, dst); G2_DMA4(gptr + ghalf, dst + 4096); } \
        ++tld; gptr = (isb && tld == 56) ? gptr_o : gptr + gstep; \
        ++slot; const int wrap = slot == G2_NS; slot = wrap ? 0 : slot; dst += wrap ? (unsigned)(-(G2_NS - 1) * G2_SLOT) : (unsigned)G2_SLOT; } while (0)
#pragma unroll 1
        for (int t = 0; t < 6; ++t) G2_STAGE();
        WAIT_V(40); BAR();
#pragma unroll 1
        for (int t = 0; t < 66; ++t) { WAIT_V(32); BAR(); G2_STAGE(); }
        WAIT_V(32); BAR(); WAIT_V(24); BAR(); WAIT_V(16); BAR(); WAIT_V(8); BAR(); WAIT_V(0); BAR(); BAR();
        STAMP(3, 7);
        return;
    }
    const int wr = w >> 1, wc = w & 1;
    const int G = l >> 4, q4 = (l >> 2) & 3, p4 = l & 3;
    const int r8 = 4 * (G & 1) + q4;
    unsigned rA = lbase + wr * 4096 + l * 16;
    unsigned rB[2];
#pragma unroll
    for (int n = 0; n < 2; ++n) rB[n] = lbase + 16384 + (G >> 1) * 1024 + r8 * 128 + (((2 * wc + n) ^ (r8 >> 1)) * 32) + p4 * 8;
#define G2_MOVE(d) do { rA += (d); rB[0] += (d); rB[1] += (d); OPAQUE(rA); OPAQUE(rB[0]); OPAQUE(rB[1]); } while (0)

    f32x4 acc[4][2];
    const f32x4 z4 = {0.f, 0.f, 0.f, 0.f};
#pragma unroll
    for (int m = 0; m < 4; ++m) { acc[m][0] = z4; acc[m][1] = z4; }
    bf16x8 fa[2][2][4], fb[2][2][2];
    const int hh = bid & 31, po = bid >> 5, mbo = po * 16 + w * 4;
    f32x4 tmp[16];

#define G2_RD_A(buf, so, kk, m) fa[buf][kk][m] = *(LAS const bf16x8*)(LP(rA) + (so) + (kk) * 8192 + (m) * 1024)
#define G2_RD_B(buf, so, kk, n) fb[buf][kk][n] = tr_frag(LP(rB[n]) + (so) + (kk) * 4096, LP(rB[n]) + (so) + (kk) * 4096 + 2048)
#define G2_READ(buf, so) do { _Pragma("unroll") for (int kk = 0; kk < 2; ++kk) { G2_RD_B(buf, so, kk, 0); G2_RD_B(buf, so, kk, 1); \
        _Pragma("unroll") for (int m = 0; m < 4; ++m) G2_RD_A(buf, so, kk, m); } } while (0)
#define G2_STEP(cur, so, RD) do { BAR(); SCHED(); \
        if (!(G2_EXP & 4)) _Pragma("unroll") for (int kk = 0; kk < 2; ++kk) _Pragma("unroll") for (int m = 0; m < 4; ++m) { \
            MFMA16_ACC(acc[m][0], fb[cur][kk][0], fa[cur][kk][m]); \
            if (RD) { if (m < 2) G2_RD_B((cur) ^ 1, so, kk, m); else G2_RD_A((cur) ^ 1, so, kk, 2 * (m - 2)); } \
            SCHED(); \
            MFMA16_ACC(acc[m][1], fb[cur][kk][1], fa[cur][kk][m]); \
            if (RD) { if (m >= 2) G2_RD_A((cur) ^ 1, so, kk, 2 * (m - 2) + 1); } \
            SCHED(); } \
        WAIT_L0(); SCHED(); } while (0)

    int sl = 0;
#define G2_RUN(n) do { _Pragma("unroll 1") for (int i_ = 0; i_ < (n); ++i_) { \
        G2_STEP(0, G2_SLOT, 1); \
        sl += 2; const bool wrap_ = sl == G2_NS; sl = wrap_ ? 0 : sl; const unsigned d_ = wrap_ ? (unsigned)(-(G2_NS - 2) * G2_SLOT) : (unsigned)(2 * G2_SLOT); G2_MOVE(d_); \
        G2_STEP(1, 0, 1); } } while (0)

    const char* kvsrc = ws + WS_KVP + ((size_t)hh * 12 + 3 * w) * 1024 + l * 16;
    G2_MOVE(0);
    BAR(); SCHED();
    G2_READ(0, 0);
    WAIT_L0(); SCHED();
    STAMP(3, 1);
#pragma unroll
    for (int k = 0; k < 16; ++k) tmp[k] = *(const f32x4*)(kvsrc + (size_t)(k & 7) * 32 * 12 * 1024 + (k >> 3) * 1024);
    SCHED();
    G2_RUN(3);
    STAMP(3, 3);
    {
#pragma unroll
        for (int i = 0; i < 2; ++i) {
            const f32x4 t = ((tmp[8 * i] + tmp[8 * i + 1]) + (tmp[8 * i + 2] + tmp[8 * i + 3])) + ((tmp[8 * i + 4] + tmp[8 * i + 5]) + (tmp[8 * i + 6] + tmp[8 * i + 7]));
            const int id = 3 * w + i;
            u32x2 v = {pk2(t[0], t[1]), pk2(t[2], t[3])};
            *(LAS u32x2*)(lds + G2_KV + (id >> 1) * 1024 + l * 16 + (id & 1) * 8) = v;
        }
        const char* pq = ws + WS_PHIQ + ((size_t)hh * NMB + mbo) * 2048 + l * 16;
#pragma unroll
        for (int k = 0; k < 8; ++k) { tmp[k] = *(const f32x4*)(kvsrc + (size_t)k * 32 * 12 * 1024 + 2048); tmp[8 + k] = *(const f32x4*)(pq + k * 1024); }
        SCHED();
    }
    G2_RUN(3);
    {
        const f32x4 t = ((tmp[0] + tmp[1]) + (tmp[2] + tmp[3])) + ((tmp[4] + tmp[5]) + (tmp[6] + tmp[7]));
        const int id = 3 * w + 2;
        u32x2 v = {pk2(t[0], t[1]), pk2(t[2], t[3])};
        *(LAS u32x2*)(lds + G2_KV + (id >> 1) * 1024 + l * 16 + (id & 1) * 8) = v;
        WAIT_L0(); SCHED();
    }
    G2_RUN(1);
    {
        bf16x8 kf[2][3];
#pragma unroll
        for (int s = 0; s < 2; ++s)
#pragma unroll
            for (int c = 0; c < 3; ++c) kf[s][c] = *(LAS const bf16x8*)(lds + G2_KV + (s * 3 + c) * 1024 + l * 16);
        const __amdgpu_buffer_rsrc_t ro = __builtin_amdgcn_make_buffer_rsrc(ws + WS_A2 + ((size_t)(112 + hh) * NMB + mbo) * 1024, 0, 4096, 0x00020000);
#pragma unroll
        for (int m = 0; m < 4; ++m) {
            const bf16x8 p0 = __builtin_bit_cast(bf16x8, tmp[8 + 2 * m]), p1 = __builtin_bit_cast(bf16x8, tmp[9 + 2 * m]);
            f32x4 n0 = MFMA16(kf[0][0], p0, z4); n0 = MFMA16(kf[1][0], p1, n0);
            f32x4 n1 = MFMA16(kf[0][1], p0, z4); n1 = MFMA16(kf[1][1], p1, n1);
            f32x4 dn = MFMA16(kf[0][2], p0, z4); dn = MFMA16(kf[1][2], p1, dn);
            const float den = bfr(bfr(dn[0]) + 1e-6f);
            const float rd = 1.0f / den;
#pragma unroll
            for (int i = 0; i < 4; ++i) { n0[i] = bfr(n0[i]) * rd; n1[i] = bfr(n1[i]) * rd; }
            __builtin_amdgcn_raw_buffer_store_b128(__builtin_bit_cast(u32x4, pack8(n0, n1)), ro, m * 1024 + l * 16, 0, 16);
        }
        WAIT_L0(); SCHED();
    }
    G2_RUN(3);
    WAIT_V(0); SCHED();
    G2_RUN(1);
    if (tid == 0) __hip_atomic_fetch_add(cnt + 64 * po, 1u, __ATOMIC_RELAXED, __HIP_MEMORY_SCOPE_AGENT);
    SCHED();
    STAMP(3, 4);
    G2_RUN(14);
    STAMP(3, 5);
    G2_RUN(8);
    STAMP(3, 6);
    const int fq = l >> 4, fr = l & 15;
    u32x2 xv[4][2];
    const size_t off0 = (size_t)(pm * 128 + wr * 64 + fr) * D_ + pn * 64 + wc * 32 + 4 * fq;
#pragma unroll
    for (int m = 0; m < 4; ++m) { xv[m][0] = *(const u32x2*)(x + off0 + m * 16 * D_); xv[m][1] = *(const u32x2*)(x + off0 + m * 16 * D_ + 16); }
    SCHED();
    G2_RUN(2);
    G2_STEP(0, G2_SLOT, 1);
    G2_STEP(1, 0, 0);
    asm volatile("s_nop 7\n\ts_nop 7" ::: "memory");
    SCHED();
    STAMP(3, 2);
#pragma unroll
    for (int m = 0; m < 4; ++m)
#pragma unroll
        for (int n = 0; n < 2; ++n) {
            u32x2 o = {pk2(bflo(xv[m][n][0]) + acc[m][n][0], bfhi(xv[m][n][0]) + acc[m][n][1]), pk2(bflo(xv[m][n][1]) + acc[m][n][2], bfhi(xv[m][n][1]) + acc[m][n][3])};
            *(u32x2*)(out + off0 + m * 16 * D_ + n * 16) = o;
        }
    STAMP(3, 7);
}

extern "C" void kernel_launch(void* const* d_in, const int* in_sizes, int n_in, void* d_out, int out_size, void* d_ws, size_t ws_size,
                              hipStream_t stream) {
    const uint16_t* x = (const uint16_t*)d_in[0];
    const uint16_t* wqkv = (const uint16_t*)d_in[1];
    const uint16_t* wphi = (const uint16_t*)d_in[2];
    const uint16_t* wo = (const uint16_t*)d_in[3];
    const uint16_t* wgate = (const uint16_t*)d_in[5];
    const uint16_t* wup = (const uint16_t*)d_in[6];
    const uint16_t* wdown = (const uint16_t*)d_in[7];
    char* ws = (char*)d_ws;
    k_ln<<<256, 256, 0, stream>>>(x, ws);
    k_gemm1<<<256, 512, 0, stream>>>(wqkv, wphi, wgate, wup, ws);
    k_gemm2<<<256, 512, 0, stream>>>(x, wdown, wo, ws, (uint16_t*)d_out);
}
```

```cpp
#include <hip/hip_runtime.h>
#include <hip/hip_bf16.h>
#include <stdint.h>

#define LAS __attribute__((address_space(3)))
#define GAS __attribute__((address_space(1)))
#define DI __device__ __forceinline__
typedef __bf16 bf16x8 __attribute__((ext_vector_type(8)));
typedef __bf16 bf16x2 __attribute__((ext_vector_type(2)));
typedef short s16x4 __attribute__((ext_vector_type(4)));
typedef short s16x8 __attribute__((ext_vector_type(8)));
typedef float f32x4 __attribute__((ext_vector_type(4)));
typedef float f32x2 __attribute__((ext_vector_type(2)));
typedef unsigned u32x4 __attribute__((ext_vector_type(4)));
typedef unsigned u32x2 __attribute__((ext_vector_type(2)));

constexpr int S_ = 2048, D_ = 1024, F_ = 3584, H_ = 32;
constexpr int NMB = S_ / 16;
constexpr size_t WS_XN = 0;
constexpr size_t WS_A2 = WS_XN + (size_t)32 * NMB * 1024;
constexpr size_t WS_PHIQ = WS_A2 + (size_t)144 * NMB * 1024;
constexpr size_t WS_KVP = WS_PHIQ + (size_t)32 * NMB * 2048;
constexpr int KVP_SZ = 8448;
constexpr size_t WS_PART = WS_KVP + (size_t)8 * 32 * KVP_SZ;
constexpr size_t WS_CNT = WS_PART + (size_t)128 * 65536;
constexpr size_t WS_END = WS_CNT + 132 * 256;

#define STAMP(kid, i) do {} while (0)
#define MFMA16(a, b, c) __builtin_amdgcn_mfma_f32_16x16x32_bf16((a), (b), (c), 0, 0, 0)
#define WAIT_V(n) asm volatile("s_waitcnt vmcnt(" #n ")" ::: "memory")
#define WAIT_L(n) asm volatile("s_waitcnt lgkmcnt(" #n ")" ::: "memory")
#define WAIT_L0() __builtin_amdgcn_s_waitcnt(0xC07F)
#define MFMA16_ACC(acc, a, b) asm volatile("v_mfma_f32_16x16x32_bf16 %0, %1, %2, %0" : "+v"(acc) : "v"(a), "v"(b))
#define BAR() __builtin_amdgcn_s_barrier()
#define SCHED() __builtin_amdgcn_sched_barrier(0)

DI unsigned pk2(float lo, float hi) {
    f32x2 v = {lo, hi};
    return __builtin_bit_cast(unsigned, __builtin_convertvector(v, bf16x2));
}
DI float bflo(unsigned u) { return __uint_as_float(u << 16); }
DI float bfhi(unsigned u) { return __uint_as_float(u & 0xffff0000u); }
DI float bfr(float x) { return bflo(pk2(x, x)); }
DI bf16x8 pack8(const f32x4& a, const f32x4& b) {
    u32x4 r = {pk2(a[0], a[1]), pk2(a[2], a[3]), pk2(b[0], b[1]), pk2(b[2], b[3])};
    return __builtin_bit_cast(bf16x8, r);
}
DI bf16x8 tr_frag(LAS const char* p0, LAS const char* p1) {
    s16x4 a = __builtin_amdgcn_ds_read_tr16_b64_v4i16((LAS s16x4*)p0);
    s16x4 b = __builtin_amdgcn_ds_read_tr16_b64_v4i16((LAS s16x4*)p1);
    s16x8 c = __builtin_shufflevector(a, b, 0, 1, 2, 3, 4, 5, 6, 7);
    return __builtin_bit_cast(bf16x8, c);
}
template <int OFF> DI void dma1(const char* g, unsigned voff, unsigned ldsdst) {
    asm volatile("s_mov_b32 m0, %2\n\ts_nop 0\n\tglobal_load_lds_dwordx4 %0, %1 offset:%3" :: "v"(voff), "s"(g), "s"(ldsdst), "i"(OFF) : "memory");
}
DI void dma2(const char* g, unsigned voff, unsigned ldsdst) {
    asm volatile("s_mov_b32 m0, %2\n\ts_nop 0\n\tglobal_load_lds_dwordx4 %0, %1\n\tglobal_load_lds_dwordx4 %0, %1 offset:1024"
                 :: "v"(voff), "s"(g), "s"(ldsdst) : "memory");
}
DI void dma4(const char* g, unsigned voff, unsigned ldsdst) {
    asm volatile("s_mov_b32 m0, %2\n\ts_nop 0\n\tglobal_load_lds_dwordx4 %0, %1\n\tglobal_load_lds_dwordx4 %0, %1 offset:1024\n\t"
                 "global_load_lds_dwordx4 %0, %1 offset:2048\n\tglobal_load_lds_dwordx4 %0, %1 offset:3072"
                 :: "v"(voff), "s"(g), "s"(ldsdst) : "memory");
}
typedef __amdgpu_buffer_rsrc_t rsrc_t;
DI rsrc_t mkrsrc(const void* p, unsigned bytes) { return __builtin_amdgcn_make_buffer_rsrc((void*)p, 0, bytes, 0x00020000); }
template <class T> DI void stw(const T& v, rsrc_t r, unsigned voff, unsigned soff) { __builtin_amdgcn_raw_buffer_store_b128(__builtin_bit_cast(u32x4, v), r, voff + soff, 0, 16); }
#define OPAQUE(x) asm volatile("" : "+v"(x))
#define OPAQUE_S(x) asm volatile("" : "+s"(x))
DI float fmx(float a, float b) { return __builtin_elementwise_maximum(a, b); }
DI void swap16(float& a, float& b) { auto r = __builtin_amdgcn_permlane16_swap(__float_as_uint(a), __float_as_uint(b), false, false); a = __uint_as_float(r[0]); b = __uint_as_float(r[1]); }
DI void swap32(float& a, float& b) { auto r = __builtin_amdgcn_permlane32_swap(__float_as_uint(a), __float_as_uint(b), false, false); a = __uint_as_float(r[0]); b = __uint_as_float(r[1]); }
DI void swap16u(unsigned& a, unsigned& b) { auto r = __builtin_amdgcn_permlane16_swap(a, b, false, false); a = r[0]; b = r[1]; }
template <bool MAX> DI void red2(float& a, float& b) {
    swap16(a, b); float v = MAX ? fmx(a, b) : a + b;
    float u = v; swap32(v, u); v = MAX ? fmx(v, u) : v + u;
    a = v; b = v; swap16(a, b);
}

__global__ __launch_bounds__(256) void k_ln(const uint16_t* __restrict__ x, char* __restrict__ ws) {
    __shared__ float red[2][4][8];
    const int tid = threadIdx.x, w = tid >> 6, l = tid & 63, c = l & 7, r8 = l >> 3;
    const int row = blockIdx.x * 8 + r8;
    STAMP(0, 0);
    const char* xr = (const char*)x + (size_t)row * 2048 + w * 512 + c * 16;
    u32x4 v[4];
#pragma unroll
    for (int i = 0; i < 4; ++i) v[i] = *(const u32x4*)(xr + i * 128);
    float f[32];
#pragma unroll
    for (int i = 0; i < 4; ++i)
#pragma unroll
        for (int j = 0; j < 4; ++j) { f[i * 8 + 2 * j] = bflo(v[i][j]); f[i * 8 + 2 * j + 1] = bfhi(v[i][j]); }
    float s = 0.f;
#pragma unroll
    for (int i = 0; i < 32; ++i) s += f[i];
    s += __shfl_xor(s, 1); s += __shfl_xor(s, 2); s += __shfl_xor(s, 4);
    if (c == 0) red[0][w][r8] = s;
    __syncthreads();
    const float mean = (red[0][0][r8] + red[0][1][r8] + red[0][2][r8] + red[0][3][r8]) * (1.0f / 1024.0f);
    float q = 0.f;
#pragma unroll
    for (int i = 0; i < 32; ++i) { const float d = f[i] - mean; q += d * d; }
    q += __shfl_xor(q, 1); q += __shfl_xor(q, 2); q += __shfl_xor(q, 4);
    if (c == 0) red[1][w][r8] = q;
    __syncthreads();
    const float var = (red[1][0][r8] + red[1][1][r8] + red[1][2][r8] + red[1][3][r8]) * (1.0f / 1024.0f);
    const float mb = bfr(mean);
    const float sd = bfr(sqrtf(bfr(bfr(var) + 1e-5f)));
    const float rs = 1.0f / sd;
    const int mbk = row >> 4, fr = row & 15;
#pragma unroll
    for (int i = 0; i < 4; ++i) {
        u32x4 o;
#pragma unroll
        for (int j = 0; j < 4; ++j) {
            const unsigned d2 = pk2(f[i * 8 + 2 * j] - mb, f[i * 8 + 2 * j + 1] - mb);
            o[j] = pk2(bflo(d2) * rs, bfhi(d2) * rs);
        }
        const int kb = (w * 4 + i) * 2 + (c >> 2), fq = c & 3;
        stw(o, mkrsrc(ws, (unsigned)WS_END), (unsigned)WS_XN + (kb * NMB + mbk) * 1024 + (fq * 16 + fr) * 16, 0);
    }
    STAMP(0, 7);
}

constexpr int G1_SLOT = 36864;
constexpr int G1_WPHI = 4 * G1_SLOT;
constexpr int G1_PF = G1_WPHI + 4096;
constexpr int G1_LDS = G1_PF + 256;
#define G1_PFD 6

DI void hedgehog4(const f32x4 (&p)[4][2], bf16x8 (&pos)[4], bf16x8 (&neg)[4]) {
    const float L2E = 1.44269504088896340736f;
    float mx[4], nm[4];
#pragma unroll
    for (int m = 0; m < 4; ++m) {
        mx[m] = fmx(fmx(fmx(fmx(p[m][0][0], p[m][0][1]), p[m][0][2]), fmx(fmx(p[m][0][3], p[m][1][0]), p[m][1][1])), fmx(p[m][1][2], p[m][1][3]));
        nm[m] = fmx(fmx(fmx(fmx(-p[m][0][0], -p[m][0][1]), -p[m][0][2]), fmx(fmx(-p[m][0][3], -p[m][1][0]), -p[m][1][1])), fmx(-p[m][1][2], -p[m][1][3]));
    }
#pragma unroll
    for (int m = 0; m < 4; ++m) red2<true>(mx[m], nm[m]);
    f32x2 ep[4][4], en[4][4];
    float sp[4], sn[4];
#pragma unroll
    for (int m = 0; m < 4; ++m) {
        const float cp = -mx[m] * L2E, cn = -nm[m] * L2E;
#pragma unroll
        for (int i = 0; i < 4; ++i) {
            const f32x2 x = {p[m][i >> 1][2 * (i & 1)], p[m][i >> 1][2 * (i & 1) + 1]};
            const f32x2 a = __builtin_elementwise_fma(x, (f32x2){L2E, L2E}, (f32x2){cp, cp}), b = __builtin_elementwise_fma(x, (f32x2){-L2E, -L2E}, (f32x2){cn, cn});
            ep[m][i] = (f32x2){__builtin_amdgcn_exp2f(a[0]), __builtin_amdgcn_exp2f(a[1])};
            en[m][i] = (f32x2){__builtin_amdgcn_exp2f(b[0]), __builtin_amdgcn_exp2f(b[1])};
        }
        const f32x2 tp = (ep[m][0] + ep[m][1]) + (ep[m][2] + ep[m][3]), tn = (en[m][0] + en[m][1]) + (en[m][2] + en[m][3]);
        sp[m] = tp[0] + tp[1]; sn[m] = tn[0] + tn[1];
    }
#pragma unroll
    for (int m = 0; m < 4; ++m) red2<false>(sp[m], sn[m]);
#pragma unroll
    for (int m = 0; m < 4; ++m) {
        const float rp = __builtin_amdgcn_rcpf(sp[m]), rn = __builtin_amdgcn_rcpf(sn[m]);
        u32x4 a, b;
#pragma unroll
        for (int i = 0; i < 4; ++i) { const f32x2 x = ep[m][i] * rp, y = en[m][i] * rn; a[i] = pk2(x[0], x[1]); b[i] = pk2(y[0], y[1]); }
        pos[m] = __builtin_bit_cast(bf16x8, a); neg[m] = __builtin_bit_cast(bf16x8, b);
    }
}
DI u32x2 glu4(const f32x4& g, const f32x4& u) {
    u32x2 r;
#pragma unroll
    for (int i = 0; i < 2; ++i) {
        const f32x2 t = (f32x2){g[2 * i], g[2 * i + 1]} * -1.44269504088896340736f;
        const f32x2 d = (f32x2){__builtin_amdgcn_exp2f(t[0]), __builtin_amdgcn_exp2f(t[1])} + 1.0f;
        const f32x2 h = (f32x2){u[2 * i], u[2 * i + 1]} * (f32x2){__builtin_amdgcn_rcpf(d[0]), __builtin_amdgcn_rcpf(d[1])};
        r[i] = pk2(h[0], h[1]);
    }
    return r;
}
DI u32x4 glu8(const f32x4& g0, const f32x4& u0, const f32x4& g1, const f32x4& u1) { const u32x2 a = glu4(g0, u0), b = glu4(g1, u1); return (u32x4){a[0], a[1], b[0], b[1]}; }

__global__ __launch_bounds__(512, 2) void k_gemm1(const uint16_t* __restrict__ wqkv, const uint16_t* __restrict__ wphi,
                                                 const uint16_t* __restrict__ wgate, const uint16_t* __restrict__ wup,
                                                 const uint16_t* __restrict__ wdown, const uint16_t* __restrict__ wo,
                                                 char* __restrict__ ws) {
    __shared__ __attribute__((aligned(16))) char smem_[G1_LDS];
    LAS char* lds = (LAS char*)smem_;
    const int tid = threadIdx.x, l = tid & 63;
    const int w = __builtin_amdgcn_readfirstlane(tid >> 6);
    const int grp = w >> 2;
    const int wr = w & 3, wc = grp;
    const int bid = blockIdx.x, idx = bid >> 3;
    const int ct = (bid & 7) * 4 + (idx & 3), pm = idx >> 2;
    const bool kvt = (ct & 1) != 0;
    const int j = ct >> 1;
    const int F0 = kvt ? 2048 + 96 * j : 128 * j;

    const char* aptr = ws + WS_XN + (size_t)(pm * 16 + 2 * w) * 1024;
    const unsigned avoff = l * 16;
    const char* bptr[3]; unsigned bstep[3], bvoff[3], bdst[3];
#pragma unroll
    for (int i = 0; i < 3; ++i) {
        int t = grp == 0 ? 3 * w + i : 12 + 2 * (w - 4) + (i < 2 ? i : 0);
        const int u = t >> 1, hf = t & 1, uc = u / 5, r = u % 5;
        const uint16_t* base; int col, stride;
        if (!kvt) {
            if (r == 0) { base = wqkv; col = (2 * j + uc) * 32; stride = 3 * D_; }
            else { base = (r & 1) ? wgate : wup; col = F0 + 64 * uc + (r >= 3 ? 32 : 0); stride = F_; }
        } else {
            if (r < 2) { base = wqkv; col = D_ * (1 + r) + (2 * j + uc) * 32; stride = 3 * D_; }
            else if (r >= 3) { base = (r == 3) ? wgate : wup; col = F0 + 32 * uc; stride = F_; }
            else { base = uc == 0 ? wgate : wup; col = F0 + 64; stride = F_; }
        }
        bptr[i] = (const char*)(base + (size_t)(16 * hf) * stride + col);
        bstep[i] = 64u * stride;
        bvoff[i] = (l >> 2) * stride * 2 + ((((l >> 1) & 1) ^ ((l >> 5) & 1)) * 32) + (l & 1) * 16;
        bdst[i] = (unsigned)(uintptr_t)lds + 16384 + t * 1024;
    }
    const unsigned lbase = (unsigned)(uintptr_t)lds;
    const unsigned adst = lbase + 2 * w * 1024;
    int pfleft = 0;
    if (grp == 1) {
        if (w == 7) {
            const int u = bid * 18;
            bptr[2] = u < 3584 - 17 ? (const char*)wdown + (size_t)u * 2048 : u >= 3600 ? (const char*)wo + (size_t)(u - 3584) * 2048 : (const char*)wdown + (size_t)(3584 - 18) * 2048;
            bstep[2] = 2048; bvoff[2] = (l & 15) * 128; pfleft = 17;
        } else {
            const int stride = w == 4 ? 3 * D_ : F_;
            const uint16_t* base = w == 4 ? wqkv + (kvt ? D_ : 0) + 64 * j : (w == 5 ? wgate : wup) + F0;
            bptr[2] = (const char*)(base + (size_t)(32 * G1_PFD + 4 * pm) * stride);
            bstep[2] = 64u * stride; bvoff[2] = ((l >> 1) & 3) * stride * 2 + (l & 1) * (w == 4 ? (kvt ? D_ * 2 : 0) : 128); pfleft = 31 - G1_PFD;
        }
    }
#define G1_STAGE(slot) do { \
        dma2(aptr, avoff, adst + (slot) * G1_SLOT); \
        dma1<0>(bptr[0], bvoff[0], bdst[0] + (slot) * G1_SLOT); dma1<0>(bptr[1], bvoff[1], bdst[1] + (slot) * G1_SLOT); \
        if (grp == 0) { dma1<0>(bptr[2], bvoff[2], bdst[2] + (slot) * G1_SLOT); bptr[2] += bstep[2]; } \
        else { asm volatile("s_mov_b32 m0, %2\n\ts_nop 0\n\tglobal_load_lds_dword %0, %1" :: "v"(bvoff[2]), "s"(bptr[2]), "s"(lbase + G1_PF) : "memory"); \
               bptr[2] += pfleft > 0 ? bstep[2] : 0u; --pfleft; } \
        aptr += NMB * 1024; bptr[0] += bstep[0]; bptr[1] += bstep[1]; \
        OPAQUE_S(aptr); OPAQUE_S(bptr[0]); OPAQUE_S(bptr[1]); OPAQUE_S(bptr[2]); } while (0)
#define G1_WAIT() do { if (grp == 0) WAIT_V(10); else WAIT_V(11); } while (0)

    const int G = l >> 4, q4 = (l >> 2) & 3, p4 = l & 3;
    const unsigned lp0 = G * 512 + q4 * 64 + ((G & 1) * 32) + p4 * 8;
    const unsigned lp1 = lp0 ^ 32;
    unsigned rA = lbase + wr * 4096 + l * 16;
    unsigned rB0 = lbase + 16384 + 5 * wc * 2048 + lp0;
    unsigned rB1 = lbase + 16384 + 5 * wc * 2048 + lp1;
    unsigned rB8 = kvt ? lbase + 16384 + 2 * 2048 + (wc ? lp1 : lp0) : rB0 + 2 * 2048;
    unsigned rB9 = kvt ? lbase + 16384 + 7 * 2048 + (wc ? lp1 : lp0) : rB1 + 2 * 2048;
#define G1_MOVE(d) do { rA += (d); rB0 += (d); rB1 += (d); rB8 += (d); rB9 += (d); \
        OPAQUE(rA); OPAQUE(rB0); OPAQUE(rB1); OPAQUE(rB8); OPAQUE(rB9); } while (0)
#define LP(a) ((LAS const char*)(uintptr_t)(a))

    f32x4 acc[4][10];
#pragma unroll
    for (int m = 0; m < 4; ++m)
#pragma unroll
        for (int n = 0; n < 10; ++n) acc[m][n] = (f32x4){0.f, 0.f, 0.f, 0.f};
    bf16x8 fa[4], fb[10];

#define G1_READ(so) do { \
        _Pragma("unroll") for (int m = 0; m < 4; ++m) fa[m] = *(LAS const bf16x8*)(LP(rA) + (so) + m * 1024); \
        _Pragma("unroll") for (int u = 0; u < 5; ++u) if (u != 2) { \
            fb[2 * u] = tr_frag(LP(rB0) + (so) + u * 2048, LP(rB0) + (so) + u * 2048 + 256); \
            fb[2 * u + 1] = tr_frag(LP(rB1) + (so) + u * 2048, LP(rB1) + (so) + u * 2048 + 256); } \
        fb[4] = tr_frag(LP(rB8) + (so), LP(rB8) + (so) + 256); \
        fb[5] = tr_frag(LP(rB9) + (so), LP(rB9) + (so) + 256); } while (0)
#define G1_MMA() do { __builtin_amdgcn_s_setprio(1); \
        _Pragma("unroll") for (int n = 0; n < 10; ++n) _Pragma("unroll") for (int m = 0; m < 4; ++m) MFMA16_ACC(acc[m][n], fb[n], fa[m]); \
        __builtin_amdgcn_s_setprio(0); } while (0)
#define G1_PHASE(so, STG, VW) do { G1_READ(so); STG; WAIT_L0(); VW; SCHED(); BAR(); SCHED(); G1_MMA(); SCHED(); BAR(); SCHED(); } while (0)

    STAMP(1, 0);
    if (bid == 0 && tid < 132) *(unsigned*)(ws + WS_CNT + tid * 256) = 0u;
    if (grp == 0) dma1<0>((const char*)wphi + (size_t)(2 * j) * 2048 + w * 1024, avoff, lbase + G1_WPHI + w * 1024);
    G1_STAGE(0); G1_STAGE(1); G1_STAGE(2);
    G1_WAIT();
    BAR();
    STAMP(1, 1);
    if (grp == 1) BAR();
    G1_MOVE(0);
    SCHED();
    for (int it = 0; it < 7; ++it) {
        if (w == 0) STAMP(4, it);
        G1_PHASE(0, G1_STAGE(3), G1_WAIT());
        G1_PHASE(G1_SLOT, G1_STAGE(0), G1_WAIT());
        G1_MOVE(2 * G1_SLOT);
        G1_PHASE(0, G1_STAGE(1), G1_WAIT());
        G1_PHASE(G1_SLOT, G1_STAGE(2), G1_WAIT());
        G1_MOVE(-2 * G1_SLOT);
    }
    if (w == 0) STAMP(4, 7);
    G1_PHASE(0, G1_STAGE(3), G1_WAIT());
    G1_PHASE(G1_SLOT, (void)0, WAIT_V(5));
    G1_MOVE(2 * G1_SLOT);
    G1_PHASE(0, (void)0, WAIT_V(0));
    G1_READ(G1_SLOT); WAIT_L0(); SCHED(); BAR(); SCHED(); G1_MMA(); SCHED();
    if (grp == 0) BAR();
    asm volatile("s_nop 7\n\ts_nop 7" ::: "memory");
    SCHED();
    STAMP(1, 2);

    const int fq = l >> 4, fr = l & 15;
    const int hh = 2 * j + wc;
    const int mb0 = pm * 16 + wr * 4;
    const rsrc_t rw = mkrsrc(ws, (unsigned)WS_END);
    const unsigned lo16 = l * 16;
    bf16x8 wp[2];
#pragma unroll
    for (int c = 0; c < 2; ++c) {
        LAS const char* p = lds + G1_WPHI + wc * 2048 + (4 * G + q4) * 64 + c * 32 + p4 * 8;
        wp[c] = tr_frag(p, p + 1024);
    }
    const f32x4 z4 = {0.f, 0.f, 0.f, 0.f};
    f32x4 pf[4][2];
#pragma unroll
    for (int m = 0; m < 4; ++m) {
        const bf16x8 qf = pack8(acc[m][0], acc[m][1]);
        pf[m][0] = MFMA16(wp[0], qf, z4); pf[m][1] = MFMA16(wp[1], qf, z4);
    }
    const unsigned hb = (unsigned)WS_A2 + (((kvt ? F0 + 32 * wc : F0 + 64 * wc + 32) >> 5) * NMB + mb0) * 1024;
#pragma unroll
    for (int m = 0; m < 4; ++m) stw(glu8(acc[m][6], acc[m][8], acc[m][7], acc[m][9]), rw, lo16 + m * 1024, hb);
    u32x4 i0 = {0u, 0u, 0u, 0u}, i1 = {0u, 0u, 0u, 0u};
    {
        const int d = fr - 4 * fq;
        const unsigned lo = (d == 0) ? 0x3f80u : (d == 1) ? 0x3f800000u : 0u;
        const unsigned hi = (d == 2) ? 0x3f80u : (d == 3) ? 0x3f800000u : 0u;
        i0[0] = lo; i0[1] = hi; i1[2] = lo; i1[3] = hi;
    }
    const bf16x8 I0 = __builtin_bit_cast(bf16x8, i0), I1 = __builtin_bit_cast(bf16x8, i1);
    constexpr int ZRED = 65536, HX = ZRED + 2048;
    bf16x8 V[2][2];
    if (!kvt) {
#pragma unroll
        for (int m = 0; m < 4; ++m) stw(glu8(acc[m][2], acc[m][4], acc[m][3], acc[m][5]), rw, lo16 + m * 1024, hb - NMB * 1024);
        V[0][0] = V[0][1] = V[1][0] = V[1][1] = I0;
    } else {
        f32x4 TV[4][2];
#pragma unroll
        for (int m = 0; m < 4; ++m) {
            const bf16x8 vf = pack8(acc[m][2], acc[m][3]);
            TV[m][0] = MFMA16(vf, I0, z4); TV[m][1] = MFMA16(vf, I1, z4);
        }
#pragma unroll
        for (int m = 0; m < 4; ++m) *(LAS u32x2*)(lds + HX + (wr * 4 + m) * 1024 + lo16 + 8 * wc) = glu4(acc[m][4], acc[m][5]);
#pragma unroll
        for (int mp = 0; mp < 2; ++mp) { V[mp][0] = pack8(TV[2 * mp][0], TV[2 * mp + 1][0]); V[mp][1] = pack8(TV[2 * mp][1], TV[2 * mp + 1][1]); }
    }
    SCHED();
    bf16x8 pos[4], neg[4];
    hedgehog4(pf, pos, neg);
    if (!kvt) {
        const unsigned pq = (unsigned)WS_PHIQ + (hh * NMB + mb0) * 2048;
#pragma unroll
        for (int m = 0; m < 4; ++m) { stw(pos[m], rw, lo16 + (m & 1) * 2048, pq + (m >> 1) * 4096); stw(neg[m], rw, lo16 + (m & 1) * 2048 + 1024, pq + (m >> 1) * 4096); }
    } else {
        const u32x4 o4 = {0x3f803f80u, 0x3f803f80u, 0x3f803f80u, 0x3f803f80u};
        const bf16x8 ONES = __builtin_bit_cast(bf16x8, o4);
        f32x4 T[4][2][2];
#pragma unroll
        for (int m = 0; m < 4; ++m) {
            T[m][0][0] = MFMA16(pos[m], I0, z4); T[m][0][1] = MFMA16(pos[m], I1, z4);
            T[m][1][0] = MFMA16(neg[m], I0, z4); T[m][1][1] = MFMA16(neg[m], I1, z4);
        }
        LAS char* red = lds + (wc * 4 + wr) * 8192 + lo16;
#pragma unroll
        for (int s = 0; s < 2; ++s)
#pragma unroll
            for (int cc = 0; cc < 2; ++cc) {
                const bf16x8 P0 = pack8(T[0][s][cc], T[1][s][cc]), P1 = pack8(T[2][s][cc], T[3][s][cc]);
                const f32x4 k0 = MFMA16(P1, V[1][0], MFMA16(P0, V[0][0], z4));
                const f32x4 k1 = MFMA16(P1, V[1][1], MFMA16(P0, V[0][1], z4));
                const f32x4 zz = MFMA16(P1, ONES, MFMA16(P0, ONES, z4));
                *(LAS f32x4*)(red + ((s * 2 + 0) * 2 + cc) * 1024) = k0;
                *(LAS f32x4*)(red + ((s * 2 + 1) * 2 + cc) * 1024) = k1;
                *(LAS f32x4*)(lds + ZRED + (wc * 4 + wr) * 256 + (s * 32 + cc * 16 + 4 * fq) * 4) = zz;
            }
        WAIT_L0(); SCHED(); BAR(); SCHED();
        const unsigned kvo = (unsigned)WS_KVP + (pm * 32 + hh) * KVP_SZ;
#pragma unroll
        for (int i = 0; i < 2; ++i) {
            LAS const char* p = lds + wc * 32768 + (2 * wr + i) * 1024 + lo16;
            const f32x4 sum = (*(LAS const f32x4*)p + *(LAS const f32x4*)(p + 8192)) + (*(LAS const f32x4*)(p + 16384) + *(LAS const f32x4*)(p + 24576));
            stw(sum, rw, lo16 + i * 1024, kvo + 2 * wr * 1024);
            stw(*(LAS const u32x4*)(lds + HX + (wr * 4 + 2 * wc + i) * 1024 + lo16), rw, lo16 + i * 1024, (unsigned)WS_A2 + (((F0 + 64) >> 5) * NMB + mb0 + 2 * wc) * 1024);
        }
        if (wr == 0 && l < 16) {
            LAS const char* p = lds + ZRED + wc * 1024 + lo16;
            const f32x4 sum = (*(LAS const f32x4*)p + *(LAS const f32x4*)(p + 256)) + (*(LAS const f32x4*)(p + 512) + *(LAS const f32x4*)(p + 768));
            stw(sum, rw, lo16, kvo + 8192);
        }
    }
    STAMP(1, 6);
    STAMP(1, 7);
}

constexpr int G2_SLOT = 32768;
constexpr int G2_NS = 4;
constexpr int G2_KV = G2_NS * G2_SLOT;
constexpr int G2_PF = G2_KV + 4352;
constexpr int G2_LDS = G2_PF + 256;
#define G2_NA 32
#define G2_PFD 4
constexpr int G2_NB = 72 - G2_NA;
static_assert(G2_NA % 4 == 0 && G2_NA >= 16 && G2_NA <= 40, "");
typedef GAS unsigned gu32;
DI void wait_word(gu32* p, unsigned seen, unsigned want) {
    if (__builtin_amdgcn_readfirstlane(seen) < want) {
        unsigned spins = 0;
        while (__builtin_amdgcn_readfirstlane(__hip_atomic_load(p, __ATOMIC_RELAXED, __HIP_MEMORY_SCOPE_AGENT)) < want && ++spins < (1u << 22))
            __builtin_amdgcn_s_sleep(2);
    }
}
#define ACQUIRE() asm volatile("buffer_inv sc1" ::: "memory")
__global__ __launch_bounds__(512, 2) void k_gemm2(const uint16_t* __restrict__ x, const uint16_t* __restrict__ wdown, const uint16_t* __restrict__ wo,
                                                 char* __restrict__ ws, uint16_t* __restrict__ out) {
    __shared__ __attribute__((aligned(16))) char smem_[G2_LDS];
    LAS char* lds = (LAS char*)smem_;
    const int tid = threadIdx.x, l = tid & 63;
    const int w8 = __builtin_amdgcn_readfirstlane(tid >> 6), w = w8 & 3;
    const int bid = blockIdx.x, xcd = bid & 7, idx = bid >> 3, t16 = idx >> 1;
    const bool tb = (idx & 1) != 0;
    const int pm = (xcd >> 1) * 4 + (t16 & 3), pn = (xcd & 1) * 4 + (t16 >> 2);
    const int tile = pm * 8 + pn;
    const int k0 = tb ? G2_NA : 0;
    const unsigned lbase = (unsigned)(uintptr_t)lds, lo16 = l * 16;
    gu32* cnt = (gu32*)(ws + WS_CNT);
    gu32* f0 = cnt + 64 * (pm >> 2);
    char* part = ws + WS_PART + (size_t)tile * 65536;
    const rsrc_t rw = mkrsrc(ws, (unsigned)WS_END);
    const int wr = w >> 1, wc = w & 1;
    const int G = l >> 4, q4 = (l >> 2) & 3, p4 = l & 3;
    const f32x4 z4 = {0.f, 0.f, 0.f, 0.f};
    const size_t off0 = (size_t)(pm * 128 + wr * 64 + (l & 15)) * D_ + pn * 128 + wc * 64 + (G & 1) * 16 + (G >> 1) * 8;
    STAMP(3, 0);

    const int hh = xcd * 4 + (t16 & 3), mbo = (t16 >> 2) * 32 + w8 * 4;
    bf16x8 ph[8];
    u32x4 xv[4][2];
    if (!tb) {
        const char* kvsrc = ws + WS_KVP + (size_t)hh * KVP_SZ;
        f32x4 t[8]; float zt[8];
#pragma unroll
        for (int k = 0; k < 8; ++k) t[k] = *(const f32x4*)(kvsrc + (size_t)k * 32 * KVP_SZ + w8 * 1024 + lo16);
        if (w8 == 0) {
#pragma unroll
            for (int k = 0; k < 8; ++k) zt[k] = *(const float*)(kvsrc + (size_t)k * 32 * KVP_SZ + 8192 + l * 4);
        }
        const char* pq = ws + WS_PHIQ + ((size_t)hh * NMB + mbo) * 2048 + lo16;
#pragma unroll
        for (int k = 0; k < 8; ++k) ph[k] = *(const bf16x8*)(pq + k * 1024);
        if (w8 < 4) {
#pragma unroll
            for (int m = 0; m < 4; ++m) { xv[m][0] = *(const u32x4*)(x + off0 + m * 16 * D_); xv[m][1] = *(const u32x4*)(x + off0 + m * 16 * D_ + 32); }
        }
        const f32x4 sum = ((t[0] + t[1]) + (t[2] + t[3])) + ((t[4] + t[5]) + (t[6] + t[7]));
        *(LAS u32x2*)(lds + G2_KV + (w8 >> 1) * 1024 + lo16 + (w8 & 1) * 8) = (u32x2){pk2(sum[0], sum[1]), pk2(sum[2], sum[3])};
        if (w8 == 0) {
            const float z = ((zt[0] + zt[1]) + (zt[2] + zt[3])) + ((zt[4] + zt[5]) + (zt[6] + zt[7]));
            *(LAS unsigned short*)(lds + G2_KV + 4096 + l * 2) = (unsigned short)pk2(z, z);
        }
        __builtin_amdgcn_s_waitcnt(0x0070);
        SCHED(); BAR(); SCHED();
    }
    const bool isb = w >= 2;
    const char* gptr = isb ? (const char*)(wdown + (size_t)(64 * k0 + 32 * (w - 2)) * D_ + pn * 128) : ws + WS_A2 + ((size_t)(2 * k0 + w) * NMB + pm * 8) * 1024;
    const char* gptr_o = (const char*)(wo + (size_t)(32 * (w & 1)) * D_ + pn * 128);
    const unsigned gstep = isb ? 64 * D_ * 2 : 2 * NMB * 1024;
    unsigned voff[4];
#pragma unroll
    for (int i = 0; i < 4; ++i) {
        const int r = 4 * i + G;
        voff[i] = isb ? r * (D_ * 2) + ((((l & 15) >> 1) ^ (r & 7)) * 32) + (l & 1) * 16 - 1024 * i : lo16;
    }
    const unsigned ghalf = isb ? 16 * D_ * 2 : 4096;
    unsigned dst = lbase + w * 8192;
    int tld = k0, slot = 0;
#define G2_DMA4(g, d) asm volatile("s_mov_b32 m0, %5\n\ts_nop 0\n\tglobal_load_lds_dwordx4 %0, %4\n\tglobal_load_lds_dwordx4 %1, %4 offset:1024\n\t" \
            "global_load_lds_dwordx4 %2, %4 offset:2048\n\tglobal_load_lds_dwordx4 %3, %4 offset:3072" \
            :: "v"(voff[0]), "v"(voff[1]), "v"(voff[2]), "v"(voff[3]), "s"(g), "s"(d) : "memory")
#define G2_NEXTSLOT() do { ++slot; const int wrap = slot == G2_NS; slot = wrap ? 0 : slot; dst += wrap ? (unsigned)(-(G2_NS - 1) * G2_SLOT) : (unsigned)G2_SLOT; } while (0)
#define G2_STAGE() do { G2_DMA4(gptr, dst); G2_DMA4(gptr + ghalf, dst + 4096); \
        ++tld; gptr = (isb && tld == 56) ? gptr_o : gptr + gstep; G2_NEXTSLOT(); } while (0)
#define G2_PRUN(n) do { _Pragma("unroll 1") for (int t_ = 0; t_ < (n); ++t_) { WAIT_V(16); BAR(); G2_STAGE(); } } while (0)
    if (w8 >= 4) {
#pragma unroll 1
        for (int t = 0; t < G2_NS; ++t) G2_STAGE();
    }
    if (!tb) {
        bf16x8 kf[2][2], zf[2];
#pragma unroll
        for (int s = 0; s < 2; ++s) {
            kf[s][0] = *(LAS const bf16x8*)(lds + G2_KV + (s * 2) * 1024 + lo16); kf[s][1] = *(LAS const bf16x8*)(lds + G2_KV + (s * 2 + 1) * 1024 + lo16);
            const u32x2 za = *(LAS const u32x2*)(lds + G2_KV + 4096 + s * 64 + G * 8), zb = *(LAS const u32x2*)(lds + G2_KV + 4096 + s * 64 + 32 + G * 8);
            zf[s] = __builtin_bit_cast(bf16x8, (u32x4){za[0], za[1], zb[0], zb[1]});
        }
        f32x4 n0[4], n1[4], dn[4];
#pragma unroll
        for (int m = 0; m < 4; ++m) {
            dn[m] = MFMA16(zf[1], ph[2 * m + 1], MFMA16(zf[0], ph[2 * m], z4));
            n0[m] = MFMA16(kf[1][0], ph[2 * m + 1], MFMA16(kf[0][0], ph[2 * m], z4));
            n1[m] = MFMA16(kf[1][1], ph[2 * m + 1], MFMA16(kf[0][1], ph[2 * m], z4));
        }
#pragma unroll
        for (int m = 0; m < 4; ++m) {
            const float rd = __builtin_amdgcn_rcpf(dn[m][0] + 1e-6f);
            stw(pack8(n0[m] * rd, n1[m] * rd), rw, lo16 + m * 1024, (unsigned)WS_A2 + ((112 + hh) * NMB + mbo) * 1024);
        }
        SCHED();
    }
    if (w8 >= 4) {
        if (!tb) {
            WAIT_V(24); BAR();
            G2_PRUN(G2_NA - 4);
            WAIT_V(16); BAR(); WAIT_V(8); BAR(); WAIT_V(0); BAR(); BAR();
            BAR();
        } else {
            WAIT_V(24); BAR();
            G2_PRUN(G2_NB - 23);
            unsigned seen;
            asm volatile("global_load_dword %0, %1, off sc1" : "=v"(seen) : "v"(f0) : "memory");
            G2_PRUN(3);
            asm volatile("s_waitcnt vmcnt(16)" : "+v"(seen) :: "memory");
            if (!isb) { wait_word(f0, seen, 32u); ACQUIRE(); }
            BAR(); G2_STAGE();
            G2_PRUN(13);
            gu32* f1 = cnt + 64 * (4 + tile);
            asm volatile("global_load_dword %0, %1, off sc1" : "=v"(seen) : "v"(f1) : "memory");
            G2_PRUN(2);
            asm volatile("s_waitcnt vmcnt(16)" : "+v"(seen) :: "memory");
            wait_word(f1, seen, 1u); ACQUIRE();
            BAR(); dma4(part + w * 8192, lo16, dst); dma4(part + w * 8192 + 4096, lo16, dst + 4096); G2_NEXTSLOT();
            WAIT_V(17); BAR(); dma4(part + 32768 + w * 8192, lo16, dst); dma4(part + 32768 + w * 8192 + 4096, lo16, dst + 4096);
            WAIT_V(17); BAR(); BAR();
            WAIT_V(0); BAR();
        }
        STAMP(3, 7);
        return;
    }
    f32x4 acc[4][4];
#pragma unroll
    for (int m = 0; m < 4; ++m)
#pragma unroll
        for (int n = 0; n < 4; ++n) acc[m][n] = z4;
    if (!tb) {
#pragma unroll
        for (int m = 0; m < 4; ++m)
#pragma unroll
            for (int i = 0; i < 2; ++i) {
                unsigned d0 = xv[m][i][0], d1 = xv[m][i][1], d2 = xv[m][i][2], d3 = xv[m][i][3];
                swap16u(d0, d2); swap16u(d1, d3);
                acc[m][2 * i] = (f32x4){bflo(d0), bfhi(d0), bflo(d1), bfhi(d1)}; acc[m][2 * i + 1] = (f32x4){bflo(d2), bfhi(d2), bflo(d3), bfhi(d3)};
            }
    }
    const int r8 = 4 * (G & 1) + q4;
    unsigned rA = lbase + wr * 4096 + lo16;
    unsigned rB[4];
#pragma unroll
    for (int n = 0; n < 4; ++n) rB[n] = lbase + 16384 + (4 * G + q4) * 256 + (((4 * wc + n) ^ r8) * 32) + p4 * 8;
#define G2_MOVE(d) do { rA += (d); rB[0] += (d); rB[1] += (d); rB[2] += (d); rB[3] += (d); OPAQUE(rA); OPAQUE(rB[0]); OPAQUE(rB[1]); OPAQUE(rB[2]); OPAQUE(rB[3]); } while (0)
    bf16x8 fa[2][2][4], fb[2][2][4];
#define G2_RD_A(buf, so, kk, m) fa[buf][kk][m] = *(LAS const bf16x8*)(LP(rA) + (so) + (kk) * 8192 + (m) * 1024)
#define G2_RD_B(buf, so, kk, n) fb[buf][kk][n] = tr_frag(LP(rB[n]) + (so) + (kk) * 8192, LP(rB[n]) + (so) + (kk) * 8192 + 4096)
#define G2_READ(buf, so) do { _Pragma("unroll") for (int kk = 0; kk < 2; ++kk) _Pragma("unroll") for (int i = 0; i < 4; ++i) { G2_RD_B(buf, so, kk, i); G2_RD_A(buf, so, kk, i); } } while (0)
#define G2_STEP(cur, so, RD) do { BAR(); SCHED(); \
        _Pragma("unroll") for (int kk = 0; kk < 2; ++kk) _Pragma("unroll") for (int m = 0; m < 4; ++m) _Pragma("unroll") for (int n = 0; n < 4; ++n) { \
            MFMA16_ACC(acc[m][n], fb[cur][kk][n], fa[cur][kk][m]); \
            if ((RD) && kk == 0) { const int i_ = m * 4 + n; if (i_ & 1) G2_RD_A((cur) ^ 1, so, i_ >> 3, (i_ >> 1) & 3); else G2_RD_B((cur) ^ 1, so, i_ >> 3, (i_ >> 1) & 3); } \
            SCHED(); } \
        WAIT_L0(); SCHED(); } while (0)
    int pfk = k0 / 2 + G2_PFD;
    const int pfend = w >= 2 ? 0 : tb ? 36 : G2_NA / 2;
    const char* pfp = w == 0 ? ws + WS_A2 + ((size_t)(4 * pfk + (t16 >> 2)) * NMB + pm * 8) * 1024 : (const char*)(wdown + (size_t)(128 * pfk + 32 * (t16 & 3)) * D_ + pn * 128);
    const char* pfo = (const char*)(wo + (size_t)(32 * (t16 & 3)) * D_ + pn * 128);
    const unsigned pfs = w == 0 ? 4 * NMB * 1024 : 128 * D_ * 2;
    const unsigned pfv = w == 0 ? l * 128 : (l >> 1) * (D_ * 2) + (l & 1) * 128;
#define G2_PREFETCH() do { if (pfk < pfend) asm volatile("s_mov_b32 m0, %2\n\ts_nop 0\n\tglobal_load_lds_dword %0, %1" :: "v"(pfv), "s"(pfp), "s"(lbase + G2_PF) : "memory"); \
        ++pfk; pfp = (w != 0 && pfk == 28) ? pfo : pfp + pfs; } while (0)
    unsigned mv = 2 * G2_SLOT;
#define G2_RUN(n) do { _Pragma("unroll 1") for (int i_ = 0; i_ < (n); ++i_) { \
        G2_STEP(0, G2_SLOT, 1); G2_MOVE(mv); mv = -mv; G2_PREFETCH(); G2_STEP(1, 0, 1); } } while (0)

    G2_MOVE(0);
    BAR(); SCHED();
    G2_READ(0, 0);
    WAIT_L0(); SCHED();
    STAMP(3, 1);
    if (!tb) WAIT_V(0);
    G2_RUN(2);
    if (!tb && tid == 0) __hip_atomic_fetch_add(cnt + 64 * (t16 >> 2), 1u, __ATOMIC_RELAXED, __HIP_MEMORY_SCOPE_AGENT);
    SCHED();
    constexpr int PCHK = 28 - G2_NA / 2 - G2_PFD;
    static_assert(PCHK >= 4 && PCHK + 1 <= G2_NA / 2 - 1, "");
    G2_RUN(PCHK - 3);
    const unsigned seen = __hip_atomic_load(f0, __ATOMIC_RELAXED, __HIP_MEMORY_SCOPE_AGENT);
    SCHED();
    G2_RUN(1);
    if (tb && w == 0) wait_word(f0, seen, 32u);
    asm volatile("" :: "v"(seen));
    SCHED();
    const int nrest = (tb ? G2_NB : G2_NA) / 2 - 1 - PCHK;
    G2_RUN(nrest);
    G2_STEP(0, G2_SLOT, 1);
    G2_STEP(1, 0, 0);
    asm volatile("s_nop 7\n\ts_nop 7" ::: "memory");
    SCHED();
    if (!tb) {
        STAMP(3, 2);
#pragma unroll
        for (int m = 0; m < 4; ++m)
#pragma unroll
            for (int n = 0; n < 4; ++n) stw(acc[m][n], rw, lo16 + n * 1024, (unsigned)WS_PART + tile * 65536 + w * 16384 + m * 4096);
        WAIT_V(0); BAR();
        if (tid == 0) __hip_atomic_store(cnt + 64 * (4 + tile), 1u, __ATOMIC_RELAXED, __HIP_MEMORY_SCOPE_AGENT);
        STAMP(3, 6);
        return;
    }
    STAMP(3, 4);
    BAR(); SCHED();
    STAMP(3, 3);
#pragma unroll
    for (int m = 0; m < 4; ++m)
#pragma unroll
        for (int i = 0; i < 2; ++i) {
            const f32x4 r0 = acc[m][2 * i] + *(LAS const f32x4*)(lds + w * 16384 + (m * 4 + 2 * i) * 1024 + lo16);
            const f32x4 r1 = acc[m][2 * i + 1] + *(LAS const f32x4*)(lds + w * 16384 + (m * 4 + 2 * i + 1) * 1024 + lo16);
            unsigned d0 = pk2(r0[0], r0[1]), d1 = pk2(r0[2], r0[3]), d2 = pk2(r1[0], r1[1]), d3 = pk2(r1[2], r1[3]);
            swap16u(d0, d2); swap16u(d1, d3);
            *(u32x4*)(out + off0 + m * 16 * D_ + i * 32) = (u32x4){d0, d1, d2, d3};
        }
    STAMP(3, 5);
}

extern "C" void kernel_launch(void* const* d_in, const int* in_sizes, int n_in, void* d_out, int out_size, void* d_ws, size_t ws_size,
                              hipStream_t stream) {
    const uint16_t* x = (const uint16_t*)d_in[0];
    const uint16_t* wqkv = (const uint16_t*)d_in[1];
    const uint16_t* wphi = (const uint16_t*)d_in[2];
    const uint16_t* wo = (const uint16_t*)d_in[3];
    const uint16_t* wgate = (const uint16_t*)d_in[5];
    const uint16_t* wup = (const uint16_t*)d_in[6];
    const uint16_t* wdown = (const uint16_t*)d_in[7];
    char* ws = (char*)d_ws;
    k_ln<<<256, 256, 0, stream>>>(x, ws);
    k_gemm1<<<256, 512, 0, stream>>>(wqkv, wphi, wgate, wup, wdown, wo, ws);
    k_gemm2<<<256, 512, 0, stream>>>(x, wdown, wo, ws, (uint16_t*)d_out);
}
```

```cpp
#include <hip/hip_runtime.h>
#include <hip/hip_bf16.h>
#include <stdint.h>

#define LAS __attribute__((address_space(3)))
#define GAS __attribute__((address_space(1)))
#define DI __device__ __forceinline__
typedef __bf16 bf16x8 __attribute__((ext_vector_type(8)));
typedef __bf16 bf16x2 __attribute__((ext_vector_type(2)));
typedef short s16x4 __attribute__((ext_vector_type(4)));
typedef short s16x8 __attribute__((ext_vector_type(8)));
typedef float f32x4 __attribute__((ext_vector_type(4)));
typedef float f32x2 __attribute__((ext_vector_type(2)));
typedef unsigned u32x4 __attribute__((ext_vector_type(4)));
typedef unsigned u32x2 __attribute__((ext_vector_type(2)));

constexpr int S_ = 2048, D_ = 1024, F_ = 3584, H_ = 32;
constexpr int NMB = S_ / 16;
constexpr size_t WS_XN = 0;
constexpr size_t WS_A2 = WS_XN + (size_t)32 * NMB * 1024;
constexpr size_t WS_PHIQ = WS_A2 + (size_t)144 * NMB * 1024;
constexpr size_t WS_KVP = WS_PHIQ + (size_t)32 * NMB * 2048;
constexpr int KVP_SZ = 8448;
constexpr size_t WS_PART = WS_KVP + (size_t)8 * 32 * KVP_SZ;
constexpr size_t WS_CNT = WS_PART + (size_t)128 * 65536;
constexpr size_t WS_END = WS_CNT + 132 * 256;

#define STAMP(kid, i) do {} while (0)
#define MFMA16(a, b, c) __builtin_amdgcn_mfma_f32_16x16x32_bf16((a), (b), (c), 0, 0, 0)
#define WAIT_V(n) asm volatile("s_waitcnt vmcnt(" #n ")" ::: "memory")
#define WAIT_L(n) asm volatile("s_waitcnt lgkmcnt(" #n ")" ::: "memory")
#define WAIT_L0() __builtin_amdgcn_s_waitcnt(0xC07F)
#define MFMA16_ACC(acc, a, b) asm volatile("v_mfma_f32_16x16x32_bf16 %0, %1, %2, %0" : "+v"(acc) : "v"(a), "v"(b))
#define BAR() __builtin_amdgcn_s_barrier()
#define SCHED() __builtin_amdgcn_sched_barrier(0)

DI unsigned pk2(float lo, float hi) {
    f32x2 v = {lo, hi};
    return __builtin_bit_cast(unsigned, __builtin_convertvector(v, bf16x2));
}
DI float bflo(unsigned u) { return __uint_as_float(u << 16); }
DI float bfhi(unsigned u) { return __uint_as_float(u & 0xffff0000u); }
DI float bfr(float x) { return bflo(pk2(x, x)); }
DI bf16x8 pack8(const f32x4& a, const f32x4& b) {
    u32x4 r = {pk2(a[0], a[1]), pk2(a[2], a[3]), pk2(b[0], b[1]), pk2(b[2], b[3])};
    return __builtin_bit_cast(bf16x8, r);
}
DI bf16x8 tr_frag(LAS const char* p0, LAS const char* p1) {
    s16x4 a = __builtin_amdgcn_ds_read_tr16_b64_v4i16((LAS s16x4*)p0);
    s16x4 b = __builtin_amdgcn_ds_read_tr16_b64_v4i16((LAS s16x4*)p1);
    s16x8 c = __builtin_shufflevector(a, b, 0, 1, 2, 3, 4, 5, 6, 7);
    return __builtin_bit_cast(bf16x8, c);
}
template <int OFF> DI void dma1(const char* g, unsigned voff, unsigned ldsdst) {
    asm volatile("s_mov_b32 m0, %2\n\ts_nop 0\n\tglobal_load_lds_dwordx4 %0, %1 offset:%3" :: "v"(voff), "s"(g), "s"(ldsdst), "i"(OFF) : "memory");
}
DI void dma2(const char* g, unsigned voff, unsigned ldsdst) {
    asm volatile("s_mov_b32 m0, %2\n\ts_nop 0\n\tglobal_load_lds_dwordx4 %0, %1\n\tglobal_load_lds_dwordx4 %0, %1 offset:1024"
                 :: "v"(voff), "s"(g), "s"(ldsdst) : "memory");
}
DI void dma4(const char* g, unsigned voff, unsigned ldsdst) {
    asm volatile("s_mov_b32 m0, %2\n\ts_nop 0\n\tglobal_load_lds_dwordx4 %0, %1\n\tglobal_load_lds_dwordx4 %0, %1 offset:1024\n\t"
                 "global_load_lds_dwordx4 %0, %1 offset:2048\n\tglobal_load_lds_dwordx4 %0, %1 offset:3072"
                 :: "v"(voff), "s"(g), "s"(ldsdst) : "memory");
}
DI void pf_lines(const char* g, unsigned voff, unsigned ldspad, unsigned long long mask) {
    asm volatile("s_mov_b32 m0, %2\n\ts_mov_b64 exec, %3\n\tglobal_load_lds_dword %0, %1\n\ts_mov_b64 exec, -1" :: "v"(voff), "s"(g), "s"(ldspad), "s"(mask) : "memory");
}
typedef __amdgpu_buffer_rsrc_t rsrc_t;
DI rsrc_t mkrsrc(const void* p, unsigned bytes) { return __builtin_amdgcn_make_buffer_rsrc((void*)p, 0, bytes, 0x00020000); }
template <class T> DI void stw(const T& v, rsrc_t r, unsigned voff, unsigned soff) { __builtin_amdgcn_raw_buffer_store_b128(__builtin_bit_cast(u32x4, v), r, voff + soff, 0, 16); }
#define OPAQUE(x) asm volatile("" : "+v"(x))
#define OPAQUE_S(x) asm volatile("" : "+s"(x))
DI float fmx(float a, float b) { return __builtin_elementwise_maximum(a, b); }
DI void swap16(float& a, float& b) { auto r = __builtin_amdgcn_permlane16_swap(__float_as_uint(a), __float_as_uint(b), false, false); a = __uint_as_float(r[0]); b = __uint_as_float(r[1]); }
DI void swap32(float& a, float& b) { auto r = __builtin_amdgcn_permlane32_swap(__float_as_uint(a), __float_as_uint(b), false, false); a = __uint_as_float(r[0]); b = __uint_as_float(r[1]); }
DI void swap16u(unsigned& a, unsigned& b) { auto r = __builtin_amdgcn_permlane16_swap(a, b, false, false); a = r[0]; b = r[1]; }
template <bool MAX> DI void red2(float& a, float& b) {
    swap16(a, b); float v = MAX ? fmx(a, b) : a + b;
    float u = v; swap32(v, u); v = MAX ? fmx(v, u) : v + u;
    a = v; b = v; swap16(a, b);
}

__global__ __launch_bounds__(256) void k_ln(const uint16_t* __restrict__ x, char* __restrict__ ws) {
    __shared__ float red[2][4][8];
    const int tid = threadIdx.x, w = tid >> 6, l = tid & 63, c = l & 7, r8 = l >> 3;
    const int row = blockIdx.x * 8 + r8;
    STAMP(0, 0);
    const char* xr = (const char*)x + (size_t)row * 2048 + w * 512 + c * 16;
    u32x4 v[4];
#pragma unroll
    for (int i = 0; i < 4; ++i) v[i] = *(const u32x4*)(xr + i * 128);
    float f[32];
#pragma unroll
    for (int i = 0; i < 4; ++i)
#pragma unroll
        for (int j = 0; j < 4; ++j) { f[i * 8 + 2 * j] = bflo(v[i][j]); f[i * 8 + 2 * j + 1] = bfhi(v[i][j]); }
    float s = 0.f;
#pragma unroll
    for (int i = 0; i < 32; ++i) s += f[i];
    s += __shfl_xor(s, 1); s += __shfl_xor(s, 2); s += __shfl_xor(s, 4);
    if (c == 0) red[0][w][r8] = s;
    __syncthreads();
    const float mean = (red[0][0][r8] + red[0][1][r8] + red[0][2][r8] + red[0][3][r8]) * (1.0f / 1024.0f);
    float q = 0.f;
#pragma unroll
    for (int i = 0; i < 32; ++i) { const float d = f[i] - mean; q += d * d; }
    q += __shfl_xor(q, 1); q += __shfl_xor(q, 2); q += __shfl_xor(q, 4);
    if (c == 0) red[1][w][r8] = q;
    __syncthreads();
    const float var = (red[1][0][r8] + red[1][1][r8] + red[1][2][r8] + red[1][3][r8]) * (1.0f / 1024.0f);
    const float mb = bfr(mean);
    const float sd = bfr(sqrtf(bfr(bfr(var) + 1e-5f)));
    const float rs = 1.0f / sd;
    const int mbk = row >> 4, fr = row & 15;
#pragma unroll
    for (int i = 0; i < 4; ++i) {
        u32x4 o;
#pragma unroll
        for (int j = 0; j < 4; ++j) {
            const unsigned d2 = pk2(f[i * 8 + 2 * j] - mb, f[i * 8 + 2 * j + 1] - mb);
            o[j] = pk2(bflo(d2) * rs, bfhi(d2) * rs);
        }
        const int kb = (w * 4 + i) * 2 + (c >> 2), fq = c & 3;
        stw(o, mkrsrc(ws, (unsigned)WS_END), (unsigned)WS_XN + (kb * NMB + mbk) * 1024 + (fq * 16 + fr) * 16, 0);
    }
    STAMP(0, 7);
}

constexpr int G1_SLOT = 36864;
constexpr int G1_WPHI = 4 * G1_SLOT;
constexpr int G1_PF = G1_WPHI + 4096;
constexpr int G1_LDS = G1_PF + 256;
#define G1_PFIT 3

DI void hedgehog4(const f32x4 (&p)[4][2], bf16x8 (&pos)[4], bf16x8 (&neg)[4]) {
    const float L2E = 1.44269504088896340736f;
    float mx[4], nm[4];
#pragma unroll
    for (int m = 0; m < 4; ++m) {
        mx[m] = fmx(fmx(fmx(fmx(p[m][0][0], p[m][0][1]), p[m][0][2]), fmx(fmx(p[m][0][3], p[m][1][0]), p[m][1][1])), fmx(p[m][1][2], p[m][1][3]));
        nm[m] = fmx(fmx(fmx(fmx(-p[m][0][0], -p[m][0][1]), -p[m][0][2]), fmx(fmx(-p[m][0][3], -p[m][1][0]), -p[m][1][1])), fmx(-p[m][1][2], -p[m][1][3]));
    }
#pragma unroll
    for (int m = 0; m < 4; ++m) red2<true>(mx[m], nm[m]);
    f32x2 ep[4][4], en[4][4];
    float sp[4], sn[4];
#pragma unroll
    for (int m = 0; m < 4; ++m) {
        const float cp = -mx[m] * L2E, cn = -nm[m] * L2E;
#pragma unroll
        for (int i = 0; i < 4; ++i) {
            const f32x2 x = {p[m][i >> 1][2 * (i & 1)], p[m][i >> 1][2 * (i & 1) + 1]};
            const f32x2 a = __builtin_elementwise_fma(x, (f32x2){L2E, L2E}, (f32x2){cp, cp}), b = __builtin_elementwise_fma(x, (f32x2){-L2E, -L2E}, (f32x2){cn, cn});
            ep[m][i] = (f32x2){__builtin_amdgcn_exp2f(a[0]), __builtin_amdgcn_exp2f(a[1])};
            en[m][i] = (f32x2){__builtin_amdgcn_exp2f(b[0]), __builtin_amdgcn_exp2f(b[1])};
        }
        const f32x2 tp = (ep[m][0] + ep[m][1]) + (ep[m][2] + ep[m][3]), tn = (en[m][0] + en[m][1]) + (en[m][2] + en[m][3]);
        sp[m] = tp[0] + tp[1]; sn[m] = tn[0] + tn[1];
    }
#pragma unroll
    for (int m = 0; m < 4; ++m) red2<false>(sp[m], sn[m]);
#pragma unroll
    for (int m = 0; m < 4; ++m) {
        const float rp = __builtin_amdgcn_rcpf(sp[m]), rn = __builtin_amdgcn_rcpf(sn[m]);
        u32x4 a, b;
#pragma unroll
        for (int i = 0; i < 4; ++i) { const f32x2 x = ep[m][i] * rp, y = en[m][i] * rn; a[i] = pk2(x[0], x[1]); b[i] = pk2(y[0], y[1]); }
        pos[m] = __builtin_bit_cast(bf16x8, a); neg[m] = __builtin_bit_cast(bf16x8, b);
    }
}
DI u32x2 glu4(const f32x4& g, const f32x4& u) {
    u32x2 r;
#pragma unroll
    for (int i = 0; i < 2; ++i) {
        const f32x2 t = (f32x2){g[2 * i], g[2 * i + 1]} * -1.44269504088896340736f;
        const f32x2 d = (f32x2){__builtin_amdgcn_exp2f(t[0]), __builtin_amdgcn_exp2f(t[1])} + 1.0f;
        const f32x2 h = (f32x2){u[2 * i], u[2 * i + 1]} * (f32x2){__builtin_amdgcn_rcpf(d[0]), __builtin_amdgcn_rcpf(d[1])};
        r[i] = pk2(h[0], h[1]);
    }
    return r;
}
DI u32x4 glu8(const f32x4& g0, const f32x4& u0, const f32x4& g1, const f32x4& u1) { const u32x2 a = glu4(g0, u0), b = glu4(g1, u1); return (u32x4){a[0], a[1], b[0], b[1]}; }

__global__ __launch_bounds__(512, 2) void k_gemm1(const uint16_t* __restrict__ wqkv, const uint16_t* __restrict__ wphi,
                                                 const uint16_t* __restrict__ wgate, const uint16_t* __restrict__ wup,
                                                 const uint16_t* __restrict__ wdown, const uint16_t* __restrict__ wo,
                                                 char* __restrict__ ws) {
    __shared__ __attribute__((aligned(16))) char smem_[G1_LDS];
    LAS char* lds = (LAS char*)smem_;
    const int tid = threadIdx.x, l = tid & 63;
    const int w = __builtin_amdgcn_readfirstlane(tid >> 6);
    const int grp = w >> 2;
    const int wr = w & 3, wc = grp;
    const int bid = blockIdx.x, idx = bid >> 3;
    const int ct = (bid & 7) * 4 + (idx & 3), pm = idx >> 2;
    const bool kvt = (ct & 1) != 0;
    const int j = ct >> 1;
    const int F0 = kvt ? 2048 + 96 * j : 128 * j;

    const char* aptr = ws + WS_XN + (size_t)(pm * 16 + 2 * w) * 1024;
    const unsigned avoff = l * 16;
    const char* bptr[3]; unsigned bstep[3], bvoff[3], bdst[3];
#pragma unroll
    for (int i = 0; i < 3; ++i) {
        int t = grp == 0 ? 3 * w + i : 12 + 2 * (w - 4) + (i < 2 ? i : 0);
        const int u = t >> 1, hf = t & 1, uc = u / 5, r = u % 5;
        const uint16_t* base; int col, stride;
        if (!kvt) {
            if (r == 0) { base = wqkv; col = (2 * j + uc) * 32; stride = 3 * D_; }
            else { base = (r & 1) ? wgate : wup; col = F0 + 64 * uc + (r >= 3 ? 32 : 0); stride = F_; }
        } else {
            if (r < 2) { base = wqkv; col = D_ * (1 + r) + (2 * j + uc) * 32; stride = 3 * D_; }
            else if (r >= 3) { base = (r == 3) ? wgate : wup; col = F0 + 32 * uc; stride = F_; }
            else { base = uc == 0 ? wgate : wup; col = F0 + 64; stride = F_; }
        }
        bptr[i] = (const char*)(base + (size_t)(16 * hf) * stride + col);
        bstep[i] = 64u * stride;
        bvoff[i] = (l >> 2) * stride * 2 + ((((l >> 1) & 1) ^ ((l >> 5) & 1)) * 32) + (l & 1) * 16;
        bdst[i] = (unsigned)(uintptr_t)lds + 16384 + t * 1024;
    }
    const unsigned lbase = (unsigned)(uintptr_t)lds;
    const unsigned adst = lbase + 2 * w * 1024;
#define G1_STAGE(slot) do { \
        dma2(aptr, avoff, adst + (slot) * G1_SLOT); \
        dma1<0>(bptr[0], bvoff[0], bdst[0] + (slot) * G1_SLOT); dma1<0>(bptr[1], bvoff[1], bdst[1] + (slot) * G1_SLOT); \
        if (grp == 0) dma1<0>(bptr[2], bvoff[2], bdst[2] + (slot) * G1_SLOT); \
        aptr += NMB * 1024; bptr[0] += bstep[0]; bptr[1] += bstep[1]; bptr[2] += bstep[2]; \
        OPAQUE_S(aptr); OPAQUE_S(bptr[0]); OPAQUE_S(bptr[1]); OPAQUE_S(bptr[2]); } while (0)
#define G1_WAIT() WAIT_V(8)

    const int G = l >> 4, q4 = (l >> 2) & 3, p4 = l & 3;
    const unsigned lp0 = G * 512 + q4 * 64 + ((G & 1) * 32) + p4 * 8;
    const unsigned lp1 = lp0 ^ 32;
    unsigned rA = lbase + wr * 4096 + l * 16;
    unsigned rB0 = lbase + 16384 + 5 * wc * 2048 + lp0;
    unsigned rB1 = lbase + 16384 + 5 * wc * 2048 + lp1;
    unsigned rB8 = kvt ? lbase + 16384 + 2 * 2048 + (wc ? lp1 : lp0) : rB0 + 2 * 2048;
    unsigned rB9 = kvt ? lbase + 16384 + 7 * 2048 + (wc ? lp1 : lp0) : rB1 + 2 * 2048;
#define G1_MOVE(d) do { rA += (d); rB0 += (d); rB1 += (d); rB8 += (d); rB9 += (d); \
        OPAQUE(rA); OPAQUE(rB0); OPAQUE(rB1); OPAQUE(rB8); OPAQUE(rB9); } while (0)
#define LP(a) ((LAS const char*)(uintptr_t)(a))

    f32x4 acc[4][10];
#pragma unroll
    for (int m = 0; m < 4; ++m)
#pragma unroll
        for (int n = 0; n < 10; ++n) acc[m][n] = (f32x4){0.f, 0.f, 0.f, 0.f};
    bf16x8 fa[4], fb[10];

#define G1_READ(so) do { \
        _Pragma("unroll") for (int m = 0; m < 4; ++m) fa[m] = *(LAS const bf16x8*)(LP(rA) + (so) + m * 1024); \
        _Pragma("unroll") for (int u = 0; u < 5; ++u) if (u != 2) { \
            fb[2 * u] = tr_frag(LP(rB0) + (so) + u * 2048, LP(rB0) + (so) + u * 2048 + 256); \
            fb[2 * u + 1] = tr_frag(LP(rB1) + (so) + u * 2048, LP(rB1) + (so) + u * 2048 + 256); } \
        fb[4] = tr_frag(LP(rB8) + (so), LP(rB8) + (so) + 256); \
        fb[5] = tr_frag(LP(rB9) + (so), LP(rB9) + (so) + 256); } while (0)
#define G1_MMA() do { __builtin_amdgcn_s_setprio(1); \
        _Pragma("unroll") for (int n = 0; n < 10; ++n) _Pragma("unroll") for (int m = 0; m < 4; ++m) MFMA16_ACC(acc[m][n], fb[n], fa[m]); \
        __builtin_amdgcn_s_setprio(0); } while (0)
#define G1_PHASE(so, STG, VW) do { G1_READ(so); STG; WAIT_L0(); VW; SCHED(); BAR(); SCHED(); G1_MMA(); SCHED(); BAR(); SCHED(); } while (0)

    STAMP(1, 0);
    if (bid == 0 && tid < 132) *(unsigned*)(ws + WS_CNT + tid * 256) = 0u;
    if (grp == 0) dma1<0>((const char*)wphi + (size_t)(2 * j) * 2048 + w * 1024, avoff, lbase + G1_WPHI + w * 1024);
    G1_STAGE(0); G1_STAGE(1);
    WAIT_V(4);
    BAR();
    G1_STAGE(2);
    STAMP(1, 1);
    if (grp == 1) BAR();
    G1_MOVE(0);
    SCHED();
    for (int it = 0; it < 7; ++it) {
        if (w == 0) STAMP(4, it);
        if (it == G1_PFIT && w >= 3) {
            const int u = bid * 5 + w - 3;
            if (u < 1152) pf_lines(u < 896 ? (const char*)wdown + (size_t)u * 8192 : (const char*)wo + (size_t)(u - 896) * 8192, avoff * 8, lbase + G1_PF, ~0ull);
        }
        G1_PHASE(0, G1_STAGE(3), G1_WAIT());
        G1_PHASE(G1_SLOT, G1_STAGE(0), G1_WAIT());
        G1_MOVE(2 * G1_SLOT);
        G1_PHASE(0, G1_STAGE(1), G1_WAIT());
        G1_PHASE(G1_SLOT, G1_STAGE(2), G1_WAIT());
        G1_MOVE(-2 * G1_SLOT);
    }
    if (w == 0) STAMP(4, 7);
    G1_PHASE(0, G1_STAGE(3), G1_WAIT());
    G1_PHASE(G1_SLOT, (void)0, WAIT_V(4));
    G1_MOVE(2 * G1_SLOT);
    G1_PHASE(0, (void)0, WAIT_V(0));
    G1_READ(G1_SLOT); WAIT_L0(); SCHED(); BAR(); SCHED(); G1_MMA(); SCHED();
    if (grp == 0) BAR();
    asm volatile("s_nop 7\n\ts_nop 7" ::: "memory");
    SCHED();
    STAMP(1, 2);

    const int fq = l >> 4, fr = l & 15;
    const int hh = 2 * j + wc;
    const int mb0 = pm * 16 + wr * 4;
    const rsrc_t rw = mkrsrc(ws, (unsigned)WS_END);
    const unsigned lo16 = l * 16;
    bf16x8 wp[2];
#pragma unroll
    for (int c = 0; c < 2; ++c) {
        LAS const char* p = lds + G1_WPHI + wc * 2048 + (4 * G + q4) * 64 + c * 32 + p4 * 8;
        wp[c] = tr_frag(p, p + 1024);
    }
    const f32x4 z4 = {0.f, 0.f, 0.f, 0.f};
    f32x4 pf[4][2];
#pragma unroll
    for (int m = 0; m < 4; ++m) {
        const bf16x8 qf = pack8(acc[m][0], acc[m][1]);
        pf[m][0] = MFMA16(wp[0], qf, z4); pf[m][1] = MFMA16(wp[1], qf, z4);
    }
    const unsigned hb = (unsigned)WS_A2 + (((kvt ? F0 + 32 * wc : F0 + 64 * wc + 32) >> 5) * NMB + mb0) * 1024;
#pragma unroll
    for (int m = 0; m < 4; ++m) stw(glu8(acc[m][6], acc[m][8], acc[m][7], acc[m][9]), rw, lo16 + m * 1024, hb);
    u32x4 i0 = {0u, 0u, 0u, 0u}, i1 = {0u, 0u, 0u, 0u};
    {
        const int d = fr - 4 * fq;
        const unsigned lo = (d == 0) ? 0x3f80u : (d == 1) ? 0x3f800000u : 0u;
        const unsigned hi = (d == 2) ? 0x3f80u : (d == 3) ? 0x3f800000u : 0u;
        i0[0] = lo; i0[1] = hi; i1[2] = lo; i1[3] = hi;
    }
    const bf16x8 I0 = __builtin_bit_cast(bf16x8, i0), I1 = __builtin_bit_cast(bf16x8, i1);
    constexpr int ZRED = 65536, HX = ZRED + 2048;
    bf16x8 V[2][2];
    if (!kvt) {
#pragma unroll
        for (int m = 0; m < 4; ++m) stw(glu8(acc[m][2], acc[m][4], acc[m][3], acc[m][5]), rw, lo16 + m * 1024, hb - NMB * 1024);
        V[0][0] = V[0][1] = V[1][0] = V[1][1] = I0;
    } else {
        f32x4 TV[4][2];
#pragma unroll
        for (int m = 0; m < 4; ++m) {
            const bf16x8 vf = pack8(acc[m][2], acc[m][3]);
            TV[m][0] = MFMA16(vf, I0, z4); TV[m][1] = MFMA16(vf, I1, z4);
        }
#pragma unroll
        for (int m = 0; m < 4; ++m) *(LAS u32x2*)(lds + HX + (wr * 4 + m) * 1024 + lo16 + 8 * wc) = glu4(acc[m][4], acc[m][5]);
#pragma unroll
        for (int mp = 0; mp < 2; ++mp) { V[mp][0] = pack8(TV[2 * mp][0], TV[2 * mp + 1][0]); V[mp][1] = pack8(TV[2 * mp][1], TV[2 * mp + 1][1]); }
    }
    SCHED();
    bf16x8 pos[4], neg[4];
    hedgehog4(pf, pos, neg);
    if (!kvt) {
        const unsigned pq = (unsigned)WS_PHIQ + (hh * NMB + mb0) * 2048;
#pragma unroll
        for (int m = 0; m < 4; ++m) { stw(pos[m], rw, lo16 + (m & 1) * 2048, pq + (m >> 1) * 4096); stw(neg[m], rw, lo16 + (m & 1) * 2048 + 1024, pq + (m >> 1) * 4096); }
    } else {
        const u32x4 o4 = {0x3f803f80u, 0x3f803f80u, 0x3f803f80u, 0x3f803f80u};
        const bf16x8 ONES = __builtin_bit_cast(bf16x8, o4);
        f32x4 T[4][2][2];
#pragma unroll
        for (int m = 0; m < 4; ++m) {
            T[m][0][0] = MFMA16(pos[m], I0, z4); T[m][0][1] = MFMA16(pos[m], I1, z4);
            T[m][1][0] = MFMA16(neg[m], I0, z4); T[m][1][1] = MFMA16(neg[m], I1, z4);
        }
        LAS char* red = lds + (wc * 4 + wr) * 8192 + lo16;
#pragma unroll
        for (int s = 0; s < 2; ++s)
#pragma unroll
            for (int cc = 0; cc < 2; ++cc) {
                const bf16x8 P0 = pack8(T[0][s][cc], T[1][s][cc]), P1 = pack8(T[2][s][cc], T[3][s][cc]);
                const f32x4 k0 = MFMA16(P1, V[1][0], MFMA16(P0, V[0][0], z4));
                const f32x4 k1 = MFMA16(P1, V[1][1], MFMA16(P0, V[0][1], z4));
                const f32x4 zz = MFMA16(P1, ONES, MFMA16(P0, ONES, z4));
                *(LAS f32x4*)(red + ((s * 2 + 0) * 2 + cc) * 1024) = k0;
                *(LAS f32x4*)(red + ((s * 2 + 1) * 2 + cc) * 1024) = k1;
                *(LAS f32x4*)(lds + ZRED + (wc * 4 + wr) * 256 + (s * 32 + cc * 16 + 4 * fq) * 4) = zz;
            }
        WAIT_L0(); SCHED(); BAR(); SCHED();
        const unsigned kvo = (unsigned)WS_KVP + (pm * 32 + hh) * KVP_SZ;
#pragma unroll
        for (int i = 0; i < 2; ++i) {
            LAS const char* p = lds + wc * 32768 + (2 * wr + i) * 1024 + lo16;
            const f32x4 sum = (*(LAS const f32x4*)p + *(LAS const f32x4*)(p + 8192)) + (*(LAS const f32x4*)(p + 16384) + *(LAS const f32x4*)(p + 24576));
            stw(sum, rw, lo16 + i * 1024, kvo + 2 * wr * 1024);
            stw(*(LAS const u32x4*)(lds + HX + (wr * 4 + 2 * wc + i) * 1024 + lo16), rw, lo16 + i * 1024, (unsigned)WS_A2 + (((F0 + 64) >> 5) * NMB + mb0 + 2 * wc) * 1024);
        }
        if (wr == 0 && l < 16) {
            LAS const char* p = lds + ZRED + wc * 1024 + lo16;
            const f32x4 sum = (*(LAS const f32x4*)p + *(LAS const f32x4*)(p + 256)) + (*(LAS const f32x4*)(p + 512) + *(LAS const f32x4*)(p + 768));
            stw(sum, rw, lo16, kvo + 8192);
        }
    }
    STAMP(1, 6);
    STAMP(1, 7);
}

constexpr int G2_SLOT = 32768;
constexpr int G2_NS = 4;
constexpr int G2_KV = G2_NS * G2_SLOT;
constexpr int G2_LDS = G2_KV + 4352;
#define G2_NA 28
constexpr int G2_NB = 72 - G2_NA;
static_assert(G2_NA % 4 == 0 && G2_NA >= 16 && G2_NA <= 40, "");
typedef GAS unsigned gu32;
DI void wait_word(gu32* p, unsigned seen, unsigned want) {
    if (__builtin_amdgcn_readfirstlane(seen) < want) {
        unsigned spins = 0;
        while (__builtin_amdgcn_readfirstlane(__hip_atomic_load(p, __ATOMIC_RELAXED, __HIP_MEMORY_SCOPE_AGENT)) < want && ++spins < (1u << 22))
            __builtin_amdgcn_s_sleep(2);
    }
}
__global__ __launch_bounds__(512, 2) void k_gemm2(const uint16_t* __restrict__ x, const uint16_t* __restrict__ wdown, const uint16_t* __restrict__ wo,
                                                 char* __restrict__ ws, uint16_t* __restrict__ out) {
    __shared__ __attribute__((aligned(16))) char smem_[G2_LDS];
    LAS char* lds = (LAS char*)smem_;
    const int tid = threadIdx.x, l = tid & 63;
    const int w8 = __builtin_amdgcn_readfirstlane(tid >> 6), w = w8 & 3;
    const int bid = blockIdx.x, xcd = bid & 7, idx = bid >> 3, t16 = idx >> 1;
    const bool tb = (idx & 1) != 0;
    const int pm = (xcd >> 1) * 4 + (t16 & 3), pn = (xcd & 1) * 4 + (t16 >> 2);
    const int tile = pm * 8 + pn;
    const int k0 = tb ? G2_NA : 0;
    const unsigned lbase = (unsigned)(uintptr_t)lds, lo16 = l * 16;
    gu32* cnt = (gu32*)(ws + WS_CNT);
    gu32* f0 = cnt + 64 * (pm >> 2);
    char* part = ws + WS_PART + (size_t)tile * 65536;
    const rsrc_t rw = mkrsrc(ws, (unsigned)WS_END);
    const int wr = w >> 1, wc = w & 1;
    const int G = l >> 4, q4 = (l >> 2) & 3, p4 = l & 3;
    const f32x4 z4 = {0.f, 0.f, 0.f, 0.f};
    const size_t off0 = (size_t)(pm * 128 + wr * 64 + (l & 15)) * D_ + pn * 128 + wc * 64 + (G & 1) * 16 + (G >> 1) * 8;
    STAMP(3, 0);
    STAMP(5 + tb, 0);

    const int hh = xcd * 4 + (t16 & 3), mbo = (t16 >> 2) * 32 + w8 * 4;
    bf16x8 ph[8];
    u32x4 xv[4][2];
    if (!tb) {
        const char* kvsrc = ws + WS_KVP + (size_t)hh * KVP_SZ;
        f32x4 t[8]; float zt[8];
#pragma unroll
        for (int k = 0; k < 8; ++k) t[k] = *(const f32x4*)(kvsrc + (size_t)k * 32 * KVP_SZ + w8 * 1024 + lo16);
        if (w8 == 0) {
#pragma unroll
            for (int k = 0; k < 8; ++k) zt[k] = *(const float*)(kvsrc + (size_t)k * 32 * KVP_SZ + 8192 + l * 4);
        }
        const char* pq = ws + WS_PHIQ + ((size_t)hh * NMB + mbo) * 2048 + lo16;
#pragma unroll
        for (int k = 0; k < 8; ++k) ph[k] = *(const bf16x8*)(pq + k * 1024);
        if (w8 < 4) {
#pragma unroll
            for (int m = 0; m < 4; ++m) { xv[m][0] = *(const u32x4*)(x + off0 + m * 16 * D_); xv[m][1] = *(const u32x4*)(x + off0 + m * 16 * D_ + 32); }
        }
        const f32x4 sum = ((t[0] + t[1]) + (t[2] + t[3])) + ((t[4] + t[5]) + (t[6] + t[7]));
        *(LAS u32x2*)(lds + G2_KV + (w8 >> 1) * 1024 + lo16 + (w8 & 1) * 8) = (u32x2){pk2(sum[0], sum[1]), pk2(sum[2], sum[3])};
        if (w8 == 0) {
            const float z = ((zt[0] + zt[1]) + (zt[2] + zt[3])) + ((zt[4] + zt[5]) + (zt[6] + zt[7]));
            *(LAS unsigned short*)(lds + G2_KV + 4096 + l * 2) = (unsigned short)pk2(z, z);
        }
        __builtin_amdgcn_s_waitcnt(0x0070);
        STAMP(5, 1);
        SCHED(); BAR(); SCHED();
        STAMP(5, 2);
    }
    const bool isb = w >= 2;
    const char* gptr = isb ? (const char*)(wdown + (size_t)(64 * k0 + 32 * (w - 2)) * D_ + pn * 128) : ws + WS_A2 + ((size_t)(2 * k0 + w) * NMB + pm * 8) * 1024;
    const char* gptr_o = (const char*)(wo + (size_t)(32 * (w & 1)) * D_ + pn * 128);
    const unsigned gstep = isb ? 64 * D_ * 2 : 2 * NMB * 1024;
    unsigned voff[4];
#pragma unroll
    for (int i = 0; i < 4; ++i) {
        const int r = 4 * i + G;
        voff[i] = isb ? r * (D_ * 2) + ((((l & 15) >> 1) ^ (r & 7)) * 32) + (l & 1) * 16 - 1024 * i : lo16;
    }
    const unsigned ghalf = isb ? 16 * D_ * 2 : 4096;
    unsigned dst = lbase + w * 8192;
    int tld = k0, slot = 0;
#define G2_DMA4_(g, d, SC) asm volatile("s_mov_b32 m0, %5\n\ts_nop 0\n\tglobal_load_lds_dwordx4 %0, %4" SC "\n\tglobal_load_lds_dwordx4 %1, %4 offset:1024" SC "\n\t" \
            "global_load_lds_dwordx4 %2, %4 offset:2048" SC "\n\tglobal_load_lds_dwordx4 %3, %4 offset:3072" SC \
            :: "v"(voff[0]), "v"(voff[1]), "v"(voff[2]), "v"(voff[3]), "s"(g), "s"(d) : "memory")
#define G2_DMA4(g, d) G2_DMA4_(g, d, " sc1")
#define G2_NEXTSLOT() do { ++slot; const int wrap = slot == G2_NS; slot = wrap ? 0 : slot; dst += wrap ? (unsigned)(-(G2_NS - 1) * G2_SLOT) : (unsigned)G2_SLOT; } while (0)
#define G2_STAGE() do { \
        if (!isb && tld >= 56) { G2_DMA4(gptr, dst); G2_DMA4(gptr + ghalf, dst + 4096); } else { G2_DMA4_(gptr, dst, ""); G2_DMA4_(gptr + ghalf, dst + 4096, ""); } \
        ++tld; gptr = (isb && tld == 56) ? gptr_o : gptr + gstep; G2_NEXTSLOT(); } while (0)
#define G2_PRUN(n) do { _Pragma("unroll 1") for (int t_ = 0; t_ < (n); ++t_) { WAIT_V(16); BAR(); G2_STAGE(); } } while (0)
    if (w8 >= 4) { G2_STAGE(); G2_STAGE(); STAMP(5 + tb, 3); }
    if (!tb) {
        bf16x8 kf[2][2], zf[2];
#pragma unroll
        for (int s = 0; s < 2; ++s) {
            kf[s][0] = *(LAS const bf16x8*)(lds + G2_KV + (s * 2) * 1024 + lo16); kf[s][1] = *(LAS const bf16x8*)(lds + G2_KV + (s * 2 + 1) * 1024 + lo16);
            const u32x2 za = *(LAS const u32x2*)(lds + G2_KV + 4096 + s * 64 + G * 8), zb = *(LAS const u32x2*)(lds + G2_KV + 4096 + s * 64 + 32 + G * 8);
            zf[s] = __builtin_bit_cast(bf16x8, (u32x4){za[0], za[1], zb[0], zb[1]});
        }
        f32x4 n0[4], n1[4], dn[4];
#pragma unroll
        for (int m = 0; m < 4; ++m) {
            dn[m] = MFMA16(zf[1], ph[2 * m + 1], MFMA16(zf[0], ph[2 * m], z4));
            n0[m] = MFMA16(kf[1][0], ph[2 * m + 1], MFMA16(kf[0][0], ph[2 * m], z4));
            n1[m] = MFMA16(kf[1][1], ph[2 * m + 1], MFMA16(kf[0][1], ph[2 * m], z4));
        }
#pragma unroll
        for (int m = 0; m < 4; ++m) {
            const float rd = __builtin_amdgcn_rcpf(dn[m][0] + 1e-6f);
            stw(pack8(n0[m] * rd, n1[m] * rd), rw, lo16 + m * 1024, (unsigned)WS_A2 + ((112 + hh) * NMB + mbo) * 1024);
        }
        SCHED();
    }
    if (w8 >= 4) {
        WAIT_V(8);
        STAMP(5 + tb, 1 + 3 * !tb);
        BAR(); G2_STAGE();
        WAIT_V(8); BAR(); G2_STAGE(); G2_STAGE();
        if (!tb) {
            G2_PRUN(G2_NA - 5);
            WAIT_V(16); BAR(); WAIT_V(8); BAR(); WAIT_V(0); BAR(); BAR();
            BAR();
        } else {
            G2_PRUN(G2_NB - 24);
            unsigned seen;
            asm volatile("global_load_dword %0, %1, off sc1" : "=v"(seen) : "v"(f0) : "memory");
            G2_PRUN(3);
            asm volatile("s_waitcnt vmcnt(16)" : "+v"(seen) :: "memory");
            STAMP(6, 2);
            if (!isb) wait_word(f0, seen, 32u);
            BAR(); G2_STAGE();
            G2_PRUN(13);
            gu32* f1 = cnt + 64 * (4 + tile);
            asm volatile("global_load_dword %0, %1, off sc1" : "=v"(seen) : "v"(f1) : "memory");
            G2_PRUN(2);
            asm volatile("s_waitcnt vmcnt(16)" : "+v"(seen) :: "memory");
            STAMP(6, 4);
            wait_word(f1, seen, 1u);
            STAMP(6, 5);
#pragma unroll
            for (int i = 0; i < 4; ++i) voff[i] = lo16;
            BAR(); G2_DMA4(part + w * 8192, dst); G2_DMA4(part + w * 8192 + 4096, dst + 4096); G2_NEXTSLOT();
            WAIT_V(16); BAR(); G2_DMA4(part + 32768 + w * 8192, dst); G2_DMA4(part + 32768 + w * 8192 + 4096, dst + 4096);
            WAIT_V(16);
            STAMP(6, 6);
            BAR(); BAR();
            WAIT_V(0);
            STAMP(6, 7);
            BAR();
        }
        STAMP(3, 7);
        return;
    }
    f32x4 acc[4][4];
#pragma unroll
    for (int m = 0; m < 4; ++m)
#pragma unroll
        for (int n = 0; n < 4; ++n) acc[m][n] = z4;
    if (!tb) {
#pragma unroll
        for (int m = 0; m < 4; ++m)
#pragma unroll
            for (int i = 0; i < 2; ++i) {
                unsigned d0 = xv[m][i][0], d1 = xv[m][i][1], d2 = xv[m][i][2], d3 = xv[m][i][3];
                swap16u(d0, d2); swap16u(d1, d3);
                acc[m][2 * i] = (f32x4){bflo(d0), bfhi(d0), bflo(d1), bfhi(d1)}; acc[m][2 * i + 1] = (f32x4){bflo(d2), bfhi(d2), bflo(d3), bfhi(d3)};
            }
    }
    const int r8 = 4 * (G & 1) + q4;
    unsigned rA = lbase + wr * 4096 + lo16;
    unsigned rB[4];
#pragma unroll
    for (int n = 0; n < 4; ++n) rB[n] = lbase + 16384 + (4 * G + q4) * 256 + (((4 * wc + n) ^ r8) * 32) + p4 * 8;
#define G2_MOVE(d) do { rA += (d); rB[0] += (d); rB[1] += (d); rB[2] += (d); rB[3] += (d); OPAQUE(rA); OPAQUE(rB[0]); OPAQUE(rB[1]); OPAQUE(rB[2]); OPAQUE(rB[3]); } while (0)
    bf16x8 fa[2][2][4], fb[2][2][4];
#define G2_RD_A(buf, so, kk, m) fa[buf][kk][m] = *(LAS const bf16x8*)(LP(rA) + (so) + (kk) * 8192 + (m) * 1024)
#define G2_RD_B(buf, so, kk, n) fb[buf][kk][n] = tr_frag(LP(rB[n]) + (so) + (kk) * 8192, LP(rB[n]) + (so) + (kk) * 8192 + 4096)
#define G2_READ(buf, so) do { _Pragma("unroll") for (int kk = 0; kk < 2; ++kk) _Pragma("unroll") for (int i = 0; i < 4; ++i) { G2_RD_B(buf, so, kk, i); G2_RD_A(buf, so, kk, i); } } while (0)
#define G2_STEP(cur, so, RD) do { BAR(); SCHED(); \
        _Pragma("unroll") for (int kk = 0; kk < 2; ++kk) _Pragma("unroll") for (int m = 0; m < 4; ++m) _Pragma("unroll") for (int n = 0; n < 4; ++n) { \
            MFMA16_ACC(acc[m][n], fb[cur][kk][n], fa[cur][kk][m]); \
            if ((RD) && kk == 0) { const int i_ = m * 4 + n; if (i_ & 1) G2_RD_A((cur) ^ 1, so, i_ >> 3, (i_ >> 1) & 3); else G2_RD_B((cur) ^ 1, so, i_ >> 3, (i_ >> 1) & 3); } \
            SCHED(); } \
        WAIT_L0(); SCHED(); } while (0)
    unsigned mv = 2 * G2_SLOT;
#define G2_RUN(n) do { _Pragma("unroll 1") for (int i_ = 0; i_ < (n); ++i_) { \
        G2_STEP(0, G2_SLOT, 1); G2_MOVE(mv); mv = -mv; G2_STEP(1, 0, 1); } } while (0)

    G2_MOVE(0);
    BAR(); SCHED();
    G2_READ(0, 0);
    WAIT_L0(); SCHED();
    STAMP(3, 1);
    if (!tb) WAIT_V(0);
    G2_RUN(1);
    if (!tb && tid == 0) __hip_atomic_fetch_add(cnt + 64 * (t16 >> 2), 1u, __ATOMIC_RELAXED, __HIP_MEMORY_SCOPE_AGENT);
    SCHED();
    const int nrest = (tb ? G2_NB : G2_NA) / 2 - 2;
    G2_RUN(nrest);
    G2_STEP(0, G2_SLOT, 1);
    G2_STEP(1, 0, 0);
    asm volatile("s_nop 7\n\ts_nop 7" ::: "memory");
    SCHED();
    if (!tb) {
        STAMP(3, 2);
#pragma unroll
        for (int m = 0; m < 4; ++m)
#pragma unroll
            for (int n = 0; n < 4; ++n) stw(acc[m][n], rw, lo16 + n * 1024, (unsigned)WS_PART + tile * 65536 + w * 16384 + m * 4096);
        WAIT_V(0); BAR();
        if (tid == 0) __hip_atomic_store(cnt + 64 * (4 + tile), 1u, __ATOMIC_RELAXED, __HIP_MEMORY_SCOPE_AGENT);
        STAMP(3, 6);
        return;
    }
    STAMP(3, 4);
    BAR(); SCHED();
    STAMP(3, 3);
#pragma unroll
    for (int m = 0; m < 4; ++m)
#pragma unroll
        for (int i = 0; i < 2; ++i) {
            const f32x4 r0 = acc[m][2 * i] + *(LAS const f32x4*)(lds + w * 16384 + (m * 4 + 2 * i) * 1024 + lo16);
            const f32x4 r1 = acc[m][2 * i + 1] + *(LAS const f32x4*)(lds + w * 16384 + (m * 4 + 2 * i + 1) * 1024 + lo16);
            unsigned d0 = pk2(r0[0], r0[1]), d1 = pk2(r0[2], r0[3]), d2 = pk2(r1[0], r1[1]), d3 = pk2(r1[2], r1[3]);
            swap16u(d0, d2); swap16u(d1, d3);
            *(u32x4*)(out + off0 + m * 16 * D_ + i * 32) = (u32x4){d0, d1, d2, d3};
        }
    STAMP(3, 5);
}

extern "C" void kernel_launch(void* const* d_in, const int* in_sizes, int n_in, void* d_out, int out_size, void* d_ws, size_t ws_size,
                              hipStream_t stream) {
    const uint16_t* x = (const uint16_t*)d_in[0];
    const uint16_t* wqkv = (const uint16_t*)d_in[1];
    const uint16_t* wphi = (const uint16_t*)d_in[2];
    const uint16_t* wo = (const uint16_t*)d_in[3];
    const uint16_t* wgate = (const uint16_t*)d_in[5];
    const uint16_t* wup = (const uint16_t*)d_in[6];
    const uint16_t* wdown = (const uint16_t*)d_in[7];
    char* ws = (char*)d_ws;
    k_ln<<<256, 256, 0, stream>>>(x, ws);
    k_gemm1<<<256, 512, 0, stream>>>(wqkv, wphi, wgate, wup, wdown, wo, ws);
    k_gemm2<<<256, 512, 0, stream>>>(x, wdown, wo, ws, (uint16_t*)d_out);
}
```

```cpp
#include <hip/hip_runtime.h>
#include <hip/hip_bf16.h>
#include <stdint.h>

#define LAS __attribute__((address_space(3)))
#define GAS __attribute__((address_space(1)))
#define DI __device__ __forceinline__
typedef __bf16 bf16x8 __attribute__((ext_vector_type(8)));
typedef __bf16 bf16x2 __attribute__((ext_vector_type(2)));
typedef short s16x4 __attribute__((ext_vector_type(4)));
typedef short s16x8 __attribute__((ext_vector_type(8)));
typedef float f32x4 __attribute__((ext_vector_type(4)));
typedef float f32x2 __attribute__((ext_vector_type(2)));
typedef unsigned u32x4 __attribute__((ext_vector_type(4)));
typedef unsigned u32x2 __attribute__((ext_vector_type(2)));

constexpr int S_ = 2048, D_ = 1024, F_ = 3584, H_ = 32;
constexpr int NMB = S_ / 16;
constexpr size_t WS_XN = 0;
constexpr size_t WS_A2 = WS_XN + (size_t)32 * NMB * 1024;
constexpr size_t WS_KVP = WS_A2 + (size_t)144 * NMB * 1024;
constexpr int KVP_SZ = 8448;
constexpr size_t WS_PART = WS_KVP + (size_t)8 * 32 * KVP_SZ;
constexpr size_t WS_CNT = WS_PART + (size_t)128 * 65536;
constexpr int C_HQ = 0, C_HK = 8, C_O = 16, C_KV = 24, C_P = 40, C_N = 168;
constexpr size_t WS_END = WS_CNT + C_N * 256;

#define STAMP(kid, i) do {} while (0)
#define MFMA16(a, b, c) __builtin_amdgcn_mfma_f32_16x16x32_bf16((a), (b), (c), 0, 0, 0)
#define WAIT_V(n) asm volatile("s_waitcnt vmcnt(" #n ")" ::: "memory")
#define WAIT_L(n) asm volatile("s_waitcnt lgkmcnt(" #n ")" ::: "memory")
#define WAIT_L0() __builtin_amdgcn_s_waitcnt(0xC07F)
#define MFMA16_ACC(acc, a, b) asm volatile("v_mfma_f32_16x16x32_bf16 %0, %1, %2, %0" : "+v"(acc) : "v"(a), "v"(b))
#define BAR() __builtin_amdgcn_s_barrier()
#define SCHED() __builtin_amdgcn_sched_barrier(0)

DI unsigned pk2(float lo, float hi) {
    f32x2 v = {lo, hi};
    return __builtin_bit_cast(unsigned, __builtin_convertvector(v, bf16x2));
}
DI float bflo(unsigned u) { return __uint_as_float(u << 16); }
DI float bfhi(unsigned u) { return __uint_as_float(u & 0xffff0000u); }
DI float bfr(float x) { return bflo(pk2(x, x)); }
DI bf16x8 pack8(const f32x4& a, const f32x4& b) {
    u32x4 r = {pk2(a[0], a[1]), pk2(a[2], a[3]), pk2(b[0], b[1]), pk2(b[2], b[3])};
    return __builtin_bit_cast(bf16x8, r);
}
DI bf16x8 tr_frag(LAS const char* p0, LAS const char* p1) {
    s16x4 a = __builtin_amdgcn_ds_read_tr16_b64_v4i16((LAS s16x4*)p0);
    s16x4 b = __builtin_amdgcn_ds_read_tr16_b64_v4i16((LAS s16x4*)p1);
    s16x8 c = __builtin_shufflevector(a, b, 0, 1, 2, 3, 4, 5, 6, 7);
    return __builtin_bit_cast(bf16x8, c);
}
template <int OFF> DI void dma1(const char* g, unsigned voff, unsigned ldsdst) {
    asm volatile("s_mov_b32 m0, %2\n\ts_nop 0\n\tglobal_load_lds_dwordx4 %0, %1 offset:%3" :: "v"(voff), "s"(g), "s"(ldsdst), "i"(OFF) : "memory");
}
DI void dma2(const char* g, unsigned voff, unsigned ldsdst) {
    asm volatile("s_mov_b32 m0, %2\n\ts_nop 0\n\tglobal_load_lds_dwordx4 %0, %1\n\tglobal_load_lds_dwordx4 %0, %1 offset:1024"
                 :: "v"(voff), "s"(g), "s"(ldsdst) : "memory");
}
DI void dma4(const char* g, unsigned voff, unsigned ldsdst) {
    asm volatile("s_mov_b32 m0, %2\n\ts_nop 0\n\tglobal_load_lds_dwordx4 %0, %1\n\tglobal_load_lds_dwordx4 %0, %1 offset:1024\n\t"
                 "global_load_lds_dwordx4 %0, %1 offset:2048\n\tglobal_load_lds_dwordx4 %0, %1 offset:3072"
                 :: "v"(voff), "s"(g), "s"(ldsdst) : "memory");
}
DI void pf_lines(const char* g, unsigned voff, unsigned ldspad, unsigned long long mask) {
    asm volatile("s_mov_b32 m0, %2\n\ts_mov_b64 exec, %3\n\tglobal_load_lds_dword %0, %1\n\ts_mov_b64 exec, -1" :: "v"(voff), "s"(g), "s"(ldspad), "s"(mask) : "memory");
}
typedef __amdgpu_buffer_rsrc_t rsrc_t;
DI rsrc_t mkrsrc(const void* p, unsigned bytes) { return __builtin_amdgcn_make_buffer_rsrc((void*)p, 0, bytes, 0x00020000); }
template <class T> DI void stw(const T& v, rsrc_t r, unsigned voff, unsigned soff) { __builtin_amdgcn_raw_buffer_store_b128(__builtin_bit_cast(u32x4, v), r, voff + soff, 0, 16); }
#define OPAQUE(x) asm volatile("" : "+v"(x))
#define OPAQUE_S(x) asm volatile("" : "+s"(x))
DI float fmx(float a, float b) { return __builtin_elementwise_maximum(a, b); }
DI void swap16(float& a, float& b) { auto r = __builtin_amdgcn_permlane16_swap(__float_as_uint(a), __float_as_uint(b), false, false); a = __uint_as_float(r[0]); b = __uint_as_float(r[1]); }
DI void swap32(float& a, float& b) { auto r = __builtin_amdgcn_permlane32_swap(__float_as_uint(a), __float_as_uint(b), false, false); a = __uint_as_float(r[0]); b = __uint_as_float(r[1]); }
DI void swap16u(unsigned& a, unsigned& b) { auto r = __builtin_amdgcn_permlane16_swap(a, b, false, false); a = r[0]; b = r[1]; }
template <bool MAX> DI void red2(float& a, float& b) {
    swap16(a, b); float v = MAX ? fmx(a, b) : a + b;
    float u = v; swap32(v, u); v = MAX ? fmx(v, u) : v + u;
    a = v; b = v; swap16(a, b);
}

__global__ __launch_bounds__(256) void k_ln(const uint16_t* __restrict__ x, char* __restrict__ ws) {
    __shared__ float red[2][4][8];
    const int tid = threadIdx.x, w = tid >> 6, l = tid & 63, c = l & 7, r8 = l >> 3;
    const int row = blockIdx.x * 8 + r8;
    STAMP(0, 0);
    if (blockIdx.x == 0 && tid < C_N) *(unsigned*)(ws + WS_CNT + tid * 256) = 0u;
    const char* xr = (const char*)x + (size_t)row * 2048 + w * 512 + c * 16;
    u32x4 v[4];
#pragma unroll
    for (int i = 0; i < 4; ++i) v[i] = *(const u32x4*)(xr + i * 128);
    float f[32];
#pragma unroll
    for (int i = 0; i < 4; ++i)
#pragma unroll
        for (int j = 0; j < 4; ++j) { f[i * 8 + 2 * j] = bflo(v[i][j]); f[i * 8 + 2 * j + 1] = bfhi(v[i][j]); }
    float s = 0.f;
#pragma unroll
    for (int i = 0; i < 32; ++i) s += f[i];
    s += __shfl_xor(s, 1); s += __shfl_xor(s, 2); s += __shfl_xor(s, 4);
    if (c == 0) red[0][w][r8] = s;
    __syncthreads();
    const float mean = (red[0][0][r8] + red[0][1][r8] + red[0][2][r8] + red[0][3][r8]) * (1.0f / 1024.0f);
    float q = 0.f;
#pragma unroll
    for (int i = 0; i < 32; ++i) { const float d = f[i] - mean; q += d * d; }
    q += __shfl_xor(q, 1); q += __shfl_xor(q, 2); q += __shfl_xor(q, 4);
    if (c == 0) red[1][w][r8] = q;
    __syncthreads();
    const float var = (red[1][0][r8] + red[1][1][r8] + red[1][2][r8] + red[1][3][r8]) * (1.0f / 1024.0f);
    const float mb = bfr(mean);
    const float sd = bfr(sqrtf(bfr(bfr(var) + 1e-5f)));
    const float rs = 1.0f / sd;
    const int mbk = row >> 4, fr = row & 15;
#pragma unroll
    for (int i = 0; i < 4; ++i) {
        u32x4 o;
#pragma unroll
        for (int j = 0; j < 4; ++j) {
            const unsigned d2 = pk2(f[i * 8 + 2 * j] - mb, f[i * 8 + 2 * j + 1] - mb);
            o[j] = pk2(bflo(d2) * rs, bfhi(d2) * rs);
        }
        const int kb = (w * 4 + i) * 2 + (c >> 2), fq = c & 3;
        stw(o, mkrsrc(ws, (unsigned)WS_END), (unsigned)WS_XN + (kb * NMB + mbk) * 1024 + (fq * 16 + fr) * 16, 0);
    }
    STAMP(0, 7);
}

constexpr int G1_SLOT = 36864;
constexpr int G1_WPHI = 4 * G1_SLOT;
constexpr int G1_PF = G1_WPHI + 4096;
constexpr int G1_ARR = G1_PF + 256;
constexpr int G1_LDS = G1_ARR + 16;
#define G1_PFIT 3

DI void hedgehog4(const f32x4 (&p)[4][2], bf16x8 (&pos)[4], bf16x8 (&neg)[4]) {
    const float L2E = 1.44269504088896340736f;
    float mx[4], nm[4];
#pragma unroll
    for (int m = 0; m < 4; ++m) {
        mx[m] = fmx(fmx(fmx(fmx(p[m][0][0], p[m][0][1]), p[m][0][2]), fmx(fmx(p[m][0][3], p[m][1][0]), p[m][1][1])), fmx(p[m][1][2], p[m][1][3]));
        nm[m] = fmx(fmx(fmx(fmx(-p[m][0][0], -p[m][0][1]), -p[m][0][2]), fmx(fmx(-p[m][0][3], -p[m][1][0]), -p[m][1][1])), fmx(-p[m][1][2], -p[m][1][3]));
    }
#pragma unroll
    for (int m = 0; m < 4; ++m) red2<true>(mx[m], nm[m]);
    f32x2 ep[4][4], en[4][4];
    float sp[4], sn[4];
#pragma unroll
    for (int m = 0; m < 4; ++m) {
        const float cp = -mx[m] * L2E, cn = -nm[m] * L2E;
#pragma unroll
        for (int i = 0; i < 4; ++i) {
            const f32x2 x = {p[m][i >> 1][2 * (i & 1)], p[m][i >> 1][2 * (i & 1) + 1]};
            const f32x2 a = __builtin_elementwise_fma(x, (f32x2){L2E, L2E}, (f32x2){cp, cp}), b = __builtin_elementwise_fma(x, (f32x2){-L2E, -L2E}, (f32x2){cn, cn});
            ep[m][i] = (f32x2){__builtin_amdgcn_exp2f(a[0]), __builtin_amdgcn_exp2f(a[1])};
            en[m][i] = (f32x2){__builtin_amdgcn_exp2f(b[0]), __builtin_amdgcn_exp2f(b[1])};
        }
        const f32x2 tp = (ep[m][0] + ep[m][1]) + (ep[m][2] + ep[m][3]), tn = (en[m][0] + en[m][1]) + (en[m][2] + en[m][3]);
        sp[m] = tp[0] + tp[1]; sn[m] = tn[0] + tn[1];
    }
#pragma unroll
    for (int m = 0; m < 4; ++m) red2<false>(sp[m], sn[m]);
#pragma unroll
    for (int m = 0; m < 4; ++m) {
        const float rp = __builtin_amdgcn_rcpf(sp[m]), rn = __builtin_amdgcn_rcpf(sn[m]);
        u32x4 a, b;
#pragma unroll
        for (int i = 0; i < 4; ++i) { const f32x2 x = ep[m][i] * rp, y = en[m][i] * rn; a[i] = pk2(x[0], x[1]); b[i] = pk2(y[0], y[1]); }
        pos[m] = __builtin_bit_cast(bf16x8, a); neg[m] = __builtin_bit_cast(bf16x8, b);
    }
}
DI u32x2 glu4(const f32x4& g, const f32x4& u) {
    u32x2 r;
#pragma unroll
    for (int i = 0; i < 2; ++i) {
        const f32x2 t = (f32x2){g[2 * i], g[2 * i + 1]} * -1.44269504088896340736f;
        const f32x2 d = (f32x2){__builtin_amdgcn_exp2f(t[0]), __builtin_amdgcn_exp2f(t[1])} + 1.0f;
        const f32x2 h = (f32x2){u[2 * i], u[2 * i + 1]} * (f32x2){__builtin_amdgcn_rcpf(d[0]), __builtin_amdgcn_rcpf(d[1])};
        r[i] = pk2(h[0], h[1]);
    }
    return r;
}
DI u32x4 glu8(const f32x4& g0, const f32x4& u0, const f32x4& g1, const f32x4& u1) { const u32x2 a = glu4(g0, u0), b = glu4(g1, u1); return (u32x4){a[0], a[1], b[0], b[1]}; }

constexpr int G2_SLOT = 32768;
constexpr int G2_NS = 4;
constexpr int G2_KV = G2_NS * G2_SLOT;
static_assert(G2_KV + 8192 + 256 <= G1_WPHI, "");
#define G2_NA 32
constexpr int G2_NB = 72 - G2_NA, G2_NBH = G2_NB - 16;
static_assert(G2_NA % 4 == 0 && G2_NA >= 32 && G2_NA <= 36, "");
typedef GAS unsigned gu32;
DI void wait_word(gu32* p, unsigned seen, unsigned want) {
    if (__builtin_amdgcn_readfirstlane(seen) < want) {
        unsigned spins = 0;
        while (__builtin_amdgcn_readfirstlane(__hip_atomic_load(p, __ATOMIC_RELAXED, __HIP_MEMORY_SCOPE_AGENT)) < want && ++spins < (1u << 18))
            __builtin_amdgcn_s_sleep(1);
    }
}
DI void publish(LAS unsigned* arr, gu32* word) {
    WAIT_V(0);
    unsigned old = 0;
    if ((threadIdx.x & 63) == 0) old = __hip_atomic_fetch_add(arr, 1u, __ATOMIC_RELAXED, __HIP_MEMORY_SCOPE_WORKGROUP);
    if (__builtin_amdgcn_readfirstlane(old) == 7u && (threadIdx.x & 63) == 0) __hip_atomic_fetch_add(word, 1u, __ATOMIC_RELAXED, __HIP_MEMORY_SCOPE_AGENT);
}

template <bool kvt>
DI void body(const uint16_t* __restrict__ x, const uint16_t* __restrict__ wqkv, const uint16_t* __restrict__ wphi, const uint16_t* __restrict__ wgate,
             const uint16_t* __restrict__ wup, const uint16_t* __restrict__ wdown, const uint16_t* __restrict__ wo, char* __restrict__ ws,
             uint16_t* __restrict__ out, LAS char* lds) {
    const int tid = threadIdx.x, l = tid & 63;
    const int w = __builtin_amdgcn_readfirstlane(tid >> 6);
    const int grp = w >> 2;
    const int wr = w & 3, wc = grp;
    const int bid = blockIdx.x, idx = bid >> 3, xcd = bid & 7;
    const int pm = idx >> 2;
    const int j = xcd * 2 + ((idx >> 1) & 1);
    const int F0 = kvt ? 2048 + 96 * j : 128 * j;
    gu32* cnt = (gu32*)(ws + WS_CNT);

    const char* aptr = ws + WS_XN + (size_t)(pm * 16 + 2 * w) * 1024;
    const unsigned avoff = l * 16;
    const char* bptr[3]; unsigned bstep[3], bvoff[3], bdst[3];
#pragma unroll
    for (int i = 0; i < 3; ++i) {
        int t = grp == 0 ? 3 * w + i : 12 + 2 * (w - 4) + (i < 2 ? i : 0);
        const int u = t >> 1, hf = t & 1, uc = u / 5, r = u % 5;
        const uint16_t* base; int col, stride;
        if (!kvt) {
            if (r == 0) { base = wqkv; col = (2 * j + uc) * 32; stride = 3 * D_; }
            else { base = (r & 1) ? wgate : wup; col = F0 + 64 * uc + (r >= 3 ? 32 : 0); stride = F_; }
        } else {
            if (r < 2) { base = wqkv; col = D_ * (1 + r) + (2 * j + uc) * 32; stride = 3 * D_; }
            else if (r >= 3) { base = (r == 3) ? wgate : wup; col = F0 + 32 * uc; stride = F_; }
            else { base = uc == 0 ? wgate : wup; col = F0 + 64; stride = F_; }
        }
        bptr[i] = (const char*)(base + (size_t)(16 * hf) * stride + col);
        bstep[i] = 64u * stride;
        bvoff[i] = (l >> 2) * stride * 2 + ((((l >> 1) & 1) ^ ((l >> 5) & 1)) * 32) + (l & 1) * 16;
        bdst[i] = (unsigned)(uintptr_t)lds + 16384 + t * 1024;
    }
    const unsigned lbase = (unsigned)(uintptr_t)lds;
    const unsigned adst = lbase + 2 * w * 1024;
#define G1_STAGE(slot) do { \
        dma2(aptr, avoff, adst + (slot) * G1_SLOT); \
        dma1<0>(bptr[0], bvoff[0], bdst[0] + (slot) * G1_SLOT); dma1<0>(bptr[1], bvoff[1], bdst[1] + (slot) * G1_SLOT); \
        if (grp == 0) dma1<0>(bptr[2], bvoff[2], bdst[2] + (slot) * G1_SLOT); \
        aptr += NMB * 1024; bptr[0] += bstep[0]; bptr[1] += bstep[1]; bptr[2] += bstep[2]; \
        OPAQUE_S(aptr); OPAQUE_S(bptr[0]); OPAQUE_S(bptr[1]); OPAQUE_S(bptr[2]); } while (0)
#define G1_WAIT() WAIT_V(8)

    const int G = l >> 4, q4 = (l >> 2) & 3, p4 = l & 3;
    const unsigned lp0 = G * 512 + q4 * 64 + ((G & 1) * 32) + p4 * 8;
    const unsigned lp1 = lp0 ^ 32;
    unsigned rA = lbase + wr * 4096 + l * 16;
    unsigned rB0 = lbase + 16384 + 5 * wc * 2048 + lp0;
    unsigned rB1 = lbase + 16384 + 5 * wc * 2048 + lp1;
    unsigned rB8 = kvt ? lbase + 16384 + 2 * 2048 + (wc ? lp1 : lp0) : rB0 + 2 * 2048;
    unsigned rB9 = kvt ? lbase + 16384 + 7 * 2048 + (wc ? lp1 : lp0) : rB1 + 2 * 2048;
#define G1_MOVE(d) do { rA += (d); rB0 += (d); rB1 += (d); rB8 += (d); rB9 += (d); \
        OPAQUE(rA); OPAQUE(rB0); OPAQUE(rB1); OPAQUE(rB8); OPAQUE(rB9); } while (0)
#define LP(a) ((LAS const char*)(uintptr_t)(a))

    f32x4 acc[4][10];
#pragma unroll
    for (int m = 0; m < 4; ++m)
#pragma unroll
        for (int n = 0; n < 10; ++n) acc[m][n] = (f32x4){0.f, 0.f, 0.f, 0.f};
    bf16x8 fa[4], fb[10];

#define G1_READ(so) do { \
        _Pragma("unroll") for (int m = 0; m < 4; ++m) fa[m] = *(LAS const bf16x8*)(LP(rA) + (so) + m * 1024); \
        _Pragma("unroll") for (int u = 0; u < 5; ++u) if (u != 2) { \
            fb[2 * u] = tr_frag(LP(rB0) + (so) + u * 2048, LP(rB0) + (so) + u * 2048 + 256); \
            fb[2 * u + 1] = tr_frag(LP(rB1) + (so) + u * 2048, LP(rB1) + (so) + u * 2048 + 256); } \
        fb[4] = tr_frag(LP(rB8) + (so), LP(rB8) + (so) + 256); \
        fb[5] = tr_frag(LP(rB9) + (so), LP(rB9) + (so) + 256); } while (0)
#define G1_MMA() do { __builtin_amdgcn_s_setprio(1); \
        _Pragma("unroll") for (int m = 0; m < 4; ++m) _Pragma("unroll") for (int n_ = 0; n_ < 10; ++n_) { const int n = (m & 1) ? 9 - n_ : n_; MFMA16_ACC(acc[m][n], fb[n], fa[m]); } \
        __builtin_amdgcn_s_setprio(0); } while (0)
#define G1_PHASE(so, STG, VW) do { G1_READ(so); STG; WAIT_L0(); VW; SCHED(); BAR(); SCHED(); G1_MMA(); SCHED(); BAR(); SCHED(); } while (0)

    STAMP(1, 0);
    if (tid < 2) *(LAS unsigned*)(lds + G1_ARR + tid * 4) = 0u;
    if (grp == 0) dma1<0>((const char*)wphi + (size_t)(2 * j) * 2048 + w * 1024, avoff, lbase + G1_WPHI + w * 1024);
    G1_STAGE(0); G1_STAGE(1);
    WAIT_V(4); WAIT_L0();
    BAR();
    G1_STAGE(2);
    STAMP(1, 1);
    if (grp == 1) BAR();
    G1_MOVE(0);
    SCHED();
    for (int it = 0; it < 7; ++it) {
        if (w == 0) STAMP(4, it);
        if (it == G1_PFIT && w >= 3) {
            const int u = bid * 5 + w - 3;
            if (u < 1152) pf_lines(u < 896 ? (const char*)wdown + (size_t)u * 8192 : (const char*)wo + (size_t)(u - 896) * 8192, avoff * 8, lbase + G1_PF, ~0ull);
        }
        G1_PHASE(0, G1_STAGE(3), G1_WAIT());
        G1_PHASE(G1_SLOT, G1_STAGE(0), G1_WAIT());
        G1_MOVE(2 * G1_SLOT);
        G1_PHASE(0, G1_STAGE(1), G1_WAIT());
        G1_PHASE(G1_SLOT, G1_STAGE(2), G1_WAIT());
        G1_MOVE(-2 * G1_SLOT);
    }
    if (w == 0) STAMP(4, 7);
    G1_PHASE(0, G1_STAGE(3), G1_WAIT());
    G1_PHASE(G1_SLOT, (void)0, WAIT_V(4));
    G1_MOVE(2 * G1_SLOT);
    G1_PHASE(0, (void)0, WAIT_V(0));
    G1_READ(G1_SLOT); WAIT_L0(); SCHED(); BAR(); SCHED(); G1_MMA(); SCHED();
    if (grp == 0) BAR();
    asm volatile("s_nop 7\n\ts_nop 7" ::: "memory");
    SCHED();
    STAMP(1, 2);

    constexpr bool tb = !kvt;
    const int t16 = idx >> 1;
    const int pm2 = (xcd >> 1) * 4 + (t16 & 3), pn = (xcd & 1) * 4 + (t16 >> 2);
    const int tile = pm2 * 8 + pn;
    constexpr int k0 = tb ? 0 : G2_NBH;
    const unsigned lo16 = l * 16;
    const bool isb = wr >= 2;
    const char* gptr = isb ? (const char*)(wdown + (size_t)(64 * k0 + 32 * (wr - 2)) * D_ + pn * 128) : ws + WS_A2 + ((size_t)(2 * k0 + wr) * NMB + pm2 * 8) * 1024;
    const char* gptr_o = isb ? (const char*)(wo + (size_t)(32 * (wr & 1)) * D_ + pn * 128) : ws + WS_A2 + ((size_t)(112 + wr) * NMB + pm2 * 8) * 1024;
    const unsigned gstep = isb ? 64 * D_ * 2 : 2 * NMB * 1024;
    unsigned voff[4];
#pragma unroll
    for (int i = 0; i < 4; ++i) {
        const int r = 4 * i + G;
        voff[i] = isb ? r * (D_ * 2) + ((((l & 15) >> 1) ^ (r & 7)) * 32) + (l & 1) * 16 - 1024 * i : lo16;
    }
    const unsigned ghalf = isb ? 16 * D_ * 2 : 4096;
    unsigned dst = lbase + wr * 8192;
    int tld = 0, slot = 0;
#define G2_DMA4_(g, d, SC) asm volatile("s_mov_b32 m0, %5\n\ts_nop 0\n\tglobal_load_lds_dwordx4 %0, %4" SC "\n\tglobal_load_lds_dwordx4 %1, %4 offset:1024" SC "\n\t" \
            "global_load_lds_dwordx4 %2, %4 offset:2048" SC "\n\tglobal_load_lds_dwordx4 %3, %4 offset:3072" SC \
            :: "v"(voff[0]), "v"(voff[1]), "v"(voff[2]), "v"(voff[3]), "s"(g), "s"(d) : "memory")
#define G2_DMA4(g, d) G2_DMA4_(g, d, " sc1")
#define G2_NEXTSLOT() do { ++slot; const int wrap = slot == G2_NS; slot = wrap ? 0 : slot; dst += wrap ? (unsigned)(-(G2_NS - 1) * G2_SLOT) : (unsigned)G2_SLOT; } while (0)
#define G2_STAGE() do { \
        if (!isb) { G2_DMA4(gptr, dst); G2_DMA4(gptr + ghalf, dst + 4096); } else { G2_DMA4_(gptr, dst, ""); G2_DMA4_(gptr + ghalf, dst + 4096, ""); } \
        ++tld; gptr = (tb && tld == G2_NBH) ? gptr_o : gptr + gstep; G2_NEXTSLOT(); } while (0)
#define G2_PRUN(n) do { _Pragma("unroll 1") for (int t_ = 0; t_ < (n); ++t_) { WAIT_V(16); BAR(); G2_STAGE(); } } while (0)
#define POLL(dst_, p_) asm volatile("global_load_dword %0, %1, off sc1" : "=v"(dst_) : "v"(p_) : "memory")
    const size_t off0 = (size_t)(pm2 * 128 + (wr >> 1) * 64 + (l & 15)) * D_ + pn * 128 + (wr & 1) * 64 + (G & 1) * 16 + (G >> 1) * 8;

    const int fq = l >> 4, fr = l & 15;
    const int hh = 2 * j + wc;
    const int mb0 = pm * 16 + wr * 4;
    const rsrc_t rw = mkrsrc(ws, (unsigned)WS_END);
    if (tb && w >= 6) { G2_STAGE(); G2_STAGE(); }
    bf16x8 wp[2];
#pragma unroll
    for (int c = 0; c < 2; ++c) {
        LAS const char* p = lds + G1_WPHI + wc * 2048 + (4 * G + q4) * 64 + c * 32 + p4 * 8;
        wp[c] = tr_frag(p, p + 1024);
    }
    const f32x4 z4 = {0.f, 0.f, 0.f, 0.f};
    f32x4 pf[4][2];
#pragma unroll
    for (int m = 0; m < 4; ++m) {
        const bf16x8 qf = pack8(acc[m][0], acc[m][1]);
        pf[m][0] = MFMA16(wp[0], qf, z4); pf[m][1] = MFMA16(wp[1], qf, z4);
    }
    const unsigned hb = (unsigned)WS_A2 + (((kvt ? F0 + 32 * wc : F0 + 64 * wc + 32) >> 5) * NMB + mb0) * 1024;
    bf16x8 pos[4], neg[4];
    u32x4 xv[4][2];
    if constexpr (!kvt) {
        __builtin_amdgcn_s_setprio(1);
#pragma unroll
        for (int m = 0; m < 4; ++m) {
            stw(glu8(acc[m][2], acc[m][4], acc[m][3], acc[m][5]), rw, lo16 + m * 1024, hb - NMB * 1024);
            stw(glu8(acc[m][6], acc[m][8], acc[m][7], acc[m][9]), rw, lo16 + m * 1024, hb);
        }
        SCHED();
        publish((LAS unsigned*)(lds + G1_ARR), cnt + 64 * (C_HQ + pm));
        __builtin_amdgcn_s_setprio(0);
        SCHED();
        STAMP(1, 5);
        hedgehog4(pf, pos, neg);
    } else {
        u32x4 i0 = {0u, 0u, 0u, 0u}, i1 = {0u, 0u, 0u, 0u};
        {
            const int d = fr - 4 * fq;
            const unsigned lo = (d == 0) ? 0x3f80u : (d == 1) ? 0x3f800000u : 0u;
            const unsigned hi = (d == 2) ? 0x3f80u : (d == 3) ? 0x3f800000u : 0u;
            i0[0] = lo; i0[1] = hi; i1[2] = lo; i1[3] = hi;
        }
        const bf16x8 I0 = __builtin_bit_cast(bf16x8, i0), I1 = __builtin_bit_cast(bf16x8, i1);
        constexpr int ZRED = 65536, HX = ZRED + 2048;
        bf16x8 V[2][2];
        {
            f32x4 TV[4][2];
#pragma unroll
            for (int m = 0; m < 4; ++m) {
                const bf16x8 vf = pack8(acc[m][2], acc[m][3]);
                TV[m][0] = MFMA16(vf, I0, z4); TV[m][1] = MFMA16(vf, I1, z4);
            }
#pragma unroll
            for (int m = 0; m < 4; ++m) *(LAS u32x2*)(lds + HX + (wr * 4 + m) * 1024 + lo16 + 8 * wc) = glu4(acc[m][4], acc[m][5]);
#pragma unroll
            for (int mp = 0; mp < 2; ++mp) { V[mp][0] = pack8(TV[2 * mp][0], TV[2 * mp + 1][0]); V[mp][1] = pack8(TV[2 * mp][1], TV[2 * mp + 1][1]); }
        }
        SCHED();
        hedgehog4(pf, pos, neg);
        const u32x4 o4 = {0x3f803f80u, 0x3f803f80u, 0x3f803f80u, 0x3f803f80u};
        const bf16x8 ONES = __builtin_bit_cast(bf16x8, o4);
        f32x4 T[4][2][2];
#pragma unroll
        for (int m = 0; m < 4; ++m) {
            T[m][0][0] = MFMA16(pos[m], I0, z4); T[m][0][1] = MFMA16(pos[m], I1, z4);
            T[m][1][0] = MFMA16(neg[m], I0, z4); T[m][1][1] = MFMA16(neg[m], I1, z4);
        }
        LAS char* red = lds + (wc * 4 + wr) * 8192 + lo16;
#pragma unroll
        for (int s = 0; s < 2; ++s)
#pragma unroll
            for (int cc = 0; cc < 2; ++cc) {
                const bf16x8 P0 = pack8(T[0][s][cc], T[1][s][cc]), P1 = pack8(T[2][s][cc], T[3][s][cc]);
                const f32x4 k0_ = MFMA16(P1, V[1][0], MFMA16(P0, V[0][0], z4));
                const f32x4 k1_ = MFMA16(P1, V[1][1], MFMA16(P0, V[0][1], z4));
                const f32x4 zz = MFMA16(P1, ONES, MFMA16(P0, ONES, z4));
                *(LAS f32x4*)(red + ((s * 2 + 0) * 2 + cc) * 1024) = k0_;
                *(LAS f32x4*)(red + ((s * 2 + 1) * 2 + cc) * 1024) = k1_;
                *(LAS f32x4*)(lds + ZRED + (wc * 4 + wr) * 256 + (s * 32 + cc * 16 + 4 * fq) * 4) = zz;
            }
        WAIT_L0(); SCHED(); BAR(); SCHED();
        const unsigned kvo = (unsigned)WS_KVP + (pm * 32 + hh) * KVP_SZ;
        u32x4 hx[2];
#pragma unroll
        for (int i = 0; i < 2; ++i) {
            LAS const char* p = lds + wc * 32768 + (2 * wr + i) * 1024 + lo16;
            const f32x4 sum = (*(LAS const f32x4*)p + *(LAS const f32x4*)(p + 8192)) + (*(LAS const f32x4*)(p + 16384) + *(LAS const f32x4*)(p + 24576));
            stw(sum, rw, lo16 + i * 1024, kvo + 2 * wr * 1024);
            hx[i] = *(LAS const u32x4*)(lds + HX + (wr * 4 + 2 * wc + i) * 1024 + lo16);
        }
        {
            LAS const char* p = lds + ZRED + wc * 1024 + (l & 15) * 16;
            const f32x4 sum = (*(LAS const f32x4*)p + *(LAS const f32x4*)(p + 256)) + (*(LAS const f32x4*)(p + 512) + *(LAS const f32x4*)(p + 768));
            stw(sum, rw, (wr == 0 && l < 16) ? lo16 : 0x80000000u, kvo + 8192);
        }
        WAIT_L0(); SCHED();
        publish((LAS unsigned*)(lds + G1_ARR), cnt + 64 * (C_KV + j));
        BAR(); SCHED();
        STAMP(1, 5);
        if (w >= 4) {
            if (!isb) wait_word(cnt + 64 * (C_HQ + (pm2 >> 1)), 0u, 16u);
            G2_STAGE(); G2_STAGE();
        } else {
#pragma unroll
            for (int m = 0; m < 4; ++m) { xv[m][0] = *(const u32x4*)(x + off0 + m * 16 * D_); xv[m][1] = *(const u32x4*)(x + off0 + m * 16 * D_ + 32); }
        }
#pragma unroll
        for (int i = 0; i < 2; ++i) stw(hx[i], rw, lo16 + i * 1024, (unsigned)WS_A2 + (((F0 + 64) >> 5) * NMB + mb0 + 2 * wc) * 1024);
#pragma unroll
        for (int m = 0; m < 4; ++m) stw(glu8(acc[m][6], acc[m][8], acc[m][7], acc[m][9]), rw, lo16 + m * 1024, hb);
        SCHED();
        publish((LAS unsigned*)(lds + G1_ARR + 4), cnt + 64 * (C_HK + pm));
    }
    SCHED();
    STAMP(1, 6);

#define G2_OCOMP() do { \
        bf16x8 kf[2][2], zf[2]; \
        _Pragma("unroll") for (int s = 0; s < 2; ++s) { \
            kf[s][0] = *(LAS const bf16x8*)(lds + G2_KV + wc * 4096 + (s * 2) * 1024 + lo16); kf[s][1] = *(LAS const bf16x8*)(lds + G2_KV + wc * 4096 + (s * 2 + 1) * 1024 + lo16); \
            const u32x2 za = *(LAS const u32x2*)(lds + G2_KV + 8192 + wc * 128 + s * 64 + G * 8), zb = *(LAS const u32x2*)(lds + G2_KV + 8192 + wc * 128 + s * 64 + 32 + G * 8); \
            zf[s] = __builtin_bit_cast(bf16x8, (u32x4){za[0], za[1], zb[0], zb[1]}); } \
        _Pragma("unroll") for (int m = 0; m < 4; ++m) { \
            const f32x4 dn = MFMA16(zf[1], neg[m], MFMA16(zf[0], pos[m], z4)); \
            const f32x4 n0 = MFMA16(kf[1][0], neg[m], MFMA16(kf[0][0], pos[m], z4)), n1 = MFMA16(kf[1][1], neg[m], MFMA16(kf[0][1], pos[m], z4)); \
            const float rd = __builtin_amdgcn_rcpf(dn[0] + 1e-6f); \
            stw(pack8(n0 * rd, n1 * rd), rw, lo16 + m * 1024, (unsigned)WS_A2 + ((112 + hh) * NMB + mb0) * 1024); } \
        WAIT_L0(); SCHED(); } while (0)

    if (w >= 4) {
        unsigned seen;
        if (tb && !isb) { wait_word(cnt + 64 * (C_HQ + (pm2 >> 1)), 0u, 16u); STAMP(6, 0); G2_STAGE(); G2_STAGE(); }
        WAIT_V(8);
        STAMP(5 + tb, 1);
        BAR(); G2_STAGE();
        WAIT_V(8); BAR(); G2_STAGE(); G2_STAGE();
        if constexpr (!tb) {
            constexpr int UK = 32 - G2_NBH - 4;
            G2_PRUN(UK - 3);
            POLL(seen, cnt + 64 * (C_HK + (pm2 >> 1)));
            G2_PRUN(2);
            asm volatile("s_waitcnt vmcnt(16)" : "+v"(seen) :: "memory");
            if (!isb) wait_word(cnt + 64 * (C_HK + (pm2 >> 1)), seen, 16u);
            BAR(); G2_STAGE();
            G2_PRUN(G2_NA - 5 - UK);
            WAIT_V(16); BAR(); WAIT_V(8); BAR(); WAIT_V(0); BAR(); BAR();
            BAR();
        } else {
            G2_PRUN(1);
            POLL(seen, cnt + 64 * (C_KV + j));
            G2_PRUN(2);
            asm volatile("s_waitcnt vmcnt(16)" : "+v"(seen) :: "memory");
            wait_word(cnt + 64 * (C_KV + j), seen, 8u);
            f32x4 t[4][8]; float zt[8];
            const char* kvs = ws + WS_KVP + (size_t)(2 * j + (wr >> 1)) * KVP_SZ;
            const unsigned kvv = (wr & 1) * 4096 + lo16, zv = 8192 + l * 4;
#define G2_KVLD(i) do { _Pragma("unroll") for (int k = 0; k < 8; ++k) \
                asm volatile("global_load_dwordx4 %0, %1, %2 offset:%3 sc1" : "=v"(t[i][k]) : "v"(kvv), "s"(kvs + (size_t)k * 32 * KVP_SZ), "i"((i) * 1024) : "memory"); \
            _Pragma("unroll") for (int k = 2 * (i); k < 2 * (i) + 2; ++k) \
                asm volatile("global_load_dword %0, %1, %2 sc1" : "=v"(zt[k]) : "v"(zv), "s"(kvs + (size_t)k * 32 * KVP_SZ) : "memory"); } while (0)
            BAR(); G2_STAGE(); G2_KVLD(0);
            WAIT_V(26); BAR(); G2_STAGE(); G2_KVLD(1);
            WAIT_V(36); BAR(); G2_STAGE(); G2_KVLD(2);
            WAIT_V(46); BAR(); G2_STAGE(); G2_KVLD(3);
            WAIT_V(46); BAR(); G2_STAGE();
            WAIT_V(36); BAR(); G2_STAGE();
            WAIT_V(26); BAR(); G2_STAGE();
            WAIT_V(16); SCHED();
#pragma unroll
            for (int i = 0; i < 4; ++i) {
                asm volatile("" : "+v"(t[i][0]), "+v"(t[i][1]), "+v"(t[i][2]), "+v"(t[i][3]), "+v"(t[i][4]), "+v"(t[i][5]), "+v"(t[i][6]), "+v"(t[i][7]), "+v"(zt[2 * i]), "+v"(zt[2 * i + 1]));
                const f32x4 sum = ((t[i][0] + t[i][1]) + (t[i][2] + t[i][3])) + ((t[i][4] + t[i][5]) + (t[i][6] + t[i][7]));
                const int id = 4 * (wr & 1) + i;
                *(LAS u32x2*)(lds + G2_KV + (wr >> 1) * 4096 + (id >> 1) * 1024 + lo16 + (id & 1) * 8) = (u32x2){pk2(sum[0], sum[1]), pk2(sum[2], sum[3])};
            }
            const float z = ((zt[0] + zt[1]) + (zt[2] + zt[3])) + ((zt[4] + zt[5]) + (zt[6] + zt[7]));
            *(LAS unsigned short*)(lds + G2_KV + 8192 + (wr >> 1) * 128 + l * 2) = (unsigned short)pk2(z, z);
            WAIT_L0(); SCHED();
            BAR(); G2_STAGE();
            G2_OCOMP();
            G2_PRUN(G2_NB - 34);
            gu32* f0 = cnt + 64 * (C_O + (pm2 >> 1));
            POLL(seen, f0);
            G2_PRUN(2);
            asm volatile("s_waitcnt vmcnt(16)" : "+v"(seen) :: "memory");
            STAMP(6, 2);
            if (!isb) wait_word(f0, seen, 16u);
            STAMP(6, 3);
            BAR(); G2_STAGE();
            G2_PRUN(13);
            gu32* f1 = cnt + 64 * (C_P + tile);
            POLL(seen, f1);
            G2_PRUN(2);
            asm volatile("s_waitcnt vmcnt(16)" : "+v"(seen) :: "memory");
            STAMP(6, 4);
            wait_word(f1, seen, 1u);
            STAMP(6, 5);
            char* part = ws + WS_PART + (size_t)tile * 65536;
#pragma unroll
            for (int i = 0; i < 4; ++i) voff[i] = lo16;
            BAR(); G2_DMA4(part + wr * 8192, dst); G2_DMA4(part + wr * 8192 + 4096, dst + 4096); G2_NEXTSLOT();
            WAIT_V(16); BAR(); G2_DMA4(part + 32768 + wr * 8192, dst); G2_DMA4(part + 32768 + wr * 8192 + 4096, dst + 4096);
            WAIT_V(16);
            BAR(); BAR();
            WAIT_V(0);
            BAR();
        }
        STAMP(3, 7);
        return;
    }
    const int cr = w >> 1, cc2 = w & 1;
    f32x4 ac2[4][4];
#pragma unroll
    for (int m = 0; m < 4; ++m)
#pragma unroll
        for (int i = 0; i < 2; ++i) {
            if constexpr (tb) { ac2[m][2 * i] = z4; ac2[m][2 * i + 1] = z4; }
            else {
                unsigned d0 = xv[m][i][0], d1 = xv[m][i][1], d2 = xv[m][i][2], d3 = xv[m][i][3];
                swap16u(d0, d2); swap16u(d1, d3);
                ac2[m][2 * i] = (f32x4){bflo(d0), bfhi(d0), bflo(d1), bfhi(d1)}; ac2[m][2 * i + 1] = (f32x4){bflo(d2), bfhi(d2), bflo(d3), bfhi(d3)};
            }
        }
    const int r8 = 4 * (G & 1) + q4;
    unsigned sA = lbase + cr * 4096 + lo16;
    unsigned sB[4];
#pragma unroll
    for (int n = 0; n < 4; ++n) sB[n] = lbase + 16384 + (4 * G + q4) * 256 + (((4 * cc2 + n) ^ r8) * 32) + p4 * 8;
#define G2_MOVE(d) do { sA += (d); sB[0] += (d); sB[1] += (d); sB[2] += (d); sB[3] += (d); OPAQUE(sA); OPAQUE(sB[0]); OPAQUE(sB[1]); OPAQUE(sB[2]); OPAQUE(sB[3]); } while (0)
    bf16x8 ga[2][2][4], gb[2][2][4];
#define G2_RD_A(buf, so, kk, m) ga[buf][kk][m] = *(LAS const bf16x8*)(LP(sA) + (so) + (kk) * 8192 + (m) * 1024)
#define G2_RD_B(buf, so, kk, n) gb[buf][kk][n] = tr_frag(LP(sB[n]) + (so) + (kk) * 8192, LP(sB[n]) + (so) + (kk) * 8192 + 4096)
#define G2_READ(buf, so) do { _Pragma("unroll") for (int kk = 0; kk < 2; ++kk) _Pragma("unroll") for (int i = 0; i < 4; ++i) { G2_RD_B(buf, so, kk, i); G2_RD_A(buf, so, kk, i); } } while (0)
#define G2_STEP(cur, so, RD) do { BAR(); SCHED(); \
        _Pragma("unroll") for (int kk = 0; kk < 2; ++kk) _Pragma("unroll") for (int m = 0; m < 4; ++m) _Pragma("unroll") for (int n_ = 0; n_ < 4; ++n_) { \
            const int n = (m & 1) ? 3 - n_ : n_; \
            MFMA16_ACC(ac2[m][n], gb[cur][kk][n], ga[cur][kk][m]); \
            if ((RD) && kk == 0) { const int i_ = m * 4 + n_; if (i_ & 1) G2_RD_A((cur) ^ 1, so, i_ >> 3, (i_ >> 1) & 3); else G2_RD_B((cur) ^ 1, so, i_ >> 3, (i_ >> 1) & 3); } \
            SCHED(); } \
        WAIT_L0(); SCHED(); } while (0)
    unsigned mv = 2 * G2_SLOT;
#define G2_RUN(n) do { _Pragma("unroll 1") for (int i_ = 0; i_ < (n); ++i_) { G2_STEP(0, G2_SLOT, 1); G2_MOVE(mv); mv = -mv; G2_STEP(1, 0, 1); } } while (0)

    G2_MOVE(0);
    BAR(); SCHED();
    G2_READ(0, 0);
    WAIT_L0(); SCHED();
    STAMP(3, 1);
    if constexpr (tb) {
        G2_RUN(6);
        G2_OCOMP();
        G2_RUN(1);
        WAIT_V(0); SCHED();
        G2_RUN(1);
        if (tid == 0) __hip_atomic_fetch_add(cnt + 64 * (C_O + pm), 1u, __ATOMIC_RELAXED, __HIP_MEMORY_SCOPE_AGENT);
        SCHED();
        G2_RUN(G2_NB / 2 - 9);
    } else {
        G2_RUN(G2_NA / 2 - 1);
    }
    G2_STEP(0, G2_SLOT, 1);
    G2_STEP(1, 0, 0);
    asm volatile("s_nop 7\n\ts_nop 7" ::: "memory");
    SCHED();
    if constexpr (!tb) {
        STAMP(3, 2);
#pragma unroll
        for (int m = 0; m < 4; ++m)
#pragma unroll
            for (int n = 0; n < 4; ++n) stw(ac2[m][n], rw, lo16 + n * 1024, (unsigned)WS_PART + tile * 65536 + w * 16384 + m * 4096);
        WAIT_V(0); BAR();
        if (tid == 0) __hip_atomic_store(cnt + 64 * (C_P + tile), 1u, __ATOMIC_RELAXED, __HIP_MEMORY_SCOPE_AGENT);
        STAMP(3, 6);
    } else {
        STAMP(3, 4);
        BAR(); SCHED();
#pragma unroll
        for (int m = 0; m < 4; ++m)
#pragma unroll
            for (int i = 0; i < 2; ++i) {
                const f32x4 r0 = ac2[m][2 * i] + *(LAS const f32x4*)(lds + w * 16384 + (m * 4 + 2 * i) * 1024 + lo16);
                const f32x4 r1 = ac2[m][2 * i + 1] + *(LAS const f32x4*)(lds + w * 16384 + (m * 4 + 2 * i + 1) * 1024 + lo16);
                unsigned d0 = pk2(r0[0], r0[1]), d1 = pk2(r0[2], r0[3]), d2 = pk2(r1[0], r1[1]), d3 = pk2(r1[2], r1[3]);
                swap16u(d0, d2); swap16u(d1, d3);
                *(u32x4*)(out + off0 + m * 16 * D_ + i * 32) = (u32x4){d0, d1, d2, d3};
            }
        STAMP(3, 5);
    }
}

__global__ __launch_bounds__(512, 2) void k_main(const uint16_t* __restrict__ x, const uint16_t* __restrict__ wqkv, const uint16_t* __restrict__ wphi,
                                                const uint16_t* __restrict__ wgate, const uint16_t* __restrict__ wup, const uint16_t* __restrict__ wdown,
                                                const uint16_t* __restrict__ wo, char* __restrict__ ws, uint16_t* __restrict__ out) {
    __shared__ __attribute__((aligned(16))) char smem_[G1_LDS];
    if (blockIdx.x & 8) body<true>(x, wqkv, wphi, wgate, wup, wdown, wo, ws, out, (LAS char*)smem_);
    else body<false>(x, wqkv, wphi, wgate, wup, wdown, wo, ws, out, (LAS char*)smem_);
}

extern "C" void kernel_launch(void* const* d_in, const int* in_sizes, int n_in, void* d_out, int out_size, void* d_ws, size_t ws_size,
                              hipStream_t stream) {
    const uint16_t* x = (const uint16_t*)d_in[0];
    const uint16_t* wqkv = (const uint16_t*)d_in[1];
    const uint16_t* wphi = (const uint16_t*)d_in[2];
    const uint16_t* wo = (const uint16_t*)d_in[3];
    const uint16_t* wgate = (const uint16_t*)d_in[5];
    const uint16_t* wup = (const uint16_t*)d_in[6];
    const uint16_t* wdown = (const uint16_t*)d_in[7];
    char* ws = (char*)d_ws;
    k_ln<<<256, 256, 0, stream>>>(x, ws);
    k_main<<<256, 512, 0, stream>>>(x, wqkv, wphi, wgate, wup, wdown, wo, ws, (uint16_t*)d_out);
}
```

```cpp
#include <hip/hip_runtime.h>
#include <hip/hip_bf16.h>
#include <stdint.h>

#define LAS __attribute__((address_space(3)))
#define GAS __attribute__((address_space(1)))
#define DI __device__ __forceinline__
typedef __bf16 bf16x8 __attribute__((ext_vector_type(8)));
typedef __bf16 bf16x2 __attribute__((ext_vector_type(2)));
typedef short s16x4 __attribute__((ext_vector_type(4)));
typedef short s16x8 __attribute__((ext_vector_type(8)));
typedef float f32x4 __attribute__((ext_vector_type(4)));
typedef float f32x2 __attribute__((ext_vector_type(2)));
typedef unsigned u32x4 __attribute__((ext_vector_type(4)));
typedef unsigned u32x2 __attribute__((ext_vector_type(2)));

constexpr int S_ = 2048, D_ = 1024, F_ = 3584, H_ = 32;
constexpr int NMB = S_ / 16;
constexpr size_t WS_XN = 0;
constexpr size_t WS_A2 = WS_XN + (size_t)32 * NMB * 1024;
constexpr size_t WS_KVP = WS_A2 + (size_t)144 * NMB * 1024;
constexpr int KVP_SZ = 8448;
constexpr size_t WS_PART = WS_KVP + (size_t)8 * 32 * KVP_SZ;
constexpr size_t WS_CNT = WS_PART + (size_t)128 * 65536;
constexpr int C_LN = 0, C_HQ = 64, C_HK = 96, C_O = 128, C_KV = 160, C_P = 224, C_E = 352, C_N = 353;
constexpr size_t WS_END = WS_CNT + C_N * 256;

#define STAMP(kid, i) do {} while (0)
#define MFMA16(a, b, c) __builtin_amdgcn_mfma_f32_16x16x32_bf16((a), (b), (c), 0, 0, 0)
#define WAIT_V(n) asm volatile("s_waitcnt vmcnt(" #n ")" ::: "memory")
#define WAIT_L(n) asm volatile("s_waitcnt lgkmcnt(" #n ")" ::: "memory")
#define WAIT_L0() __builtin_amdgcn_s_waitcnt(0xC07F)
#define MFMA16_ACC(acc, a, b) asm volatile("v_mfma_f32_16x16x32_bf16 %0, %1, %2, %0" : "+v"(acc) : "v"(a), "v"(b))
#define BAR() __builtin_amdgcn_s_barrier()
#define SCHED() __builtin_amdgcn_sched_barrier(0)

DI unsigned pk2(float lo, float hi) {
    f32x2 v = {lo, hi};
    return __builtin_bit_cast(unsigned, __builtin_convertvector(v, bf16x2));
}
DI float bflo(unsigned u) { return __uint_as_float(u << 16); }
DI float bfhi(unsigned u) { return __uint_as_float(u & 0xffff0000u); }
DI float bfr(float x) { return bflo(pk2(x, x)); }
DI bf16x8 pack8(const f32x4& a, const f32x4& b) {
    u32x4 r = {pk2(a[0], a[1]), pk2(a[2], a[3]), pk2(b[0], b[1]), pk2(b[2], b[3])};
    return __builtin_bit_cast(bf16x8, r);
}
DI bf16x8 tr_frag(LAS const char* p0, LAS const char* p1) {
    s16x4 a = __builtin_amdgcn_ds_read_tr16_b64_v4i16((LAS s16x4*)p0);
    s16x4 b = __builtin_amdgcn_ds_read_tr16_b64_v4i16((LAS s16x4*)p1);
    s16x8 c = __builtin_shufflevector(a, b, 0, 1, 2, 3, 4, 5, 6, 7);
    return __builtin_bit_cast(bf16x8, c);
}
template <int OFF> DI void dma1(const char* g, unsigned voff, unsigned ldsdst) {
    asm volatile("s_mov_b32 m0, %2\n\ts_nop 0\n\tglobal_load_lds_dwordx4 %0, %1 offset:%3" :: "v"(voff), "s"(g), "s"(ldsdst), "i"(OFF) : "memory");
}
DI void dma2(const char* g, unsigned voff, unsigned ldsdst) {
    asm volatile("s_mov_b32 m0, %2\n\ts_nop 0\n\tglobal_load_lds_dwordx4 %0, %1 sc1\n\tglobal_load_lds_dwordx4 %0, %1 offset:1024 sc1"
                 :: "v"(voff), "s"(g), "s"(ldsdst) : "memory");
}
DI void dma4(const char* g, unsigned voff, unsigned ldsdst) {
    asm volatile("s_mov_b32 m0, %2\n\ts_nop 0\n\tglobal_load_lds_dwordx4 %0, %1\n\tglobal_load_lds_dwordx4 %0, %1 offset:1024\n\t"
                 "global_load_lds_dwordx4 %0, %1 offset:2048\n\tglobal_load_lds_dwordx4 %0, %1 offset:3072"
                 :: "v"(voff), "s"(g), "s"(ldsdst) : "memory");
}
DI void pf_lines(const char* g, unsigned voff, unsigned ldspad, unsigned long long mask) {
    asm volatile("s_mov_b32 m0, %2\n\ts_mov_b64 exec, %3\n\tglobal_load_lds_dword %0, %1\n\ts_mov_b64 exec, -1" :: "v"(voff), "s"(g), "s"(ldspad), "s"(mask) : "memory");
}
typedef __amdgpu_buffer_rsrc_t rsrc_t;
DI rsrc_t mkrsrc(const void* p, unsigned bytes) { return __builtin_amdgcn_make_buffer_rsrc((void*)p, 0, bytes, 0x00020000); }
template <class T> DI void stw(const T& v, rsrc_t r, unsigned voff, unsigned soff) { __builtin_amdgcn_raw_buffer_store_b128(__builtin_bit_cast(u32x4, v), r, voff + soff, 0, 16); }
#define OPAQUE(x) asm volatile("" : "+v"(x))
#define OPAQUE_S(x) asm volatile("" : "+s"(x))
DI float fmx(float a, float b) { return __builtin_elementwise_maximum(a, b); }
DI void swap16(float& a, float& b) { auto r = __builtin_amdgcn_permlane16_swap(__float_as_uint(a), __float_as_uint(b), false, false); a = __uint_as_float(r[0]); b = __uint_as_float(r[1]); }
DI void swap32(float& a, float& b) { auto r = __builtin_amdgcn_permlane32_swap(__float_as_uint(a), __float_as_uint(b), false, false); a = __uint_as_float(r[0]); b = __uint_as_float(r[1]); }
DI void swap16u(unsigned& a, unsigned& b) { auto r = __builtin_amdgcn_permlane16_swap(a, b, false, false); a = r[0]; b = r[1]; }
template <bool MAX> DI void red2(float& a, float& b) {
    swap16(a, b); float v = MAX ? fmx(a, b) : a + b;
    float u = v; swap32(v, u); v = MAX ? fmx(v, u) : v + u;
    a = v; b = v; swap16(a, b);
}

constexpr int G1_SLOT = 36864;
constexpr int G1_WPHI = 4 * G1_SLOT;
constexpr int G1_PF = G1_WPHI + 4096;
constexpr int G1_ARR = G1_PF + 256;
constexpr int G1_LDS = G1_ARR + 16;
DI void hedgehog4(const f32x4 (&p)[4][2], bf16x8 (&pos)[4], bf16x8 (&neg)[4]) {
    const float L2E = 1.44269504088896340736f;
    float mx[4], nm[4];
#pragma unroll
    for (int m = 0; m < 4; ++m) {
        mx[m] = fmx(fmx(fmx(fmx(p[m][0][0], p[m][0][1]), p[m][0][2]), fmx(fmx(p[m][0][3], p[m][1][0]), p[m][1][1])), fmx(p[m][1][2], p[m][1][3]));
        nm[m] = fmx(fmx(fmx(fmx(-p[m][0][0], -p[m][0][1]), -p[m][0][2]), fmx(fmx(-p[m][0][3], -p[m][1][0]), -p[m][1][1])), fmx(-p[m][1][2], -p[m][1][3]));
    }
#pragma unroll
    for (int m = 0; m < 4; ++m) red2<true>(mx[m], nm[m]);
    f32x2 ep[4][4], en[4][4];
    float sp[4], sn[4];
#pragma unroll
    for (int m = 0; m < 4; ++m) {
        const float cp = -mx[m] * L2E, cn = -nm[m] * L2E;
#pragma unroll
        for (int i = 0; i < 4; ++i) {
            const f32x2 x = {p[m][i >> 1][2 * (i & 1)], p[m][i >> 1][2 * (i & 1) + 1]};
            const f32x2 a = __builtin_elementwise_fma(x, (f32x2){L2E, L2E}, (f32x2){cp, cp}), b = __builtin_elementwise_fma(x, (f32x2){-L2E, -L2E}, (f32x2){cn, cn});
            ep[m][i] = (f32x2){__builtin_amdgcn_exp2f(a[0]), __builtin_amdgcn_exp2f(a[1])};
            en[m][i] = (f32x2){__builtin_amdgcn_exp2f(b[0]), __builtin_amdgcn_exp2f(b[1])};
        }
        const f32x2 tp = (ep[m][0] + ep[m][1]) + (ep[m][2] + ep[m][3]), tn = (en[m][0] + en[m][1]) + (en[m][2] + en[m][3]);
        sp[m] = tp[0] + tp[1]; sn[m] = tn[0] + tn[1];
    }
#pragma unroll
    for (int m = 0; m < 4; ++m) red2<false>(sp[m], sn[m]);
#pragma unroll
    for (int m = 0; m < 4; ++m) {
        const float rp = __builtin_amdgcn_rcpf(sp[m]), rn = __builtin_amdgcn_rcpf(sn[m]);
        u32x4 a, b;
#pragma unroll
        for (int i = 0; i < 4; ++i) { const f32x2 x = ep[m][i] * rp, y = en[m][i] * rn; a[i] = pk2(x[0], x[1]); b[i] = pk2(y[0], y[1]); }
        pos[m] = __builtin_bit_cast(bf16x8, a); neg[m] = __builtin_bit_cast(bf16x8, b);
    }
}
DI u32x2 glu4(const f32x4& g, const f32x4& u) {
    u32x2 r;
#pragma unroll
    for (int i = 0; i < 2; ++i) {
        const f32x2 t = (f32x2){g[2 * i], g[2 * i + 1]} * -1.44269504088896340736f;
        const f32x2 d = (f32x2){__builtin_amdgcn_exp2f(t[0]), __builtin_amdgcn_exp2f(t[1])} + 1.0f;
        const f32x2 h = (f32x2){u[2 * i], u[2 * i + 1]} * (f32x2){__builtin_amdgcn_rcpf(d[0]), __builtin_amdgcn_rcpf(d[1])};
        r[i] = pk2(h[0], h[1]);
    }
    return r;
}
DI u32x4 glu8(const f32x4& g0, const f32x4& u0, const f32x4& g1, const f32x4& u1) { const u32x2 a = glu4(g0, u0), b = glu4(g1, u1); return (u32x4){a[0], a[1], b[0], b[1]}; }

constexpr int G2_SLOT = 32768;
constexpr int G2_NS = 4;
constexpr int G2_KV = G2_NS * G2_SLOT;
static_assert(G2_KV + 8192 + 256 <= G1_WPHI, "");
#define G2_NA 32
constexpr int G2_NB = 72 - G2_NA, G2_NBH = G2_NB - 16;
static_assert(G2_NA % 4 == 0 && G2_NA >= 32 && G2_NA <= 36, "");
typedef GAS unsigned gu32;
DI void wait_tags(gu32* p, unsigned seen, unsigned tag) {
    if (__builtin_amdgcn_ballot_w64(seen != tag) != 0ull) {
        unsigned spins = 0;
        while (__builtin_amdgcn_ballot_w64(__hip_atomic_load(p, __ATOMIC_RELAXED, __HIP_MEMORY_SCOPE_AGENT) != tag) != 0ull && ++spins < (1u << 18))
            __builtin_amdgcn_s_sleep(1);
    }
}
DI void arrive(LAS unsigned* arr, gu32* word, unsigned tag) {
    const int l = threadIdx.x & 63;
    unsigned old = 0;
    if (l == 0) old = __hip_atomic_fetch_add(arr, 1u, __ATOMIC_RELAXED, __HIP_MEMORY_SCOPE_WORKGROUP);
    if (__builtin_amdgcn_readfirstlane(old) == 7u && l < 4) __hip_atomic_store(word + 64 * l, tag, __ATOMIC_RELAXED, __HIP_MEMORY_SCOPE_AGENT);
}
DI float wsum(float v) {
    v += __builtin_amdgcn_update_dpp(0.f, v, 0xb1, 0xf, 0xf, true);
    v += __builtin_amdgcn_update_dpp(0.f, v, 0x4e, 0xf, 0xf, true);
    v += __builtin_amdgcn_update_dpp(0.f, v, 0x141, 0xf, 0xf, true);
    v += __builtin_amdgcn_update_dpp(0.f, v, 0x140, 0xf, 0xf, true);
    float u = v; swap16(v, u); v += u;
    u = v; swap32(v, u); return v + u;
}

#define LP(a) ((LAS const char*)(uintptr_t)(a))
#define G1_STAGE_A(slot) do { dma2(aptr, avoff, adst + (slot) * G1_SLOT); aptr += NMB * 1024; OPAQUE_S(aptr); } while (0)
#define G1_STAGE_B(slot) do { \
        dma1<0>(bptr[0], bvoff[0], bdst[0] + (slot) * G1_SLOT); dma1<0>(bptr[1], bvoff[1], bdst[1] + (slot) * G1_SLOT); \
        if (grp == 0) dma1<0>(bptr[2], bvoff[2], bdst[2] + (slot) * G1_SLOT); \
        bptr[0] += bstep[0]; bptr[1] += bstep[1]; bptr[2] += bstep[2]; \
        OPAQUE_S(bptr[0]); OPAQUE_S(bptr[1]); OPAQUE_S(bptr[2]); } while (0)
#define G1_STAGE(slot) do { G1_STAGE_A(slot); G1_STAGE_B(slot); } while (0)
#define G1_WAIT() WAIT_V(8)

template <bool kvt>
DI void tail(const uint16_t* __restrict__ x, const uint16_t* __restrict__ wphi, const uint16_t* __restrict__ wdown, const uint16_t* __restrict__ wo, char* __restrict__ ws,
             uint16_t* __restrict__ out, LAS char* lds, const unsigned tag, const char* aptr, const char* bp0, const char* bp1, const char* bp2,
             unsigned bs0, unsigned bs1, unsigned bs2, const char* pw0, const char* pw1, const char* pw2) {
    int tid = threadIdx.x;
    OPAQUE(tid);
    const int l = tid & 63;
    const int w = __builtin_amdgcn_readfirstlane(tid >> 6);
    const int grp = w >> 2, wr = w & 3, wc = grp;
    const int bid = blockIdx.x, idx = bid >> 3, xcd = bid & 7;
    const int pm = idx >> 2;
    const int j = xcd * 2 + ((idx >> 1) & 1);
    const int F0 = kvt ? 2048 + 96 * j : 128 * j;
    gu32* cnt = (gu32*)(ws + WS_CNT);
    const int G = l >> 4, q4 = (l >> 2) & 3, p4 = l & 3;
    const unsigned lbase = (unsigned)(uintptr_t)lds;
    const rsrc_t rw = mkrsrc(ws, (unsigned)WS_END);
    const char* bptr[3] = {bp0, bp1, bp2};
    const unsigned bstep[3] = {bs0, bs1, bs2};
    unsigned bvoff[3];
#pragma unroll
    for (int i = 0; i < 3; ++i) bvoff[i] = (l >> 2) * (bstep[i] >> 5) + ((((l >> 1) & 1) ^ ((l >> 5) & 1)) * 32) + (l & 1) * 16;
    const unsigned avoff = l * 16, adst = lbase + 2 * w * 1024;
    unsigned bdst[3];
#pragma unroll
    for (int i = 0; i < 3; ++i) bdst[i] = lbase + 16384 + (grp == 0 ? 3 * w + i : 12 + 2 * (w - 4) + (i < 2 ? i : 0)) * 1024;

    const unsigned lp0 = G * 512 + q4 * 64 + ((G & 1) * 32) + p4 * 8;
    const unsigned lp1 = lp0 ^ 32;
    unsigned rA = lbase + wr * 4096 + l * 16;
    unsigned rB0 = lbase + 16384 + 5 * wc * 2048 + lp0;
    unsigned rB1 = lbase + 16384 + 5 * wc * 2048 + lp1;
    unsigned rB8 = kvt ? lbase + 16384 + 2 * 2048 + (wc ? lp1 : lp0) : rB0 + 2 * 2048;
    unsigned rB9 = kvt ? lbase + 16384 + 7 * 2048 + (wc ? lp1 : lp0) : rB1 + 2 * 2048;
#define G1_MOVE(d) do { rA += (d); rB0 += (d); rB1 += (d); rB8 += (d); rB9 += (d); \
        OPAQUE(rA); OPAQUE(rB0); OPAQUE(rB1); OPAQUE(rB8); OPAQUE(rB9); } while (0)

    f32x4 acc[4][10];
#pragma unroll
    for (int m = 0; m < 4; ++m)
#pragma unroll
        for (int n = 0; n < 10; ++n) acc[m][n] = (f32x4){0.f, 0.f, 0.f, 0.f};
    bf16x8 fa[4], fb[10];

#define G1_READ(so) do { \
        _Pragma("unroll") for (int m = 0; m < 4; ++m) fa[m] = *(LAS const bf16x8*)(LP(rA) + (so) + m * 1024); \
        _Pragma("unroll") for (int u = 0; u < 5; ++u) if (u != 2) { \
            fb[2 * u] = tr_frag(LP(rB0) + (so) + u * 2048, LP(rB0) + (so) + u * 2048 + 256); \
            fb[2 * u + 1] = tr_frag(LP(rB1) + (so) + u * 2048, LP(rB1) + (so) + u * 2048 + 256); } \
        fb[4] = tr_frag(LP(rB8) + (so), LP(rB8) + (so) + 256); \
        fb[5] = tr_frag(LP(rB9) + (so), LP(rB9) + (so) + 256); } while (0)
#define G1_MMA() do { __builtin_amdgcn_s_setprio(1); \
        _Pragma("unroll") for (int m = 0; m < 4; ++m) _Pragma("unroll") for (int n_ = 0; n_ < 10; ++n_) { const int n = (m & 1) ? 9 - n_ : n_; MFMA16_ACC(acc[m][n], fb[n], fa[m]); } \
        __builtin_amdgcn_s_setprio(0); } while (0)
#define G1_PHASE(so, STG, VW) do { G1_READ(so); STG; WAIT_L0(); VW; SCHED(); BAR(); SCHED(); G1_MMA(); SCHED(); BAR(); SCHED(); } while (0)

    G1_MOVE(0);
#pragma unroll
    for (int m = 0; m < 4; ++m)
#pragma unroll
        for (int n = 0; n < 10; ++n) OPAQUE(acc[m][n]);
    SCHED();
    wait_tags(cnt + 64 * (C_LN + pm * 8 + w) + (l & 3), tag - 1u, tag);
    STAMP(0, 3);
    G1_STAGE_A(0); G1_STAGE_A(1);
    WAIT_V(2);
    BAR();
    G1_STAGE(2);
    if (grp == 1) dma1<0>((const char*)wphi + (size_t)(2 * j) * 2048 + wr * 1024, avoff, lbase + G1_WPHI + wr * 1024);
    STAMP(1, 1);
    if (grp == 1) BAR();
    SCHED();
    for (int it = 0; it < 7; ++it) {
        if (w == 0) STAMP(4, it);
        if (w >= 6) {
            const int c = bid * 2 + w - 6;
            const char* g; int lim;
            if (it < 4) { lim = 480; g = c < 144 ? pw0 + (size_t)(192 + it * 144 + c) * 8192 : (c < 312 ? pw1 - 144 * 8192 : pw2 - 312 * 8192) + (size_t)(224 + it * 168 + c) * 8192; }
            else { const int u = (it - 4) * 384 + c; lim = 384; g = u < 896 ? (const char*)wdown + (size_t)u * 8192 : (const char*)wo + (size_t)(u - 896) * 8192; }
            if (c < lim) pf_lines(g, avoff * 8, lbase + G1_PF, ~0ull);
        }
        G1_PHASE(0, G1_STAGE(3), G1_WAIT());
        G1_PHASE(G1_SLOT, G1_STAGE(0), G1_WAIT());
        G1_MOVE(2 * G1_SLOT);
        G1_PHASE(0, G1_STAGE(1), G1_WAIT());
        G1_PHASE(G1_SLOT, G1_STAGE(2), G1_WAIT());
        G1_MOVE(-2 * G1_SLOT);
    }
    if (w == 0) STAMP(4, 7);
    G1_PHASE(0, G1_STAGE(3), G1_WAIT());
    G1_PHASE(G1_SLOT, (void)0, WAIT_V(4));
    G1_MOVE(2 * G1_SLOT);
    G1_PHASE(0, (void)0, WAIT_V(0));
    G1_READ(G1_SLOT); WAIT_L0(); SCHED(); BAR(); SCHED(); G1_MMA(); SCHED();
    if (grp == 0) BAR();
    asm volatile("s_nop 7\n\ts_nop 7" ::: "memory");
    SCHED();
    STAMP(1, 2);
    if (kvt) STAMP(2, 0);

    constexpr bool tb = !kvt;
    const int t16 = idx >> 1;
    const int pm2 = (xcd >> 1) * 4 + (t16 & 3), pn = (xcd & 1) * 4 + (t16 >> 2);
    const int tile = pm2 * 8 + pn;
    const int prep = 4 * (pm2 >> 1) + (xcd & 1) * 2 + (wr & 1);
    constexpr int k0 = tb ? 0 : G2_NBH;
    const unsigned lo16 = l * 16;
    const bool isb = wr >= 2;
    const char* gptr = isb ? (const char*)(wdown + (size_t)(64 * k0 + 32 * (wr - 2)) * D_ + pn * 128) : ws + WS_A2 + ((size_t)(2 * k0 + wr) * NMB + pm2 * 8) * 1024;
    const char* gptr_o = isb ? (const char*)(wo + (size_t)(32 * (wr & 1)) * D_ + pn * 128) : ws + WS_A2 + ((size_t)(112 + wr) * NMB + pm2 * 8) * 1024;
    const unsigned gstep = isb ? 64 * D_ * 2 : 2 * NMB * 1024;
    unsigned voff[4];
#pragma unroll
    for (int i = 0; i < 4; ++i) {
        const int r = 4 * i + G;
        voff[i] = isb ? r * (D_ * 2) + ((((l & 15) >> 1) ^ (r & 7)) * 32) + (l & 1) * 16 - 1024 * i : lo16;
    }
    const unsigned ghalf = isb ? 16 * D_ * 2 : 4096;
    unsigned dst = lbase + wr * 8192;
    int tld = 0, slot = 0;
#define G2_DMA4_(g, d, SC) asm volatile("s_mov_b32 m0, %5\n\ts_nop 0\n\tglobal_load_lds_dwordx4 %0, %4" SC "\n\tglobal_load_lds_dwordx4 %1, %4 offset:1024" SC "\n\t" \
            "global_load_lds_dwordx4 %2, %4 offset:2048" SC "\n\tglobal_load_lds_dwordx4 %3, %4 offset:3072" SC \
            :: "v"(voff[0]), "v"(voff[1]), "v"(voff[2]), "v"(voff[3]), "s"(g), "s"(d) : "memory")
#define G2_DMA4(g, d) G2_DMA4_(g, d, " sc1")
#define G2_NEXTSLOT() do { ++slot; const int wrap = slot == G2_NS; slot = wrap ? 0 : slot; dst += wrap ? (unsigned)(-(G2_NS - 1) * G2_SLOT) : (unsigned)G2_SLOT; } while (0)
#define G2_STAGE() do { \
        if (!isb) { G2_DMA4(gptr, dst); G2_DMA4(gptr + ghalf, dst + 4096); } else { G2_DMA4_(gptr, dst, ""); G2_DMA4_(gptr + ghalf, dst + 4096, ""); } \
        ++tld; gptr = (tb && tld == G2_NBH) ? gptr_o : gptr + gstep; G2_NEXTSLOT(); } while (0)
#define G2_PRUN(n) do { _Pragma("unroll 1") for (int t_ = 0; t_ < (n); ++t_) { WAIT_V(16); BAR(); G2_STAGE(); } } while (0)
#define POLL(dst_, p_) asm volatile("global_load_dword %0, %1, off sc1" : "=v"(dst_) : "v"(p_) : "memory")
    const size_t off0 = (size_t)(pm2 * 128 + (wr >> 1) * 64 + (l & 15)) * D_ + pn * 128 + (wr & 1) * 64 + (G & 1) * 16 + (G >> 1) * 8;

    const int fq = l >> 4, fr = l & 15;
    const int hh = 2 * j + wc;
    const int mb0 = pm * 16 + wr * 4;
    if (tb && w >= 6) { G2_STAGE(); G2_STAGE(); }
    bf16x8 wp[2];
#pragma unroll
    for (int c = 0; c < 2; ++c) {
        LAS const char* p = lds + G1_WPHI + wc * 2048 + (4 * G + q4) * 64 + c * 32 + p4 * 8;
        wp[c] = tr_frag(p, p + 1024);
    }
    const f32x4 z4 = {0.f, 0.f, 0.f, 0.f};
    f32x4 pf[4][2];
#pragma unroll
    for (int m = 0; m < 4; ++m) {
        const bf16x8 qf = pack8(acc[m][0], acc[m][1]);
        pf[m][0] = MFMA16(wp[0], qf, z4); pf[m][1] = MFMA16(wp[1], qf, z4);
    }
    const unsigned hb = (unsigned)WS_A2 + (((kvt ? F0 + 32 * wc : F0 + 64 * wc + 32) >> 5) * NMB + mb0) * 1024;
    bf16x8 pos[4], neg[4];
    u32x4 xv[4][2];
    if constexpr (!kvt) {
        __builtin_amdgcn_s_setprio(1);
#pragma unroll
        for (int m = 0; m < 4; ++m) {
            stw(glu8(acc[m][2], acc[m][4], acc[m][3], acc[m][5]), rw, lo16 + m * 1024, hb - NMB * 1024);
            stw(glu8(acc[m][6], acc[m][8], acc[m][7], acc[m][9]), rw, lo16 + m * 1024, hb);
        }
        SCHED();
        WAIT_V(0); arrive((LAS unsigned*)(lds + G1_ARR), cnt + 64 * (C_HQ + 4 * pm) + j, tag);
        __builtin_amdgcn_s_setprio(0);
        SCHED();
        STAMP(1, 5);
        hedgehog4(pf, pos, neg);
    } else {
        u32x4 i0 = {0u, 0u, 0u, 0u}, i1 = {0u, 0u, 0u, 0u};
        {
            const int d = fr - 4 * fq;
            const unsigned lo = (d == 0) ? 0x3f80u : (d == 1) ? 0x3f800000u : 0u;
            const unsigned hi = (d == 2) ? 0x3f80u : (d == 3) ? 0x3f800000u : 0u;
            i0[0] = lo; i0[1] = hi; i1[2] = lo; i1[3] = hi;
        }
        const bf16x8 I0 = __builtin_bit_cast(bf16x8, i0), I1 = __builtin_bit_cast(bf16x8, i1);
        constexpr int ZRED = 65536, HX = ZRED + 2048;
        bf16x8 V[2][2];
        {
            f32x4 TV[4][2];
#pragma unroll
            for (int m = 0; m < 4; ++m) {
                const bf16x8 vf = pack8(acc[m][2], acc[m][3]);
                TV[m][0] = MFMA16(vf, I0, z4); TV[m][1] = MFMA16(vf, I1, z4);
            }
#pragma unroll
            for (int m = 0; m < 4; ++m) *(LAS u32x2*)(lds + HX + (wr * 4 + m) * 1024 + lo16 + 8 * wc) = glu4(acc[m][4], acc[m][5]);
#pragma unroll
            for (int mp = 0; mp < 2; ++mp) { V[mp][0] = pack8(TV[2 * mp][0], TV[2 * mp + 1][0]); V[mp][1] = pack8(TV[2 * mp][1], TV[2 * mp + 1][1]); }
        }
        SCHED();
        STAMP(2, 1);
        hedgehog4(pf, pos, neg);
        SCHED();
        STAMP(2, 2);
        const u32x4 o4 = {0x3f803f80u, 0x3f803f80u, 0x3f803f80u, 0x3f803f80u};
        const bf16x8 ONES = __builtin_bit_cast(bf16x8, o4);
        f32x4 T[4][2][2];
#pragma unroll
        for (int m = 0; m < 4; ++m) {
            T[m][0][0] = MFMA16(pos[m], I0, z4); T[m][0][1] = MFMA16(pos[m], I1, z4);
            T[m][1][0] = MFMA16(neg[m], I0, z4); T[m][1][1] = MFMA16(neg[m], I1, z4);
        }
        LAS char* red = lds + (wc * 4 + wr) * 8192 + lo16;
#pragma unroll
        for (int s = 0; s < 2; ++s)
#pragma unroll
            for (int cc = 0; cc < 2; ++cc) {
                const bf16x8 P0 = pack8(T[0][s][cc], T[1][s][cc]), P1 = pack8(T[2][s][cc], T[3][s][cc]);
                const f32x4 k0_ = MFMA16(P1, V[1][0], MFMA16(P0, V[0][0], z4));
                const f32x4 k1_ = MFMA16(P1, V[1][1], MFMA16(P0, V[0][1], z4));
                const f32x4 zz = MFMA16(P1, ONES, MFMA16(P0, ONES, z4));
                *(LAS f32x4*)(red + ((s * 2 + 0) * 2 + cc) * 1024) = k0_;
                *(LAS f32x4*)(red + ((s * 2 + 1) * 2 + cc) * 1024) = k1_;
                *(LAS f32x4*)(lds + ZRED + (wc * 4 + wr) * 256 + (s * 32 + cc * 16 + 4 * fq) * 4) = zz;
            }
        WAIT_L0(); SCHED();
        STAMP(2, 3);
        BAR(); SCHED();
        STAMP(2, 4);
        const unsigned kvo = (unsigned)WS_KVP + (pm * 32 + hh) * KVP_SZ;
        u32x4 hx[2];
#pragma unroll
        for (int i = 0; i < 2; ++i) {
            LAS const char* p = lds + wc * 32768 + (2 * wr + i) * 1024 + lo16;
            const f32x4 sum = (*(LAS const f32x4*)p + *(LAS const f32x4*)(p + 8192)) + (*(LAS const f32x4*)(p + 16384) + *(LAS const f32x4*)(p + 24576));
            stw(sum, rw, lo16 + i * 1024, kvo + 2 * wr * 1024);
            hx[i] = *(LAS const u32x4*)(lds + HX + (wr * 4 + 2 * wc + i) * 1024 + lo16);
        }
        {
            LAS const char* p = lds + ZRED + wc * 1024 + (l & 15) * 16;
            const f32x4 sum = (*(LAS const f32x4*)p + *(LAS const f32x4*)(p + 256)) + (*(LAS const f32x4*)(p + 512) + *(LAS const f32x4*)(p + 768));
            stw(sum, rw, (wr == 0 && l < 16) ? lo16 : 0x80000000u, kvo + 8192);
        }
        WAIT_L0(); SCHED(); BAR(); SCHED();
        STAMP(2, 5);
        u32x4 hg[4];
#pragma unroll
        for (int m = 0; m < 2; ++m) hg[m] = glu8(acc[m][6], acc[m][8], acc[m][7], acc[m][9]);
        SCHED();
        WAIT_V(0); arrive((LAS unsigned*)(lds + G1_ARR), cnt + 64 * (C_KV + 4 * j) + pm, tag);
        STAMP(2, 6);
        if (w >= 6) { G2_STAGE(); G2_STAGE(); }
        if (w < 4) {
#pragma unroll
            for (int m = 0; m < 4; ++m) { xv[m][0] = *(const u32x4*)(x + off0 + m * 16 * D_); xv[m][1] = *(const u32x4*)(x + off0 + m * 16 * D_ + 32); }
        }
#pragma unroll
        for (int i = 0; i < 2; ++i) stw(hx[i], rw, lo16 + i * 1024, (unsigned)WS_A2 + (((F0 + 64) >> 5) * NMB + mb0 + 2 * wc) * 1024);
#pragma unroll
        for (int m = 0; m < 4; ++m) { if (m >= 2) hg[m] = glu8(acc[m][6], acc[m][8], acc[m][7], acc[m][9]); stw(hg[m], rw, lo16 + m * 1024, hb); }
        SCHED();
        STAMP(2, 7);
        if (w == 4 || w == 5) { wait_tags(cnt + 64 * (C_HQ + prep) + (l & 15), tag - 1u, tag); G2_STAGE(); G2_STAGE(); }
        WAIT_V(0); arrive((LAS unsigned*)(lds + G1_ARR + 4), cnt + 64 * (C_HK + 4 * pm) + j, tag);
    }
    SCHED();
    STAMP(1, 6);

#define G2_OCOMP() do { \
        bf16x8 kf[2][2], zf[2]; \
        _Pragma("unroll") for (int s = 0; s < 2; ++s) { \
            kf[s][0] = *(LAS const bf16x8*)(lds + G2_KV + wc * 4096 + (s * 2) * 1024 + lo16); kf[s][1] = *(LAS const bf16x8*)(lds + G2_KV + wc * 4096 + (s * 2 + 1) * 1024 + lo16); \
            const u32x2 za = *(LAS const u32x2*)(lds + G2_KV + 8192 + wc * 128 + s * 64 + G * 8), zb = *(LAS const u32x2*)(lds + G2_KV + 8192 + wc * 128 + s * 64 + 32 + G * 8); \
            zf[s] = __builtin_bit_cast(bf16x8, (u32x4){za[0], za[1], zb[0], zb[1]}); } \
        _Pragma("unroll") for (int m = 0; m < 4; ++m) { \
            const f32x4 dn = MFMA16(zf[1], neg[m], MFMA16(zf[0], pos[m], z4)); \
            const f32x4 n0 = MFMA16(kf[1][0], neg[m], MFMA16(kf[0][0], pos[m], z4)), n1 = MFMA16(kf[1][1], neg[m], MFMA16(kf[0][1], pos[m], z4)); \
            const float rd = __builtin_amdgcn_rcpf(dn[0] + 1e-6f); \
            stw(pack8(n0 * rd, n1 * rd), rw, lo16 + m * 1024, (unsigned)WS_A2 + ((112 + hh) * NMB + mb0) * 1024); } \
        WAIT_L0(); SCHED(); } while (0)

    if (w >= 4) {
        unsigned seen;
        if (tb && !isb) { wait_tags(cnt + 64 * (C_HQ + prep) + (l & 15), tag - 1u, tag); STAMP(6, 0); G2_STAGE(); G2_STAGE(); }
        WAIT_V(8);
        STAMP(5 + tb, 1);
        BAR(); G2_STAGE();
        WAIT_V(8); BAR(); G2_STAGE(); G2_STAGE();
        if constexpr (!tb) {
            constexpr int UK = 32 - G2_NBH - 4;
            G2_PRUN(UK - 3);
            POLL(seen, cnt + 64 * (C_HK + prep) + (l & 15));
            G2_PRUN(2);
            asm volatile("s_waitcnt vmcnt(16)" : "+v"(seen) :: "memory");
            if (!isb) wait_tags(cnt + 64 * (C_HK + prep) + (l & 15), seen, tag);
            BAR(); G2_STAGE();
            G2_PRUN(G2_NA - 5 - UK);
            WAIT_V(16); BAR(); WAIT_V(8); BAR(); WAIT_V(0); BAR(); BAR();
            BAR();
        } else {
            G2_PRUN(1);
            POLL(seen, cnt + 64 * (C_KV + 4 * j + wr) + (l & 7));
            G2_PRUN(2);
            asm volatile("s_waitcnt vmcnt(16)" : "+v"(seen) :: "memory");
            wait_tags(cnt + 64 * (C_KV + 4 * j + wr) + (l & 7), seen, tag);
            f32x4 t[4][8]; float zt[8];
            const char* kvs = ws + WS_KVP + (size_t)(2 * j + (wr >> 1)) * KVP_SZ;
            const unsigned kvv = (wr & 1) * 4096 + lo16, zv = 8192 + l * 4;
#define G2_KVLD(i) do { _Pragma("unroll") for (int k = 0; k < 8; ++k) \
                asm volatile("global_load_dwordx4 %0, %1, %2 offset:%3 sc1" : "=v"(t[i][k]) : "v"(kvv), "s"(kvs + (size_t)k * 32 * KVP_SZ), "i"((i) * 1024) : "memory"); \
            _Pragma("unroll") for (int k = 2 * (i); k < 2 * (i) + 2; ++k) \
                asm volatile("global_load_dword %0, %1, %2 sc1" : "=v"(zt[k]) : "v"(zv), "s"(kvs + (size_t)k * 32 * KVP_SZ) : "memory"); } while (0)
            BAR(); G2_STAGE(); G2_KVLD(0);
            WAIT_V(26); BAR(); G2_STAGE(); G2_KVLD(1);
            WAIT_V(36); BAR(); G2_STAGE(); G2_KVLD(2);
            WAIT_V(46); BAR(); G2_STAGE(); G2_KVLD(3);
            WAIT_V(46); BAR(); G2_STAGE();
            WAIT_V(36); BAR(); G2_STAGE();
            WAIT_V(26); BAR(); G2_STAGE();
            WAIT_V(16); SCHED();
#pragma unroll
            for (int i = 0; i < 4; ++i) {
                asm volatile("" : "+v"(t[i][0]), "+v"(t[i][1]), "+v"(t[i][2]), "+v"(t[i][3]), "+v"(t[i][4]), "+v"(t[i][5]), "+v"(t[i][6]), "+v"(t[i][7]), "+v"(zt[2 * i]), "+v"(zt[2 * i + 1]));
                const f32x4 sum = ((t[i][0] + t[i][1]) + (t[i][2] + t[i][3])) + ((t[i][4] + t[i][5]) + (t[i][6] + t[i][7]));
                const int id = 4 * (wr & 1) + i;
                *(LAS u32x2*)(lds + G2_KV + (wr >> 1) * 4096 + (id >> 1) * 1024 + lo16 + (id & 1) * 8) = (u32x2){pk2(sum[0], sum[1]), pk2(sum[2], sum[3])};
            }
            const float z = ((zt[0] + zt[1]) + (zt[2] + zt[3])) + ((zt[4] + zt[5]) + (zt[6] + zt[7]));
            *(LAS unsigned short*)(lds + G2_KV + 8192 + (wr >> 1) * 128 + l * 2) = (unsigned short)pk2(z, z);
            WAIT_L0(); SCHED();
            BAR(); G2_STAGE();
            G2_OCOMP();
            G2_PRUN(G2_NB - 34);
            gu32* f0 = cnt + 64 * (C_O + prep) + (l & 15);
            POLL(seen, f0);
            G2_PRUN(2);
            asm volatile("s_waitcnt vmcnt(16)" : "+v"(seen) :: "memory");
            STAMP(6, 2);
            if (!isb) wait_tags(f0, seen, tag);
            STAMP(6, 3);
            BAR(); G2_STAGE();
            G2_PRUN(13);
            gu32* f1 = cnt + 64 * (C_P + tile);
            POLL(seen, f1);
            G2_PRUN(2);
            asm volatile("s_waitcnt vmcnt(16)" : "+v"(seen) :: "memory");
            STAMP(6, 4);
            wait_tags(f1, seen, tag);
            STAMP(6, 5);
            char* part = ws + WS_PART + (size_t)tile * 65536;
#pragma unroll
            for (int i = 0; i < 4; ++i) voff[i] = lo16;
            BAR(); G2_DMA4(part + wr * 8192, dst); G2_DMA4(part + wr * 8192 + 4096, dst + 4096); G2_NEXTSLOT();
            WAIT_V(16); BAR(); G2_DMA4(part + 32768 + wr * 8192, dst); G2_DMA4(part + 32768 + wr * 8192 + 4096, dst + 4096);
            WAIT_V(16);
            BAR(); BAR();
            WAIT_V(0);
            BAR();
        }
        STAMP(3, 7);
        return;
    }
    const int cr = w >> 1, cc2 = w & 1;
    f32x4 ac2[4][4];
#pragma unroll
    for (int m = 0; m < 4; ++m)
#pragma unroll
        for (int i = 0; i < 2; ++i) {
            if constexpr (tb) { ac2[m][2 * i] = z4; ac2[m][2 * i + 1] = z4; }
            else {
                unsigned d0 = xv[m][i][0], d1 = xv[m][i][1], d2 = xv[m][i][2], d3 = xv[m][i][3];
                swap16u(d0, d2); swap16u(d1, d3);
                ac2[m][2 * i] = (f32x4){bflo(d0), bfhi(d0), bflo(d1), bfhi(d1)}; ac2[m][2 * i + 1] = (f32x4){bflo(d2), bfhi(d2), bflo(d3), bfhi(d3)};
            }
        }
    const int r8 = 4 * (G & 1) + q4;
    unsigned sA = lbase + cr * 4096 + lo16;
    unsigned sB[4];
#pragma unroll
    for (int n = 0; n < 4; ++n) sB[n] = lbase + 16384 + (4 * G + q4) * 256 + (((4 * cc2 + n) ^ r8) * 32) + p4 * 8;
#define G2_MOVE(d) do { sA += (d); sB[0] += (d); sB[1] += (d); sB[2] += (d); sB[3] += (d); OPAQUE(sA); OPAQUE(sB[0]); OPAQUE(sB[1]); OPAQUE(sB[2]); OPAQUE(sB[3]); } while (0)
    bf16x8 ga[2][2][4], gb[2][2][4];
#define G2_RD_A(buf, so, kk, m) ga[buf][kk][m] = *(LAS const bf16x8*)(LP(sA) + (so) + (kk) * 8192 + (m) * 1024)
#define G2_RD_B(buf, so, kk, n) gb[buf][kk][n] = tr_frag(LP(sB[n]) + (so) + (kk) * 8192, LP(sB[n]) + (so) + (kk) * 8192 + 4096)
#define G2_READ(buf, so) do { _Pragma("unroll") for (int kk = 0; kk < 2; ++kk) _Pragma("unroll") for (int i = 0; i < 4; ++i) { G2_RD_B(buf, so, kk, i); G2_RD_A(buf, so, kk, i); } } while (0)
#define G2_STEP(cur, so, RD) do { BAR(); SCHED(); \
        _Pragma("unroll") for (int kk = 0; kk < 2; ++kk) _Pragma("unroll") for (int m = 0; m < 4; ++m) _Pragma("unroll") for (int n_ = 0; n_ < 4; ++n_) { \
            const int n = (m & 1) ? 3 - n_ : n_; \
            MFMA16_ACC(ac2[m][n], gb[cur][kk][n], ga[cur][kk][m]); \
            if ((RD) && kk == 0) { const int i_ = m * 4 + n_; if (i_ & 1) G2_RD_A((cur) ^ 1, so, i_ >> 3, (i_ >> 1) & 3); else G2_RD_B((cur) ^ 1, so, i_ >> 3, (i_ >> 1) & 3); } \
            SCHED(); } \
        WAIT_L0(); SCHED(); } while (0)
    unsigned mv = 2 * G2_SLOT;
#define G2_RUN(n) do { _Pragma("unroll 1") for (int i_ = 0; i_ < (n); ++i_) { G2_STEP(0, G2_SLOT, 1); G2_MOVE(mv); mv = -mv; G2_STEP(1, 0, 1); } } while (0)

    G2_MOVE(0);
    BAR(); SCHED();
    G2_READ(0, 0);
    WAIT_L0(); SCHED();
    STAMP(3, 1);
    if constexpr (tb) {
        G2_RUN(6);
        G2_OCOMP();
        G2_RUN(1);
        WAIT_V(0); SCHED();
        G2_RUN(1);
        if (tid < 4) __hip_atomic_store(cnt + 64 * (C_O + 4 * pm + tid) + j, tag, __ATOMIC_RELAXED, __HIP_MEMORY_SCOPE_AGENT);
        SCHED();
        G2_RUN(G2_NB / 2 - 9);
    } else {
        G2_RUN(G2_NA / 2 - 1);
    }
    G2_STEP(0, G2_SLOT, 1);
    G2_STEP(1, 0, 0);
    asm volatile("s_nop 7\n\ts_nop 7" ::: "memory");
    SCHED();
    if constexpr (!tb) {
        STAMP(3, 2);
#pragma unroll
        for (int m = 0; m < 4; ++m)
#pragma unroll
            for (int n = 0; n < 4; ++n) stw(ac2[m][n], rw, lo16 + n * 1024, (unsigned)WS_PART + tile * 65536 + w * 16384 + m * 4096);
        WAIT_V(0); BAR();
        if (tid == 0) __hip_atomic_store(cnt + 64 * (C_P + tile), tag, __ATOMIC_RELAXED, __HIP_MEMORY_SCOPE_AGENT);
        STAMP(3, 6);
    } else {
        STAMP(3, 4);
        BAR(); SCHED();
#pragma unroll
        for (int m = 0; m < 4; ++m)
#pragma unroll
            for (int i = 0; i < 2; ++i) {
                const f32x4 r0 = ac2[m][2 * i] + *(LAS const f32x4*)(lds + w * 16384 + (m * 4 + 2 * i) * 1024 + lo16);
                const f32x4 r1 = ac2[m][2 * i + 1] + *(LAS const f32x4*)(lds + w * 16384 + (m * 4 + 2 * i + 1) * 1024 + lo16);
                unsigned d0 = pk2(r0[0], r0[1]), d1 = pk2(r0[2], r0[3]), d2 = pk2(r1[0], r1[1]), d3 = pk2(r1[2], r1[3]);
                swap16u(d0, d2); swap16u(d1, d3);
                *(u32x4*)(out + off0 + m * 16 * D_ + i * 32) = (u32x4){d0, d1, d2, d3};
            }
        if (bid == 0 && tid == 0) __hip_atomic_store(cnt + 64 * C_E, tag, __ATOMIC_RELAXED, __HIP_MEMORY_SCOPE_AGENT);
        STAMP(3, 5);
    }
}

__global__ __launch_bounds__(512, 2) void k_main(const uint16_t* __restrict__ x, const uint16_t* __restrict__ wqkv, const uint16_t* __restrict__ wphi,
                                                const uint16_t* __restrict__ wgate, const uint16_t* __restrict__ wup, const uint16_t* __restrict__ wdown,
                                                const uint16_t* __restrict__ wo, char* __restrict__ ws, uint16_t* __restrict__ out) {
    __shared__ __attribute__((aligned(16))) char smem_[G1_LDS];
    LAS char* lds = (LAS char*)smem_;
    const int tid = threadIdx.x, l = tid & 63;
    const int w = __builtin_amdgcn_readfirstlane(tid >> 6);
    const int grp = w >> 2;
    const int wr = w & 3, wc = grp;
    const int bid = blockIdx.x, idx = bid >> 3, xcd = bid & 7;
    const int pm = idx >> 2;
    const int j = xcd * 2 + ((idx >> 1) & 1);
    const bool kvt = (idx & 1) != 0;
    const int F0 = kvt ? 2048 + 96 * j : 128 * j;
    gu32* cnt = (gu32*)(ws + WS_CNT);
    STAMP(0, 0);
    const int lnslot = xcd * 4 + (idx & 3);
    u32x4 xa, xb; unsigned tag;
    {
        const char* xr = (const char*)x + (size_t)(pm * 256 + lnslot * 8 + w) * 2048;
        asm volatile("global_load_dwordx4 %0, %3, %4\n\tglobal_load_dwordx4 %1, %3, %4 offset:1024\n\tglobal_load_dword %2, %5, off sc1"
                     : "=&v"(xa), "=&v"(xb), "=&v"(tag) : "v"(l * 16), "s"(xr), "v"(cnt + 64 * C_E) : "memory");
    }

    const char* aptr = ws + WS_XN + (size_t)(pm * 16 + 2 * w) * 1024;
    const unsigned avoff = l * 16;
    const char* bptr[3]; unsigned bstep[3], bvoff[3], bdst[3];
#pragma unroll
    for (int i = 0; i < 3; ++i) {
        int t = grp == 0 ? 3 * w + i : 12 + 2 * (w - 4) + (i < 2 ? i : 0);
        const unsigned e = (unsigned)((kvt ? 0x491898502061204ull : 0xcb18a1412442040ull) >> (6 * (t >> 1))) & 63u;
        const unsigned mat = e & 3u, stride = mat == 0 ? 3 * D_ : F_;
        const uintptr_t base = mat == 0 ? (uintptr_t)wqkv : mat == 1 ? (uintptr_t)wgate : (uintptr_t)wup;
        bptr[i] = (const char*)(base + ((t & 1) * 16 * stride + (mat == 0 ? 64 * j : F0) + ((e >> 2) & 3u) * 1024 + (e >> 4) * 32) * 2u);
        bstep[i] = 64u * stride;
        bvoff[i] = (l >> 2) * stride * 2 + ((((l >> 1) & 1) ^ ((l >> 5) & 1)) * 32) + (l & 1) * 16;
        bdst[i] = (unsigned)(uintptr_t)lds + 16384 + t * 1024;
    }
    const unsigned lbase = (unsigned)(uintptr_t)lds;
    const unsigned adst = lbase + 2 * w * 1024;
    STAMP(1, 0);
    if (tid < 2) *(LAS unsigned*)(lds + G1_ARR + tid * 4) = 0u;
    G1_STAGE_B(0); G1_STAGE_B(1);
    if (grp == 0) WAIT_V(6); else WAIT_V(4);
    asm volatile("" : "+v"(xa), "+v"(xb), "+v"(tag) :: "memory");
    tag = __builtin_amdgcn_readfirstlane(tag) + 1u;
    const rsrc_t rw = mkrsrc(ws, (unsigned)WS_END);
    STAMP(0, 1);
    {
        float f[16];
#pragma unroll
        for (int i = 0; i < 4; ++i) { f[2 * i] = bflo(xa[i]); f[2 * i + 1] = bfhi(xa[i]); f[8 + 2 * i] = bflo(xb[i]); f[9 + 2 * i] = bfhi(xb[i]); }
        float sm = 0.f;
#pragma unroll
        for (int i = 0; i < 16; ++i) sm += f[i];
        const float mean = wsum(sm) * (1.0f / 1024.0f);
        float q = 0.f;
#pragma unroll
        for (int i = 0; i < 16; ++i) { f[i] -= mean; q += f[i] * f[i]; }
        const float rs = __builtin_amdgcn_rsqf(wsum(q) * (1.0f / 1024.0f) + 1e-5f);
        LAS char* ex = lds + 3 * G1_SLOT;
        *(LAS u32x4*)(ex + l * 144 + w * 16) = (u32x4){pk2(f[0] * rs, f[1] * rs), pk2(f[2] * rs, f[3] * rs), pk2(f[4] * rs, f[5] * rs), pk2(f[6] * rs, f[7] * rs)};
        *(LAS u32x4*)(ex + (64 + l) * 144 + w * 16) = (u32x4){pk2(f[8] * rs, f[9] * rs), pk2(f[10] * rs, f[11] * rs), pk2(f[12] * rs, f[13] * rs), pk2(f[14] * rs, f[15] * rs)};
        WAIT_L0(); SCHED(); BAR(); SCHED();
#pragma unroll
        for (int i = 0; i < 2; ++i) {
            const int c = i * 64 + (tid >> 3), r = tid & 7;
            stw(*(LAS const u32x4*)(ex + c * 144 + r * 16), rw, ((c >> 2) * NMB * 1024) + ((c & 3) * 16 + r) * 16, (unsigned)WS_XN + (pm * 16 + (lnslot >> 1)) * 1024 + (lnslot & 1) * 128);
        }
        SCHED();
        WAIT_V(0);
        SCHED(); BAR(); SCHED();
        STAMP(0, 2);
        if (tid == 0) __hip_atomic_store(cnt + 64 * (C_LN + pm * 8 + (lnslot >> 2)) + (lnslot & 3), tag, __ATOMIC_RELAXED, __HIP_MEMORY_SCOPE_AGENT);
        SCHED();
    }
    if (kvt) tail<true>(x, wphi, wdown, wo, ws, out, lds, tag, aptr, bptr[0], bptr[1], bptr[2], bstep[0], bstep[1], bstep[2], (const char*)wqkv, (const char*)wgate, (const char*)wup);
    else tail<false>(x, wphi, wdown, wo, ws, out, lds, tag, aptr, bptr[0], bptr[1], bptr[2], bstep[0], bstep[1], bstep[2], (const char*)wqkv, (const char*)wgate, (const char*)wup);
}

extern "C" void kernel_launch(void* const* d_in, const int* in_sizes, int n_in, void* d_out, int out_size, void* d_ws, size_t ws_size,
                              hipStream_t stream) {
    const uint16_t* x = (const uint16_t*)d_in[0];
    const uint16_t* wqkv = (const uint16_t*)d_in[1];
    const uint16_t* wphi = (const uint16_t*)d_in[2];
    const uint16_t* wo = (const uint16_t*)d_in[3];
    const uint16_t* wgate = (const uint16_t*)d_in[5];
    const uint16_t* wup = (const uint16_t*)d_in[6];
    const uint16_t* wdown = (const uint16_t*)d_in[7];
    char* ws = (char*)d_ws;
    k_main<<<256, 512, 0, stream>>>(x, wqkv, wphi, wgate, wup, wdown, wo, ws, (uint16_t*)d_out);
}
```

```cpp
#include <hip/hip_runtime.h>
#include <hip/hip_bf16.h>
#include <stdint.h>

#define LAS __attribute__((address_space(3)))
#define GAS __attribute__((address_space(1)))
#define DI __device__ __forceinline__
typedef __bf16 bf16x8 __attribute__((ext_vector_type(8)));
typedef __bf16 bf16x2 __attribute__((ext_vector_type(2)));
typedef short s16x4 __attribute__((ext_vector_type(4)));
typedef short s16x8 __attribute__((ext_vector_type(8)));
typedef float f32x4 __attribute__((ext_vector_type(4)));
typedef float f32x2 __attribute__((ext_vector_type(2)));
typedef unsigned u32x4 __attribute__((ext_vector_type(4)));
typedef unsigned u32x2 __attribute__((ext_vector_type(2)));

constexpr int S_ = 2048, D_ = 1024, F_ = 3584, H_ = 32;
constexpr int NMB = S_ / 16;
constexpr size_t WS_XN = 0;
constexpr size_t WS_A2 = WS_XN + (size_t)32 * NMB * 1024;
constexpr size_t WS_KVP = WS_A2 + (size_t)144 * NMB * 1024;
constexpr int KVP_SZ = 4352;
constexpr size_t WS_PART = WS_KVP + (size_t)8 * 32 * KVP_SZ;
constexpr size_t WS_CNT = WS_PART + (size_t)128 * 32768;
constexpr int C_LN = 0, C_HQ = 64, C_HK = 96, C_O = 128, C_KV = 160, C_P = 224, C_E = 352, C_N = 353;
constexpr size_t WS_END = WS_CNT + C_N * 256;

#define STAMP(kid, i) do {} while (0)
#define MFMA16(a, b, c) __builtin_amdgcn_mfma_f32_16x16x32_bf16((a), (b), (c), 0, 0, 0)
#define WAIT_V(n) asm volatile("s_waitcnt vmcnt(" #n ")" ::: "memory")
#define WAIT_L(n) asm volatile("s_waitcnt lgkmcnt(" #n ")" ::: "memory")
#define WAIT_L0() __builtin_amdgcn_s_waitcnt(0xC07F)
#define MFMA16_ACC(acc, a, b) asm volatile("v_mfma_f32_16x16x32_bf16 %0, %1, %2, %0" : "+v"(acc) : "v"(a), "v"(b))
#define BAR() __builtin_amdgcn_s_barrier()
#define SCHED() __builtin_amdgcn_sched_barrier(0)

DI unsigned pk2(float lo, float hi) {
    f32x2 v = {lo, hi};
    return __builtin_bit_cast(unsigned, __builtin_convertvector(v, bf16x2));
}
DI float bflo(unsigned u) { return __uint_as_float(u << 16); }
DI float bfhi(unsigned u) { return __uint_as_float(u & 0xffff0000u); }
DI float bfr(float x) { return bflo(pk2(x, x)); }
DI bf16x8 pack8(const f32x4& a, const f32x4& b) {
    u32x4 r = {pk2(a[0], a[1]), pk2(a[2], a[3]), pk2(b[0], b[1]), pk2(b[2], b[3])};
    return __builtin_bit_cast(bf16x8, r);
}
DI bf16x8 tr_frag(LAS const char* p0, LAS const char* p1) {
    s16x4 a = __builtin_amdgcn_ds_read_tr16_b64_v4i16((LAS s16x4*)p0);
    s16x4 b = __builtin_amdgcn_ds_read_tr16_b64_v4i16((LAS s16x4*)p1);
    s16x8 c = __builtin_shufflevector(a, b, 0, 1, 2, 3, 4, 5, 6, 7);
    return __builtin_bit_cast(bf16x8, c);
}
template <int OFF> DI void dma1(const char* g, unsigned voff, unsigned ldsdst) {
    asm volatile("s_mov_b32 m0, %2\n\ts_nop 0\n\tglobal_load_lds_dwordx4 %0, %1 offset:%3" :: "v"(voff), "s"(g), "s"(ldsdst), "i"(OFF) : "memory");
}
DI void dma2(const char* g, unsigned voff, unsigned ldsdst) {
    asm volatile("s_mov_b32 m0, %2\n\ts_nop 0\n\tglobal_load_lds_dwordx4 %0, %1 sc1\n\tglobal_load_lds_dwordx4 %0, %1 offset:1024 sc1"
                 :: "v"(voff), "s"(g), "s"(ldsdst) : "memory");
}
DI void dma4(const char* g, unsigned voff, unsigned ldsdst) {
    asm volatile("s_mov_b32 m0, %2\n\ts_nop 0\n\tglobal_load_lds_dwordx4 %0, %1\n\tglobal_load_lds_dwordx4 %0, %1 offset:1024\n\t"
                 "global_load_lds_dwordx4 %0, %1 offset:2048\n\tglobal_load_lds_dwordx4 %0, %1 offset:3072"
                 :: "v"(voff), "s"(g), "s"(ldsdst) : "memory");
}
DI void pf_lines(const char* g, unsigned voff, unsigned ldspad, unsigned long long mask) {
    asm volatile("s_mov_b32 m0, %2\n\ts_mov_b64 exec, %3\n\tglobal_load_lds_dword %0, %1\n\ts_mov_b64 exec, -1" :: "v"(voff), "s"(g), "s"(ldspad), "s"(mask) : "memory");
}
typedef __amdgpu_buffer_rsrc_t rsrc_t;
DI rsrc_t mkrsrc(const void* p, unsigned bytes) { return __builtin_amdgcn_make_buffer_rsrc((void*)p, 0, bytes, 0x00020000); }
template <class T> DI void stw(const T& v, rsrc_t r, unsigned voff, unsigned soff) { __builtin_amdgcn_raw_buffer_store_b128(__builtin_bit_cast(u32x4, v), r, voff + soff, 0, 16); }
#define OPAQUE(x) asm volatile("" : "+v"(x))
#define OPAQUE_S(x) asm volatile("" : "+s"(x))
DI float fmx(float a, float b) { return __builtin_elementwise_maximum(a, b); }
DI void swap16(float& a, float& b) { auto r = __builtin_amdgcn_permlane16_swap(__float_as_uint(a), __float_as_uint(b), false, false); a = __uint_as_float(r[0]); b = __uint_as_float(r[1]); }
DI void swap32(float& a, float& b) { auto r = __builtin_amdgcn_permlane32_swap(__float_as_uint(a), __float_as_uint(b), false, false); a = __uint_as_float(r[0]); b = __uint_as_float(r[1]); }
DI void swap16u(unsigned& a, unsigned& b) { auto r = __builtin_amdgcn_permlane16_swap(a, b, false, false); a = r[0]; b = r[1]; }
template <bool MAX> DI void red2(float& a, float& b) {
    swap16(a, b); float v = MAX ? fmx(a, b) : a + b;
    float u = v; swap32(v, u); v = MAX ? fmx(v, u) : v + u;
    a = v; b = v; swap16(a, b);
}

constexpr int G1_SLOT = 36864;
constexpr int G1_WPHI = 149504;
constexpr int G1_PF = G1_WPHI + 4096;
constexpr int G1_ARR = G1_PF + 256;
constexpr int G1_LDS = G1_ARR + 16;
DI void hedgehog4(const f32x4 (&p)[4][2], bf16x8 (&pos)[4], bf16x8 (&neg)[4]) {
    const float L2E = 1.44269504088896340736f;
    float mx[4], nm[4];
#pragma unroll
    for (int m = 0; m < 4; ++m) {
        mx[m] = fmx(fmx(fmx(fmx(p[m][0][0], p[m][0][1]), p[m][0][2]), fmx(fmx(p[m][0][3], p[m][1][0]), p[m][1][1])), fmx(p[m][1][2], p[m][1][3]));
        nm[m] = fmx(fmx(fmx(fmx(-p[m][0][0], -p[m][0][1]), -p[m][0][2]), fmx(fmx(-p[m][0][3], -p[m][1][0]), -p[m][1][1])), fmx(-p[m][1][2], -p[m][1][3]));
    }
#pragma unroll
    for (int m = 0; m < 4; ++m) red2<true>(mx[m], nm[m]);
    f32x2 ep[4][4], en[4][4];
    float sp[4], sn[4];
#pragma unroll
    for (int m = 0; m < 4; ++m) {
        const float cp = -mx[m] * L2E, cn = -nm[m] * L2E;
#pragma unroll
        for (int i = 0; i < 4; ++i) {
            const f32x2 x = {p[m][i >> 1][2 * (i & 1)], p[m][i >> 1][2 * (i & 1) + 1]};
            const f32x2 a = __builtin_elementwise_fma(x, (f32x2){L2E, L2E}, (f32x2){cp, cp}), b = __builtin_elementwise_fma(x, (f32x2){-L2E, -L2E}, (f32x2){cn, cn});
            ep[m][i] = (f32x2){__builtin_amdgcn_exp2f(a[0]), __builtin_amdgcn_exp2f(a[1])};
            en[m][i] = (f32x2){__builtin_amdgcn_exp2f(b[0]), __builtin_amdgcn_exp2f(b[1])};
        }
        const f32x2 tp = (ep[m][0] + ep[m][1]) + (ep[m][2] + ep[m][3]), tn = (en[m][0] + en[m][1]) + (en[m][2] + en[m][3]);
        sp[m] = tp[0] + tp[1]; sn[m] = tn[0] + tn[1];
    }
#pragma unroll
    for (int m = 0; m < 4; ++m) red2<false>(sp[m], sn[m]);
#pragma unroll
    for (int m = 0; m < 4; ++m) {
        const float rp = __builtin_amdgcn_rcpf(sp[m]), rn = __builtin_amdgcn_rcpf(sn[m]);
        u32x4 a, b;
#pragma unroll
        for (int i = 0; i < 4; ++i) { const f32x2 x = ep[m][i] * rp, y = en[m][i] * rn; a[i] = pk2(x[0], x[1]); b[i] = pk2(y[0], y[1]); }
        pos[m] = __builtin_bit_cast(bf16x8, a); neg[m] = __builtin_bit_cast(bf16x8, b);
    }
}
DI u32x2 glu4(const f32x4& g, const f32x4& u) {
    u32x2 r;
#pragma unroll
    for (int i = 0; i < 2; ++i) {
        const f32x2 t = (f32x2){g[2 * i], g[2 * i + 1]} * -1.44269504088896340736f;
        const f32x2 d = (f32x2){__builtin_amdgcn_exp2f(t[0]), __builtin_amdgcn_exp2f(t[1])} + 1.0f;
        const f32x2 h = (f32x2){u[2 * i], u[2 * i + 1]} * (f32x2){__builtin_amdgcn_rcpf(d[0]), __builtin_amdgcn_rcpf(d[1])};
        r[i] = pk2(h[0], h[1]);
    }
    return r;
}
DI u32x4 glu8(const f32x4& g0, const f32x4& u0, const f32x4& g1, const f32x4& u1) { const u32x2 a = glu4(g0, u0), b = glu4(g1, u1); return (u32x4){a[0], a[1], b[0], b[1]}; }

constexpr int G2_SLOT = 32768;
constexpr int G2_NS = 4;
constexpr int G2_KV = G2_NS * G2_SLOT;
static_assert(G2_KV + 8192 + 256 <= G1_WPHI, "");
#define G2_NA 36
constexpr int G2_NB = 72 - G2_NA, G2_NBH = G2_NB - 16;
static_assert(G2_NA % 4 == 0 && G2_NA >= 32 && G2_NA <= 36, "");
typedef GAS unsigned gu32;
DI void wait_tags(gu32* p, unsigned seen, unsigned tag) {
    if (__builtin_amdgcn_ballot_w64(seen != tag) != 0ull) {
        unsigned spins = 0;
        while (__builtin_amdgcn_ballot_w64(__hip_atomic_load(p, __ATOMIC_RELAXED, __HIP_MEMORY_SCOPE_AGENT) != tag) != 0ull && ++spins < (1u << 18))
            __builtin_amdgcn_s_sleep(1);
    }
}
DI void arrive(LAS unsigned* arr, gu32* word, unsigned tag) {
    const int l = threadIdx.x & 63;
    unsigned old = 0;
    if (l == 0) old = __hip_atomic_fetch_add(arr, 1u, __ATOMIC_RELAXED, __HIP_MEMORY_SCOPE_WORKGROUP);
    if (__builtin_amdgcn_readfirstlane(old) == 7u && l < 4) __hip_atomic_store(word + 64 * l, tag, __ATOMIC_RELAXED, __HIP_MEMORY_SCOPE_AGENT);
}
DI float wsum(float v) {
    v += __builtin_amdgcn_update_dpp(0.f, v, 0xb1, 0xf, 0xf, true);
    v += __builtin_amdgcn_update_dpp(0.f, v, 0x4e, 0xf, 0xf, true);
    v += __builtin_amdgcn_update_dpp(0.f, v, 0x141, 0xf, 0xf, true);
    v += __builtin_amdgcn_update_dpp(0.f, v, 0x140, 0xf, 0xf, true);
    float u = v; swap16(v, u); v += u;
    u = v; swap32(v, u); return v + u;
}

#define LP(a) ((LAS const char*)(uintptr_t)(a))
#define G1_STAGE_A(slot) do { dma2(aptr, avoff, adst + (slot) * G1_SLOT); aptr += NMB * 1024; OPAQUE_S(aptr); } while (0)
#define G1_STAGE_B(slot) do { \
        dma1<0>(bptr[0], bvoff[0], bdst[0] + (slot) * G1_SLOT); dma1<0>(bptr[1], bvoff[1], bdst[1] + (slot) * G1_SLOT); \
        if (grp == 0) dma1<0>(bptr[2], bvoff[2], bdst[2] + (slot) * G1_SLOT); \
        bptr[0] += bstep[0]; bptr[1] += bstep[1]; bptr[2] += bstep[2]; \
        OPAQUE_S(bptr[0]); OPAQUE_S(bptr[1]); OPAQUE_S(bptr[2]); } while (0)
#define G1_STAGE(slot) do { G1_STAGE_A(slot); G1_STAGE_B(slot); } while (0)
#define G1_WAIT() WAIT_V(8)

template <bool kvt>
DI void tail(const uint16_t* __restrict__ x, const uint16_t* __restrict__ wphi, const uint16_t* __restrict__ wdown, const uint16_t* __restrict__ wo, char* __restrict__ ws,
             uint16_t* __restrict__ out, LAS char* lds, const unsigned tag, const char* aptr, const char* bp0, const char* bp1, const char* bp2,
             unsigned bs0, unsigned bs1, unsigned bs2, const char* pw0, const char* pw1, const char* pw2) {
    int tid = threadIdx.x;
    OPAQUE(tid);
    const int l = tid & 63;
    const int w = __builtin_amdgcn_readfirstlane(tid >> 6);
    const int grp = w >> 2, wr = w & 3, wc = grp;
    const int bid = blockIdx.x, idx = bid >> 3, xcd = bid & 7;
    const int pm = idx >> 2;
    const int j = xcd * 2 + ((idx >> 1) & 1);
    const int F0 = kvt ? 2048 + 96 * j : 128 * j;
    gu32* cnt = (gu32*)(ws + WS_CNT);
    const int G = l >> 4, q4 = (l >> 2) & 3, p4 = l & 3;
    const unsigned lbase = (unsigned)(uintptr_t)lds;
    const rsrc_t rw = mkrsrc(ws, (unsigned)WS_END);
    const char* bptr[3] = {bp0, bp1, bp2};
    const unsigned bstep[3] = {bs0, bs1, bs2};
    unsigned bvoff[3];
#pragma unroll
    for (int i = 0; i < 3; ++i) bvoff[i] = (l >> 2) * (bstep[i] >> 5) + ((((l >> 1) & 1) ^ ((l >> 5) & 1)) * 32) + (l & 1) * 16;
    const unsigned avoff = l * 16, adst = lbase + 2 * w * 1024;
    unsigned bdst[3];
#pragma unroll
    for (int i = 0; i < 3; ++i) bdst[i] = lbase + 16384 + (grp == 0 ? 3 * w + i : 12 + 2 * (w - 4) + (i < 2 ? i : 0)) * 1024;

    const unsigned lp0 = G * 512 + q4 * 64 + ((G & 1) * 32) + p4 * 8;
    const unsigned lp1 = lp0 ^ 32;
    unsigned rA = lbase + wr * 4096 + l * 16;
    unsigned rB0 = lbase + 16384 + 5 * wc * 2048 + lp0;
    unsigned rB1 = lbase + 16384 + 5 * wc * 2048 + lp1;
    unsigned rB8 = kvt ? lbase + 16384 + 2 * 2048 + (wc ? lp1 : lp0) : rB0 + 2 * 2048;
    unsigned rB9 = kvt ? lbase + 16384 + 7 * 2048 + (wc ? lp1 : lp0) : rB1 + 2 * 2048;
#define G1_MOVE(d) do { rA += (d); rB0 += (d); rB1 += (d); rB8 += (d); rB9 += (d); \
        OPAQUE(rA); OPAQUE(rB0); OPAQUE(rB1); OPAQUE(rB8); OPAQUE(rB9); } while (0)

    f32x4 acc[4][10];
#pragma unroll
    for (int m = 0; m < 4; ++m)
#pragma unroll
        for (int n = 0; n < 10; ++n) acc[m][n] = (f32x4){0.f, 0.f, 0.f, 0.f};
    bf16x8 fa[4], fb[10];

#define G1_READ(so) do { \
        _Pragma("unroll") for (int m = 0; m < 4; ++m) fa[m] = *(LAS const bf16x8*)(LP(rA) + (so) + m * 1024); \
        _Pragma("unroll") for (int u = 0; u < 5; ++u) if (u != 2) { \
            fb[2 * u] = tr_frag(LP(rB0) + (so) + u * 2048, LP(rB0) + (so) + u * 2048 + 256); \
            fb[2 * u + 1] = tr_frag(LP(rB1) + (so) + u * 2048, LP(rB1) + (so) + u * 2048 + 256); } \
        fb[4] = tr_frag(LP(rB8) + (so), LP(rB8) + (so) + 256); \
        fb[5] = tr_frag(LP(rB9) + (so), LP(rB9) + (so) + 256); } while (0)
#define G1_MMA() do { __builtin_amdgcn_s_setprio(1); \
        _Pragma("unroll") for (int m = 0; m < 4; ++m) _Pragma("unroll") for (int n_ = 0; n_ < 10; ++n_) { const int n = (m & 1) ? 9 - n_ : n_; \
            if (kvt && (n == 2 || n == 3)) MFMA16_ACC(acc[m][n], fa[m], fb[n]); else MFMA16_ACC(acc[m][n], fb[n], fa[m]); } \
        __builtin_amdgcn_s_setprio(0); } while (0)
#define G1_PHASE(so, STG, VW) do { G1_READ(so); STG; WAIT_L0(); VW; SCHED(); BAR(); SCHED(); G1_MMA(); SCHED(); BAR(); SCHED(); } while (0)

    G1_MOVE(0);
#pragma unroll
    for (int m = 0; m < 4; ++m)
#pragma unroll
        for (int n = 0; n < 10; ++n) OPAQUE(acc[m][n]);
    SCHED();
    wait_tags(cnt + 64 * (C_LN + pm * 8 + w) + (l & 3), tag - 1u, tag);
    STAMP(0, 3);
    G1_STAGE_A(0); G1_STAGE_A(1);
    WAIT_V(2);
    BAR();
    G1_STAGE(2);
    if (grp == 1) dma1<0>((const char*)wphi + (size_t)(2 * j) * 2048 + wr * 1024, avoff, lbase + G1_WPHI + wr * 1024);
    STAMP(1, 1);
    if (grp == 1) BAR();
    SCHED();
    for (int it = 0; it < 7; ++it) {
        if (w == 0) STAMP(4, it);
        if (w >= 6) {
            const int c = bid * 2 + w - 6;
            const char* g; int lim;
            if (it < 4) { lim = 480; g = c < 144 ? pw0 + (size_t)(192 + it * 144 + c) * 8192 : (c < 312 ? pw1 - 144 * 8192 : pw2 - 312 * 8192) + (size_t)(224 + it * 168 + c) * 8192; }
            else { const int u = (it - 4) * 384 + c; lim = 384; g = u < 896 ? (const char*)wdown + (size_t)u * 8192 : (const char*)wo + (size_t)(u - 896) * 8192; }
            if (c < lim) pf_lines(g, avoff * 8, lbase + G1_PF, ~0ull);
        }
        G1_PHASE(0, G1_STAGE(3), G1_WAIT());
        G1_PHASE(G1_SLOT, G1_STAGE(0), G1_WAIT());
        G1_MOVE(2 * G1_SLOT);
        G1_PHASE(0, G1_STAGE(1), G1_WAIT());
        G1_PHASE(G1_SLOT, G1_STAGE(2), G1_WAIT());
        G1_MOVE(-2 * G1_SLOT);
    }
    if (w == 0) STAMP(4, 7);
    G1_PHASE(0, G1_STAGE(3), G1_WAIT());
    G1_PHASE(G1_SLOT, (void)0, WAIT_V(4));
    G1_MOVE(2 * G1_SLOT);
    G1_PHASE(0, (void)0, WAIT_V(0));
    G1_READ(G1_SLOT); WAIT_L0(); SCHED(); BAR(); SCHED(); G1_MMA(); SCHED();
    if (grp == 0) BAR();
    asm volatile("s_nop 7\n\ts_nop 7" ::: "memory");
    SCHED();
    STAMP(1, 2);
    if (kvt) STAMP(2, 0);

    constexpr bool tb = !kvt;
    const int t16 = idx >> 1;
    const int pm2 = (xcd >> 1) * 4 + (t16 & 3), pn = (xcd & 1) * 4 + (t16 >> 2);
    const int tile = pm2 * 8 + pn;
    const int prep = 4 * (pm2 >> 1) + (xcd & 1) * 2 + (wr & 1);
    constexpr int k0 = tb ? 0 : G2_NBH;
    const unsigned lo16 = l * 16;
    const bool isb = wr >= 2;
    const char* gptr = isb ? (const char*)(wdown + (size_t)(64 * k0 + 32 * (wr - 2)) * D_ + pn * 128) : ws + WS_A2 + ((size_t)(2 * k0 + wr) * NMB + pm2 * 8) * 1024;
    const char* gptr_o = isb ? (const char*)(wo + (size_t)(32 * (wr & 1)) * D_ + pn * 128) : ws + WS_A2 + ((size_t)(112 + wr) * NMB + pm2 * 8) * 1024;
    const unsigned gstep = isb ? 64 * D_ * 2 : 2 * NMB * 1024;
    unsigned voff[4];
#pragma unroll
    for (int i = 0; i < 4; ++i) {
        const int r = 4 * i + G;
        voff[i] = isb ? r * (D_ * 2) + ((((l & 15) >> 1) ^ (r & 7)) * 32) + (l & 1) * 16 - 1024 * i : lo16;
    }
    const unsigned ghalf = isb ? 16 * D_ * 2 : 4096;
    unsigned dst = lbase + wr * 8192;
    int tld = 0, slot = 0;
#define G2_DMA4_(g, d, SC) asm volatile("s_mov_b32 m0, %5\n\ts_nop 0\n\tglobal_load_lds_dwordx4 %0, %4" SC "\n\tglobal_load_lds_dwordx4 %1, %4 offset:1024" SC "\n\t" \
            "global_load_lds_dwordx4 %2, %4 offset:2048" SC "\n\tglobal_load_lds_dwordx4 %3, %4 offset:3072" SC \
            :: "v"(voff[0]), "v"(voff[1]), "v"(voff[2]), "v"(voff[3]), "s"(g), "s"(d) : "memory")
#define G2_DMA4(g, d) G2_DMA4_(g, d, " sc1")
#define G2_NEXTSLOT() do { ++slot; const int wrap = slot == G2_NS; slot = wrap ? 0 : slot; dst += wrap ? (unsigned)(-(G2_NS - 1) * G2_SLOT) : (unsigned)G2_SLOT; } while (0)
#define G2_STAGE() do { \
        if (!isb) { G2_DMA4(gptr, dst); G2_DMA4(gptr + ghalf, dst + 4096); } else { G2_DMA4_(gptr, dst, ""); G2_DMA4_(gptr + ghalf, dst + 4096, ""); } \
        ++tld; gptr = (tb && tld == G2_NBH) ? gptr_o : gptr + gstep; G2_NEXTSLOT(); } while (0)
#define G2_PRUN(n) do { _Pragma("unroll 1") for (int t_ = 0; t_ < (n); ++t_) { WAIT_V(16); BAR(); G2_STAGE(); } } while (0)
#define POLL(dst_, p_) asm volatile("global_load_dword %0, %1, off sc1" : "=v"(dst_) : "v"(p_) : "memory")
    const size_t off0 = (size_t)(pm2 * 128 + (wr >> 1) * 64 + (l & 15)) * D_ + pn * 128 + (wr & 1) * 64 + (G & 1) * 16 + (G >> 1) * 8;

    const int fq = l >> 4, fr = l & 15;
    const int hh = 2 * j + wc;
    const int mb0 = pm * 16 + wr * 4;
    if (w >= 6) { G2_STAGE(); G2_STAGE(); }
    const f32x4 z4 = {0.f, 0.f, 0.f, 0.f};
    const unsigned hb = (unsigned)WS_A2 + (((kvt ? F0 + 32 * wc : F0 + 64 * wc + 32) >> 5) * NMB + mb0) * 1024;
    f32x4 pf[4][2];
    bf16x8 pos[4], neg[4];
    u32x4 xv[4][2];
#define G1_LOGITS() do { bf16x8 wp[2]; \
        _Pragma("unroll") for (int c = 0; c < 2; ++c) { LAS const char* p = lds + G1_WPHI + wc * 2048 + (4 * G + q4) * 64 + c * 32 + p4 * 8; wp[c] = tr_frag(p, p + 1024); } \
        _Pragma("unroll") for (int m = 0; m < 4; ++m) { const bf16x8 qf = pack8(acc[m][0], acc[m][1]); pf[m][0] = MFMA16(wp[0], qf, z4); pf[m][1] = MFMA16(wp[1], qf, z4); } } while (0)
    __builtin_amdgcn_s_setprio(1);
    if constexpr (!kvt) {
#pragma unroll
        for (int m = 0; m < 4; ++m) {
            stw(glu8(acc[m][2], acc[m][4], acc[m][3], acc[m][5]), rw, lo16 + m * 1024, hb - NMB * 1024);
            stw(glu8(acc[m][6], acc[m][8], acc[m][7], acc[m][9]), rw, lo16 + m * 1024, hb);
        }
        SCHED();
        G1_LOGITS();
        SCHED();
        WAIT_V(0); arrive((LAS unsigned*)(lds + G1_ARR), cnt + 64 * (C_HQ + 4 * pm) + j, tag);
        __builtin_amdgcn_s_setprio(0);
        SCHED();
        STAMP(1, 5);
        if (w == 4 || w == 5) { wait_tags(cnt + 64 * (C_HQ + prep) + (l & 15), tag - 1u, tag); STAMP(6, 0); G2_STAGE(); G2_STAGE(); }
        hedgehog4(pf, pos, neg);
    } else {
        G1_LOGITS();
        u32x4 i0 = {0u, 0u, 0u, 0u}, i1 = {0u, 0u, 0u, 0u};
        {
            const int d = fr - 4 * fq;
            const unsigned lo = (d == 0) ? 0x3f80u : (d == 1) ? 0x3f800000u : 0u;
            const unsigned hi = (d == 2) ? 0x3f80u : (d == 3) ? 0x3f800000u : 0u;
            i0[0] = lo; i0[1] = hi; i1[2] = lo; i1[3] = hi;
        }
        const bf16x8 I0 = __builtin_bit_cast(bf16x8, i0), I1 = __builtin_bit_cast(bf16x8, i1);
        constexpr int RED = 65536, ZRED = RED + 65536, HX = ZRED + 2048;
        static_assert(HX + 16384 <= G1_WPHI, "");
        bf16x8 V[2][2];
#pragma unroll
        for (int mp = 0; mp < 2; ++mp) { V[mp][0] = pack8(acc[2 * mp][2], acc[2 * mp + 1][2]); V[mp][1] = pack8(acc[2 * mp][3], acc[2 * mp + 1][3]); }
        SCHED();
        STAMP(2, 1);
        hedgehog4(pf, pos, neg);
        SCHED();
        STAMP(2, 2);
        const u32x4 o4 = {0x3f803f80u, 0x3f803f80u, 0x3f803f80u, 0x3f803f80u};
        const bf16x8 ONES = __builtin_bit_cast(bf16x8, o4);
        f32x4 T[4][2][2];
#pragma unroll
        for (int m = 0; m < 4; ++m) {
            T[m][0][0] = MFMA16(pos[m], I0, z4); T[m][0][1] = MFMA16(pos[m], I1, z4);
            T[m][1][0] = MFMA16(neg[m], I0, z4); T[m][1][1] = MFMA16(neg[m], I1, z4);
        }
#pragma unroll
        for (int m = 0; m < 4; ++m) *(LAS u32x2*)(lds + HX + (wr * 4 + m) * 1024 + lo16 + 8 * wc) = glu4(acc[m][4], acc[m][5]);
        LAS char* red = lds + RED + (wc * 4 + wr) * 8192 + lo16;
#pragma unroll
        for (int s = 0; s < 2; ++s)
#pragma unroll
            for (int cc = 0; cc < 2; ++cc) {
                const bf16x8 P0 = pack8(T[0][s][cc], T[1][s][cc]), P1 = pack8(T[2][s][cc], T[3][s][cc]);
                const f32x4 k0_ = MFMA16(P1, V[1][0], MFMA16(P0, V[0][0], z4));
                const f32x4 k1_ = MFMA16(P1, V[1][1], MFMA16(P0, V[0][1], z4));
                const f32x4 zz = MFMA16(P1, ONES, MFMA16(P0, ONES, z4));
                stw(glu8(acc[2 * s + cc][6], acc[2 * s + cc][8], acc[2 * s + cc][7], acc[2 * s + cc][9]), rw, lo16 + (2 * s + cc) * 1024, hb);
                *(LAS f32x4*)(red + ((s * 2 + 0) * 2 + cc) * 1024) = k0_;
                *(LAS f32x4*)(red + ((s * 2 + 1) * 2 + cc) * 1024) = k1_;
                *(LAS f32x4*)(lds + ZRED + (wc * 4 + wr) * 256 + (s * 32 + cc * 16 + 4 * fq) * 4) = zz;
            }
        SCHED();
        if (w < 4) {
#pragma unroll
            for (int m = 0; m < 4; ++m) { xv[m][0] = *(const u32x4*)(x + off0 + m * 16 * D_); xv[m][1] = *(const u32x4*)(x + off0 + m * 16 * D_ + 32); }
        } else if (w < 6) { wait_tags(cnt + 64 * (C_HQ + prep) + (l & 15), tag - 1u, tag); G2_STAGE(); G2_STAGE(); }
        WAIT_L0(); SCHED();
        STAMP(2, 3);
        BAR(); SCHED();
        STAMP(2, 4);
        const unsigned kvo = (unsigned)WS_KVP + (pm * 32 + hh) * KVP_SZ;
        f32x4 sum[2];
#pragma unroll
        for (int i = 0; i < 2; ++i) {
            LAS const char* p = lds + RED + wc * 32768 + (2 * wr + i) * 1024 + lo16;
            sum[i] = (*(LAS const f32x4*)p + *(LAS const f32x4*)(p + 8192)) + (*(LAS const f32x4*)(p + 16384) + *(LAS const f32x4*)(p + 24576));
            stw(*(LAS const u32x4*)(lds + HX + (wr * 4 + 2 * wc + i) * 1024 + lo16), rw, lo16 + i * 1024, (unsigned)WS_A2 + (((F0 + 64) >> 5) * NMB + mb0 + 2 * wc) * 1024);
        }
        stw(pack8(sum[0], sum[1]), rw, lo16, kvo + wr * 1024);
        {
            LAS const char* p = lds + ZRED + wc * 1024 + (l & 15) * 16;
            const f32x4 zs = (*(LAS const f32x4*)p + *(LAS const f32x4*)(p + 256)) + (*(LAS const f32x4*)(p + 512) + *(LAS const f32x4*)(p + 768));
            stw(zs, rw, (wr == 0 && l < 16) ? lo16 : 0x80000000u, kvo + 4096);
        }
        __builtin_amdgcn_s_setprio(0);
        WAIT_L0();
    }
    SCHED();
    STAMP(1, 6);

#define G2_OCOMP() do { \
        bf16x8 kf[2][2], zf[2]; \
        _Pragma("unroll") for (int s = 0; s < 2; ++s) { \
            kf[s][0] = *(LAS const bf16x8*)(lds + G2_KV + wc * 4096 + (s * 2) * 1024 + lo16); kf[s][1] = *(LAS const bf16x8*)(lds + G2_KV + wc * 4096 + (s * 2 + 1) * 1024 + lo16); \
            const u32x2 za = *(LAS const u32x2*)(lds + G2_KV + 8192 + wc * 128 + s * 64 + G * 8), zb = *(LAS const u32x2*)(lds + G2_KV + 8192 + wc * 128 + s * 64 + 32 + G * 8); \
            zf[s] = __builtin_bit_cast(bf16x8, (u32x4){za[0], za[1], zb[0], zb[1]}); } \
        _Pragma("unroll") for (int m = 0; m < 4; ++m) { \
            const f32x4 dn = MFMA16(zf[1], neg[m], MFMA16(zf[0], pos[m], z4)); \
            const f32x4 n0 = MFMA16(kf[1][0], neg[m], MFMA16(kf[0][0], pos[m], z4)), n1 = MFMA16(kf[1][1], neg[m], MFMA16(kf[0][1], pos[m], z4)); \
            const float rd = __builtin_amdgcn_rcpf(dn[0] + 1e-6f); \
            stw(pack8(n0 * rd, n1 * rd), rw, lo16 + m * 1024, (unsigned)WS_A2 + ((112 + hh) * NMB + mb0) * 1024); } \
        WAIT_L0(); SCHED(); } while (0)

    if (w >= 4) {
        unsigned seen;
        WAIT_V(8);
        STAMP(5 + tb, 1);
        BAR(); G2_STAGE();
        WAIT_V(8); BAR();
        if (!tb && w == 7 && l < 8) __hip_atomic_store(l < 4 ? cnt + 64 * (C_KV + 4 * j + l) + pm : cnt + 64 * (C_HK + 4 * pm + l - 4) + j, tag, __ATOMIC_RELAXED, __HIP_MEMORY_SCOPE_AGENT);
        G2_STAGE(); G2_STAGE();
        if constexpr (!tb) {
            constexpr int UK = 32 - G2_NBH - 4;
            G2_PRUN(UK - 3);
            POLL(seen, cnt + 64 * (C_HK + prep) + (l & 15));
            G2_PRUN(2);
            asm volatile("s_waitcnt vmcnt(16)" : "+v"(seen) :: "memory");
            if (!isb) wait_tags(cnt + 64 * (C_HK + prep) + (l & 15), seen, tag);
            BAR(); G2_STAGE();
            G2_PRUN(G2_NA - 5 - UK);
            WAIT_V(16); BAR(); WAIT_V(8); BAR(); WAIT_V(0); BAR();
        } else {
            G2_PRUN(3);
            POLL(seen, cnt + 64 * (C_KV + 4 * j + wr) + (l & 7));
            G2_PRUN(2);
            asm volatile("s_waitcnt vmcnt(16)" : "+v"(seen) :: "memory");
            wait_tags(cnt + 64 * (C_KV + 4 * j + wr) + (l & 7), seen, tag);
            u32x4 t[2][8]; float zt[8];
            const char* kvs = ws + WS_KVP + (size_t)(2 * j + (wr >> 1)) * KVP_SZ;
            const unsigned kvv = (wr & 1) * 2048 + lo16, zv = 4096 + l * 4;
#define G2_KVLD(i) do { _Pragma("unroll") for (int k = 0; k < 8; ++k) \
                asm volatile("global_load_dwordx4 %0, %1, %2 offset:%3 sc1" : "=v"(t[i][k]) : "v"(kvv), "s"(kvs + (size_t)k * 32 * KVP_SZ), "i"((i) * 1024) : "memory"); \
            _Pragma("unroll") for (int k = 4 * (i); k < 4 * (i) + 4; ++k) \
                asm volatile("global_load_dword %0, %1, %2 sc1" : "=v"(zt[k]) : "v"(zv), "s"(kvs + (size_t)k * 32 * KVP_SZ) : "memory"); } while (0)
            BAR(); G2_STAGE(); G2_KVLD(0);
            WAIT_V(28); BAR(); G2_STAGE(); G2_KVLD(1);
            WAIT_V(40); BAR(); G2_STAGE();
            WAIT_V(40); BAR(); G2_STAGE();
            WAIT_V(28); BAR(); G2_STAGE();
            WAIT_V(16); SCHED();
#pragma unroll
            for (int i = 0; i < 2; ++i) {
                asm volatile("" : "+v"(t[i][0]), "+v"(t[i][1]), "+v"(t[i][2]), "+v"(t[i][3]), "+v"(t[i][4]), "+v"(t[i][5]), "+v"(t[i][6]), "+v"(t[i][7]),
                             "+v"(zt[4 * i]), "+v"(zt[4 * i + 1]), "+v"(zt[4 * i + 2]), "+v"(zt[4 * i + 3]));
                u32x4 r;
#pragma unroll
                for (int e = 0; e < 4; ++e) {
                    float lo = 0.f, hi = 0.f;
#pragma unroll
                    for (int k = 0; k < 8; ++k) { lo += bflo(t[i][k][e]); hi += bfhi(t[i][k][e]); }
                    r[e] = pk2(lo, hi);
                }
                *(LAS u32x4*)(lds + G2_KV + (wr >> 1) * 4096 + (2 * (wr & 1) + i) * 1024 + lo16) = r;
            }
            const float z = ((zt[0] + zt[1]) + (zt[2] + zt[3])) + ((zt[4] + zt[5]) + (zt[6] + zt[7]));
            *(LAS unsigned short*)(lds + G2_KV + 8192 + (wr >> 1) * 128 + l * 2) = (unsigned short)pk2(z, z);
            WAIT_L0(); SCHED();
            BAR(); G2_STAGE();
            G2_OCOMP();
            G2_PRUN(G2_NB - 34);
            gu32* f0 = cnt + 64 * (C_O + prep) + (l & 15);
            POLL(seen, f0);
            G2_PRUN(2);
            asm volatile("s_waitcnt vmcnt(16)" : "+v"(seen) :: "memory");
            STAMP(6, 2);
            if (!isb) wait_tags(f0, seen, tag);
            STAMP(6, 3);
            BAR(); G2_STAGE();
            G2_PRUN(15);
            gu32* f1 = cnt + 64 * (C_P + tile);
            WAIT_V(16); BAR(); WAIT_V(8); BAR();
            POLL(seen, f1);
            WAIT_V(1); BAR();
            asm volatile("s_waitcnt vmcnt(0)" : "+v"(seen) :: "memory");
            STAMP(6, 4);
            wait_tags(f1, seen, tag);
            STAMP(6, 5);
            char* part = ws + WS_PART + (size_t)tile * 32768;
#pragma unroll
            for (int i = 0; i < 4; ++i) voff[i] = lo16;
            G2_DMA4(part + wr * 8192, lbase + wr * 8192); G2_DMA4(part + wr * 8192 + 4096, lbase + wr * 8192 + 4096);
            WAIT_V(0);
            BAR();
        }
        STAMP(3, 7);
        return;
    }
    const int cr = w >> 1, cc2 = w & 1;
    f32x4 ac2[4][4];
#pragma unroll
    for (int m = 0; m < 4; ++m)
#pragma unroll
        for (int i = 0; i < 2; ++i) {
            if constexpr (tb) { ac2[m][2 * i] = z4; ac2[m][2 * i + 1] = z4; }
            else {
                unsigned d0 = xv[m][i][0], d1 = xv[m][i][1], d2 = xv[m][i][2], d3 = xv[m][i][3];
                swap16u(d0, d2); swap16u(d1, d3);
                ac2[m][2 * i] = (f32x4){bflo(d0), bfhi(d0), bflo(d1), bfhi(d1)}; ac2[m][2 * i + 1] = (f32x4){bflo(d2), bfhi(d2), bflo(d3), bfhi(d3)};
            }
        }
    const int r8 = 4 * (G & 1) + q4;
    unsigned sA = lbase + cr * 4096 + lo16;
    unsigned sB[4];
#pragma unroll
    for (int n = 0; n < 4; ++n) sB[n] = lbase + 16384 + (4 * G + q4) * 256 + (((4 * cc2 + n) ^ r8) * 32) + p4 * 8;
#define G2_MOVE(d) do { sA += (d); sB[0] += (d); sB[1] += (d); sB[2] += (d); sB[3] += (d); OPAQUE(sA); OPAQUE(sB[0]); OPAQUE(sB[1]); OPAQUE(sB[2]); OPAQUE(sB[3]); } while (0)
    bf16x8 ga[2][2][4], gb[2][2][4];
#define G2_RD_A(buf, so, kk, m) ga[buf][kk][m] = *(LAS const bf16x8*)(LP(sA) + (so) + (kk) * 8192 + (m) * 1024)
#define G2_RD_B(buf, so, kk, n) gb[buf][kk][n] = tr_frag(LP(sB[n]) + (so) + (kk) * 8192, LP(sB[n]) + (so) + (kk) * 8192 + 4096)
#define G2_READ(buf, so) do { _Pragma("unroll") for (int kk = 0; kk < 2; ++kk) _Pragma("unroll") for (int i = 0; i < 4; ++i) { G2_RD_B(buf, so, kk, i); G2_RD_A(buf, so, kk, i); } } while (0)
#define G2_STEP(cur, so, RD) do { if (RD) BAR(); SCHED(); \
        _Pragma("unroll") for (int kk = 0; kk < 2; ++kk) _Pragma("unroll") for (int m = 0; m < 4; ++m) _Pragma("unroll") for (int n_ = 0; n_ < 4; ++n_) { \
            const int n = (m & 1) ? 3 - n_ : n_; \
            MFMA16_ACC(ac2[m][n], gb[cur][kk][n], ga[cur][kk][m]); \
            if ((RD) && kk == 0) { const int i_ = m * 4 + n_; if (i_ & 1) G2_RD_A((cur) ^ 1, so, i_ >> 3, (i_ >> 1) & 3); else G2_RD_B((cur) ^ 1, so, i_ >> 3, (i_ >> 1) & 3); } \
            SCHED(); } \
        WAIT_L0(); SCHED(); } while (0)
    unsigned mv = 2 * G2_SLOT;
#define G2_RUN(n) do { _Pragma("unroll 1") for (int i_ = 0; i_ < (n); ++i_) { G2_STEP(0, G2_SLOT, 1); G2_MOVE(mv); mv = -mv; G2_STEP(1, 0, 1); } } while (0)

    G2_MOVE(0);
    BAR(); SCHED();
    G2_READ(0, 0);
    WAIT_L0(); SCHED();
    STAMP(3, 1);
    if constexpr (!tb) WAIT_V(0);
    if constexpr (tb) {
        G2_RUN(6);
        G2_OCOMP();
        G2_RUN(1);
        WAIT_V(0); SCHED();
        G2_RUN(1);
        if (tid < 4) __hip_atomic_store(cnt + 64 * (C_O + 4 * pm + tid) + j, tag, __ATOMIC_RELAXED, __HIP_MEMORY_SCOPE_AGENT);
        SCHED();
        G2_RUN(G2_NB / 2 - 9);
    } else {
        G2_RUN(G2_NA / 2 - 1);
    }
    G2_STEP(0, G2_SLOT, 1);
    G2_STEP(1, 0, 0);
    asm volatile("s_nop 7\n\ts_nop 7" ::: "memory");
    SCHED();
    if constexpr (!tb) {
        STAMP(3, 2);
#pragma unroll
        for (int m = 0; m < 4; ++m)
#pragma unroll
            for (int i = 0; i < 2; ++i) stw(pack8(ac2[m][2 * i], ac2[m][2 * i + 1]), rw, lo16 + (m & 1) * 2048 + i * 1024, (unsigned)WS_PART + tile * 32768 + w * 8192 + (m >> 1) * 4096);
        WAIT_V(0); BAR();
        if (tid == 0) __hip_atomic_store(cnt + 64 * (C_P + tile), tag, __ATOMIC_RELAXED, __HIP_MEMORY_SCOPE_AGENT);
        STAMP(3, 6);
    } else {
        STAMP(3, 4);
        BAR(); SCHED();
#pragma unroll
        for (int m = 0; m < 4; ++m)
#pragma unroll
            for (int i = 0; i < 2; ++i) {
                const u32x4 pp = *(LAS const u32x4*)(lds + w * 8192 + (m * 2 + i) * 1024 + lo16);
                const f32x4 r0 = ac2[m][2 * i] + (f32x4){bflo(pp[0]), bfhi(pp[0]), bflo(pp[1]), bfhi(pp[1])};
                const f32x4 r1 = ac2[m][2 * i + 1] + (f32x4){bflo(pp[2]), bfhi(pp[2]), bflo(pp[3]), bfhi(pp[3])};
                unsigned d0 = pk2(r0[0], r0[1]), d1 = pk2(r0[2], r0[3]), d2 = pk2(r1[0], r1[1]), d3 = pk2(r1[2], r1[3]);
                swap16u(d0, d2); swap16u(d1, d3);
                *(u32x4*)(out + off0 + m * 16 * D_ + i * 32) = (u32x4){d0, d1, d2, d3};
            }
        if (bid == 0 && tid == 0) __hip_atomic_store(cnt + 64 * C_E, tag, __ATOMIC_RELAXED, __HIP_MEMORY_SCOPE_AGENT);
        STAMP(3, 5);
    }
}

__global__ __launch_bounds__(512, 2) void k_main(const uint16_t* __restrict__ x, const uint16_t* __restrict__ wqkv, const uint16_t* __restrict__ wphi,
                                                const uint16_t* __restrict__ wgate, const uint16_t* __restrict__ wup, const uint16_t* __restrict__ wdown,
                                                const uint16_t* __restrict__ wo, char* __restrict__ ws, uint16_t* __restrict__ out) {
    __shared__ __attribute__((aligned(16))) char smem_[G1_LDS];
    LAS char* lds = (LAS char*)smem_;
    const int tid = threadIdx.x, l = tid & 63;
    const int w = __builtin_amdgcn_readfirstlane(tid >> 6);
    const int grp = w >> 2;
    const int wr = w & 3, wc = grp;
    const int bid = blockIdx.x, idx = bid >> 3, xcd = bid & 7;
    const int pm = idx >> 2;
    const int j = xcd * 2 + ((idx >> 1) & 1);
    const bool kvt = (idx & 1) != 0;
    const int F0 = kvt ? 2048 + 96 * j : 128 * j;
    gu32* cnt = (gu32*)(ws + WS_CNT);
    STAMP(0, 0);
    const int lnslot = xcd * 4 + (idx & 3);
    u32x4 xa, xb; unsigned tag;
    {
        const char* xr = (const char*)x + (size_t)(pm * 256 + lnslot * 8 + w) * 2048;
        asm volatile("global_load_dwordx4 %0, %3, %4\n\tglobal_load_dwordx4 %1, %3, %4 offset:1024\n\tglobal_load_dword %2, %5, off sc1"
                     : "=&v"(xa), "=&v"(xb), "=&v"(tag) : "v"(l * 16), "s"(xr), "v"(cnt + 64 * C_E) : "memory");
    }

    const char* aptr = ws + WS_XN + (size_t)(pm * 16 + 2 * w) * 1024;
    const unsigned avoff = l * 16;
    const char* bptr[3]; unsigned bstep[3], bvoff[3], bdst[3];
#pragma unroll
    for (int i = 0; i < 3; ++i) {
        int t = grp == 0 ? 3 * w + i : 12 + 2 * (w - 4) + (i < 2 ? i : 0);
        const unsigned e = (unsigned)((kvt ? 0x491898502061204ull : 0xcb18a1412442040ull) >> (6 * (t >> 1))) & 63u;
        const unsigned mat = e & 3u, stride = mat == 0 ? 3 * D_ : F_;
        const uintptr_t base = mat == 0 ? (uintptr_t)wqkv : mat == 1 ? (uintptr_t)wgate : (uintptr_t)wup;
        bptr[i] = (const char*)(base + ((t & 1) * 16 * stride + (mat == 0 ? 64 * j : F0) + ((e >> 2) & 3u) * 1024 + (e >> 4) * 32) * 2u);
        bstep[i] = 64u * stride;
        bvoff[i] = (l >> 2) * stride * 2 + ((((l >> 1) & 1) ^ ((l >> 5) & 1)) * 32) + (l & 1) * 16;
        bdst[i] = (unsigned)(uintptr_t)lds + 16384 + t * 1024;
    }
    const unsigned lbase = (unsigned)(uintptr_t)lds;
    const unsigned adst = lbase + 2 * w * 1024;
    STAMP(1, 0);
    if (tid < 2) *(LAS unsigned*)(lds + G1_ARR + tid * 4) = 0u;
    G1_STAGE_B(0); G1_STAGE_B(1);
    if (grp == 0) WAIT_V(6); else WAIT_V(4);
    asm volatile("" : "+v"(xa), "+v"(xb), "+v"(tag) :: "memory");
    tag = __builtin_amdgcn_readfirstlane(tag) + 1u;
    const rsrc_t rw = mkrsrc(ws, (unsigned)WS_END);
    STAMP(0, 1);
    {
        float f[16];
#pragma unroll
        for (int i = 0; i < 4; ++i) { f[2 * i] = bflo(xa[i]); f[2 * i + 1] = bfhi(xa[i]); f[8 + 2 * i] = bflo(xb[i]); f[9 + 2 * i] = bfhi(xb[i]); }
        float sm = 0.f;
#pragma unroll
        for (int i = 0; i < 16; ++i) sm += f[i];
        const float mean = wsum(sm) * (1.0f / 1024.0f);
        float q = 0.f;
#pragma unroll
        for (int i = 0; i < 16; ++i) { const float d = f[i] - mean; q += d * d; }
        const float rs = __builtin_amdgcn_rcpf(bfr(__builtin_amdgcn_sqrtf(bfr(bfr(wsum(q) * (1.0f / 1024.0f)) + 1e-5f))));
        const float mb = bfr(mean);
#pragma unroll
        for (int i = 0; i < 8; ++i) { const unsigned d2 = pk2(f[2 * i] - mb, f[2 * i + 1] - mb); f[2 * i] = bflo(d2); f[2 * i + 1] = bfhi(d2); }
        LAS char* ex = lds + 3 * G1_SLOT;
        *(LAS u32x4*)(ex + l * 144 + w * 16) = (u32x4){pk2(f[0] * rs, f[1] * rs), pk2(f[2] * rs, f[3] * rs), pk2(f[4] * rs, f[5] * rs), pk2(f[6] * rs, f[7] * rs)};
        *(LAS u32x4*)(ex + (64 + l) * 144 + w * 16) = (u32x4){pk2(f[8] * rs, f[9] * rs), pk2(f[10] * rs, f[11] * rs), pk2(f[12] * rs, f[13] * rs), pk2(f[14] * rs, f[15] * rs)};
        WAIT_L0(); SCHED(); BAR(); SCHED();
#pragma unroll
        for (int i = 0; i < 2; ++i) {
            const int c = i * 64 + (tid >> 3), r = tid & 7;
            stw(*(LAS const u32x4*)(ex + c * 144 + r * 16), rw, ((c >> 2) * NMB * 1024) + ((c & 3) * 16 + r) * 16, (unsigned)WS_XN + (pm * 16 + (lnslot >> 1)) * 1024 + (lnslot & 1) * 128);
        }
        SCHED();
        WAIT_V(0);
        SCHED(); BAR(); SCHED();
        STAMP(0, 2);
        if (tid == 0) __hip_atomic_store(cnt + 64 * (C_LN + pm * 8 + (lnslot >> 2)) + (lnslot & 3), tag, __ATOMIC_RELAXED, __HIP_MEMORY_SCOPE_AGENT);
        SCHED();
    }
    if (kvt) tail<true>(x, wphi, wdown, wo, ws, out, lds, tag, aptr, bptr[0], bptr[1], bptr[2], bstep[0], bstep[1], bstep[2], (const char*)wqkv, (const char*)wgate, (const char*)wup);
    else tail<false>(x, wphi, wdown, wo, ws, out, lds, tag, aptr, bptr[0], bptr[1], bptr[2], bstep[0], bstep[1], bstep[2], (const char*)wqkv, (const char*)wgate, (const char*)wup);
}

extern "C" void kernel_launch(void* const* d_in, const int* in_sizes, int n_in, void* d_out, int out_size, void* d_ws, size_t ws_size,
                              hipStream_t stream) {
    const uint16_t* x = (const uint16_t*)d_in[0];
    const uint16_t* wqkv = (const uint16_t*)d_in[1];
    const uint16_t* wphi = (const uint16_t*)d_in[2];
    const uint16_t* wo = (const uint16_t*)d_in[3];
    const uint16_t* wgate = (const uint16_t*)d_in[5];
    const uint16_t* wup = (const uint16_t*)d_in[6];
    const uint16_t* wdown = (const uint16_t*)d_in[7];
    char* ws = (char*)d_ws;
    k_main<<<256, 512, 0, stream>>>(x, wqkv, wphi, wgate, wup, wdown, wo, ws, (uint16_t*)d_out);
}
```

```cpp
#include <hip/hip_runtime.h>
#include <hip/hip_bf16.h>
#include <stdint.h>

#define LAS __attribute__((address_space(3)))
#define GAS __attribute__((address_space(1)))
#define DI __device__ __forceinline__
typedef __bf16 bf16x8 __attribute__((ext_vector_type(8)));
typedef __bf16 bf16x2 __attribute__((ext_vector_type(2)));
typedef short s16x4 __attribute__((ext_vector_type(4)));
typedef short s16x8 __attribute__((ext_vector_type(8)));
typedef float f32x4 __attribute__((ext_vector_type(4)));
typedef float f32x2 __attribute__((ext_vector_type(2)));
typedef unsigned u32x4 __attribute__((ext_vector_type(4)));
typedef unsigned u32x2 __attribute__((ext_vector_type(2)));

constexpr int S_ = 2048, D_ = 1024, F_ = 3584, H_ = 32;
constexpr int NMB = S_ / 16;
constexpr size_t WS_XN = 0;
constexpr size_t WS_A2 = WS_XN + (size_t)32 * NMB * 1024;
constexpr size_t WS_KVP = WS_A2 + (size_t)144 * NMB * 1024;
constexpr int KVP_SZ = 4352;
constexpr size_t WS_PART = WS_KVP + (size_t)8 * 32 * KVP_SZ;
constexpr size_t WS_CNT = WS_PART + (size_t)128 * 32768;
constexpr int C_LN = 0, C_HQ = 64, C_HK = 96, C_O = 128, C_KV = 160, C_P = 224, C_E = 352, C_N = 353;
constexpr size_t WS_END = WS_CNT + C_N * 256;

#define STAMP(kid, i) do {} while (0)
#define MFMA16(a, b, c) __builtin_amdgcn_mfma_f32_16x16x32_bf16((a), (b), (c), 0, 0, 0)
#define WAIT_V(n) asm volatile("s_waitcnt vmcnt(" #n ")" ::: "memory")
#define WAIT_L(n) asm volatile("s_waitcnt lgkmcnt(" #n ")" ::: "memory")
#define WAIT_L0() __builtin_amdgcn_s_waitcnt(0xC07F)
#define MFMA16_ACC(acc, a, b) asm volatile("v_mfma_f32_16x16x32_bf16 %0, %1, %2, %0" : "+v"(acc) : "v"(a), "v"(b))
#define BAR() __builtin_amdgcn_s_barrier()
#define SCHED() __builtin_amdgcn_sched_barrier(0)

DI unsigned pk2(float lo, float hi) {
    f32x2 v = {lo, hi};
    return __builtin_bit_cast(unsigned, __builtin_convertvector(v, bf16x2));
}
DI float bflo(unsigned u) { return __uint_as_float(u << 16); }
DI float bfhi(unsigned u) { return __uint_as_float(u & 0xffff0000u); }
DI float bfr(float x) { return bflo(pk2(x, x)); }
DI bf16x8 pack8(const f32x4& a, const f32x4& b) {
    u32x4 r = {pk2(a[0], a[1]), pk2(a[2], a[3]), pk2(b[0], b[1]), pk2(b[2], b[3])};
    return __builtin_bit_cast(bf16x8, r);
}
DI bf16x8 tr_frag(LAS const char* p0, LAS const char* p1) {
    s16x4 a = __builtin_amdgcn_ds_read_tr16_b64_v4i16((LAS s16x4*)p0);
    s16x4 b = __builtin_amdgcn_ds_read_tr16_b64_v4i16((LAS s16x4*)p1);
    s16x8 c = __builtin_shufflevector(a, b, 0, 1, 2, 3, 4, 5, 6, 7);
    return __builtin_bit_cast(bf16x8, c);
}
template <int OFF> DI void dma1(const char* g, unsigned voff, unsigned ldsdst) {
    asm volatile("s_mov_b32 m0, %2\n\ts_nop 0\n\tglobal_load_lds_dwordx4 %0, %1 offset:%3" :: "v"(voff), "s"(g), "s"(ldsdst), "i"(OFF) : "memory");
}
DI void dma2(const char* g, unsigned voff, unsigned ldsdst) {
    asm volatile("s_mov_b32 m0, %2\n\ts_nop 0\n\tglobal_load_lds_dwordx4 %0, %1 sc1\n\tglobal_load_lds_dwordx4 %0, %1 offset:1024 sc1"
                 :: "v"(voff), "s"(g), "s"(ldsdst) : "memory");
}
DI void dma4(const char* g, unsigned voff, unsigned ldsdst) {
    asm volatile("s_mov_b32 m0, %2\n\ts_nop 0\n\tglobal_load_lds_dwordx4 %0, %1\n\tglobal_load_lds_dwordx4 %0, %1 offset:1024\n\t"
                 "global_load_lds_dwordx4 %0, %1 offset:2048\n\tglobal_load_lds_dwordx4 %0, %1 offset:3072"
                 :: "v"(voff), "s"(g), "s"(ldsdst) : "memory");
}
DI void pf_lines(const char* g, unsigned voff, unsigned ldspad, unsigned long long mask) {
    asm volatile("s_mov_b32 m0, %2\n\ts_mov_b64 exec, %3\n\tglobal_load_lds_dword %0, %1\n\ts_mov_b64 exec, -1" :: "v"(voff), "s"(g), "s"(ldspad), "s"(mask) : "memory");
}
typedef __amdgpu_buffer_rsrc_t rsrc_t;
DI rsrc_t mkrsrc(const void* p, unsigned bytes) { return __builtin_amdgcn_make_buffer_rsrc((void*)p, 0, bytes, 0x00020000); }
template <class T> DI void stw(const T& v, rsrc_t r, unsigned voff, unsigned soff) { __builtin_amdgcn_raw_buffer_store_b128(__builtin_bit_cast(u32x4, v), r, voff + soff, 0, 16); }
#define OPAQUE(x) asm volatile("" : "+v"(x))
#define OPAQUE_S(x) asm volatile("" : "+s"(x))
DI float fmx(float a, float b) { return __builtin_elementwise_maximum(a, b); }
DI void swap16(float& a, float& b) { auto r = __builtin_amdgcn_permlane16_swap(__float_as_uint(a), __float_as_uint(b), false, false); a = __uint_as_float(r[0]); b = __uint_as_float(r[1]); }
DI void swap32(float& a, float& b) { auto r = __builtin_amdgcn_permlane32_swap(__float_as_uint(a), __float_as_uint(b), false, false); a = __uint_as_float(r[0]); b = __uint_as_float(r[1]); }
DI void swap16u(unsigned& a, unsigned& b) { auto r = __builtin_amdgcn_permlane16_swap(a, b, false, false); a = r[0]; b = r[1]; }
template <bool MAX> DI void red2(float& a, float& b) {
    swap16(a, b); float v = MAX ? fmx(a, b) : a + b;
    float u = v; swap32(v, u); v = MAX ? fmx(v, u) : v + u;
    a = v; b = v; swap16(a, b);
}

constexpr int G1_SLOT = 36864;
constexpr int G1_WPHI = 149504;
constexpr int G1_PF = G1_WPHI + 4096;
constexpr int G1_ARR = G1_PF + 256;
constexpr int G1_LDS = G1_ARR + 16;
template <int M0, int M1> DI void hedgehog(f32x4 (&p)[4][2]) {
    const float L2E = 1.44269504088896340736f;
    float mx[4], nm[4];
#pragma unroll
    for (int m = M0; m < M1; ++m) {
        mx[m] = fmx(fmx(fmx(fmx(p[m][0][0], p[m][0][1]), p[m][0][2]), fmx(fmx(p[m][0][3], p[m][1][0]), p[m][1][1])), fmx(p[m][1][2], p[m][1][3]));
        nm[m] = fmx(fmx(fmx(fmx(-p[m][0][0], -p[m][0][1]), -p[m][0][2]), fmx(fmx(-p[m][0][3], -p[m][1][0]), -p[m][1][1])), fmx(-p[m][1][2], -p[m][1][3]));
    }
#pragma unroll
    for (int m = M0; m < M1; ++m) red2<true>(mx[m], nm[m]);
    f32x2 ep[4][4], en[4][4];
    float sp[4], sn[4];
#pragma unroll
    for (int m = M0; m < M1; ++m) {
        const float cp = -mx[m] * L2E, cn = -nm[m] * L2E;
#pragma unroll
        for (int i = 0; i < 4; ++i) {
            const f32x2 x = {p[m][i >> 1][2 * (i & 1)], p[m][i >> 1][2 * (i & 1) + 1]};
            const f32x2 a = __builtin_elementwise_fma(x, (f32x2){L2E, L2E}, (f32x2){cp, cp}), b = __builtin_elementwise_fma(x, (f32x2){-L2E, -L2E}, (f32x2){cn, cn});
            ep[m][i] = (f32x2){__builtin_amdgcn_exp2f(a[0]), __builtin_amdgcn_exp2f(a[1])};
            en[m][i] = (f32x2){__builtin_amdgcn_exp2f(b[0]), __builtin_amdgcn_exp2f(b[1])};
        }
        const f32x2 tp = (ep[m][0] + ep[m][1]) + (ep[m][2] + ep[m][3]), tn = (en[m][0] + en[m][1]) + (en[m][2] + en[m][3]);
        sp[m] = tp[0] + tp[1]; sn[m] = tn[0] + tn[1];
    }
#pragma unroll
    for (int m = M0; m < M1; ++m) red2<false>(sp[m], sn[m]);
#pragma unroll
    for (int m = M0; m < M1; ++m) {
        const float rp = __builtin_amdgcn_rcpf(sp[m]), rn = __builtin_amdgcn_rcpf(sn[m]);
        u32x4 a, b;
#pragma unroll
        for (int i = 0; i < 4; ++i) { const f32x2 x = ep[m][i] * rp, y = en[m][i] * rn; a[i] = pk2(x[0], x[1]); b[i] = pk2(y[0], y[1]); }
        p[m][0] = __builtin_bit_cast(f32x4, a); p[m][1] = __builtin_bit_cast(f32x4, b);
    }
}
DI u32x2 glu4(const f32x4& g, const f32x4& u) {
    u32x2 r;
#pragma unroll
    for (int i = 0; i < 2; ++i) {
        const f32x2 t = (f32x2){g[2 * i], g[2 * i + 1]} * -1.44269504088896340736f;
        const f32x2 d = (f32x2){__builtin_amdgcn_exp2f(t[0]), __builtin_amdgcn_exp2f(t[1])} + 1.0f;
        const f32x2 h = (f32x2){u[2 * i], u[2 * i + 1]} * (f32x2){__builtin_amdgcn_rcpf(d[0]), __builtin_amdgcn_rcpf(d[1])};
        r[i] = pk2(h[0], h[1]);
    }
    return r;
}
DI u32x4 glu8(const f32x4& g0, const f32x4& u0, const f32x4& g1, const f32x4& u1) { const u32x2 a = glu4(g0, u0), b = glu4(g1, u1); return (u32x4){a[0], a[1], b[0], b[1]}; }

constexpr int G2_SLOT = 32768;
constexpr int G2_NS = 4;
constexpr int G2_KV = G2_NS * G2_SLOT;
static_assert(G2_KV + 8192 + 256 <= G1_WPHI, "");
#define G2_NA 36
#define G2_U 4
constexpr int G2_NB = 72 - G2_NA, G2_NBH = G2_NB - 16;
static_assert(G2_NA % 4 == 0 && G2_NA >= 32 && G2_U % 2 == 0 && G2_U >= 4 && G2_NB - 31 - G2_U >= 0, "");
typedef GAS unsigned gu32;
DI void wait_tags(gu32* p, unsigned seen, unsigned tag) {
    if (__builtin_amdgcn_ballot_w64(seen != tag) != 0ull) {
        unsigned spins = 0;
        while (__builtin_amdgcn_ballot_w64(__hip_atomic_load(p, __ATOMIC_RELAXED, __HIP_MEMORY_SCOPE_AGENT) != tag) != 0ull && ++spins < (1u << 18))
            __builtin_amdgcn_s_sleep(1);
    }
}
template <unsigned N, int NL> DI void arrive(LAS unsigned* arr, gu32* word, unsigned tag) {
    const int l = threadIdx.x & 63;
    unsigned old = 0;
    if (l == 0) old = __hip_atomic_fetch_add(arr, 1u, __ATOMIC_RELAXED, __HIP_MEMORY_SCOPE_WORKGROUP);
    if (__builtin_amdgcn_readfirstlane(old) == N - 1 && l < NL) __hip_atomic_store(word, tag, __ATOMIC_RELAXED, __HIP_MEMORY_SCOPE_AGENT);
}
DI float wsum(float v) {
    v += __builtin_amdgcn_update_dpp(0.f, v, 0xb1, 0xf, 0xf, true);
    v += __builtin_amdgcn_update_dpp(0.f, v, 0x4e, 0xf, 0xf, true);
    v += __builtin_amdgcn_update_dpp(0.f, v, 0x141, 0xf, 0xf, true);
    v += __builtin_amdgcn_update_dpp(0.f, v, 0x140, 0xf, 0xf, true);
    float u = v; swap16(v, u); v += u;
    u = v; swap32(v, u); return v + u;
}

#define LP(a) ((LAS const char*)(uintptr_t)(a))
#define G1_STAGE_A(slot) do { dma2(aptr, avoff, adst + (slot) * G1_SLOT); aptr += NMB * 1024; OPAQUE_S(aptr); } while (0)
#define G1_STAGE_B(slot) do { \
        dma1<0>(bptr[0], bvoff[0], bdst[0] + (slot) * G1_SLOT); dma1<0>(bptr[1], bvoff[1], bdst[1] + (slot) * G1_SLOT); \
        if (grp == 0) dma1<0>(bptr[2], bvoff[2], bdst[2] + (slot) * G1_SLOT); \
        bptr[0] += bstep[0]; bptr[1] += bstep[1]; bptr[2] += bstep[2]; \
        OPAQUE_S(bptr[0]); OPAQUE_S(bptr[1]); OPAQUE_S(bptr[2]); } while (0)
#define G1_STAGE(slot) do { G1_STAGE_A(slot); G1_STAGE_B(slot); } while (0)
#define G1_WAIT() WAIT_V(8)

template <bool kvt>
DI void tail(const uint16_t* __restrict__ x, const uint16_t* __restrict__ wphi, const uint16_t* __restrict__ wdown, const uint16_t* __restrict__ wo, char* __restrict__ ws,
             uint16_t* __restrict__ out, LAS char* lds, const unsigned tag, const char* aptr, const char* bp0, const char* bp1, const char* bp2,
             unsigned bs0, unsigned bs1, unsigned bs2, const char* pw0, const char* pw1, const char* pw2) {
    int tid = threadIdx.x;
    OPAQUE(tid);
    const int l = tid & 63;
    const int w = __builtin_amdgcn_readfirstlane(tid >> 6);
    const int grp = w >> 2, wr = w & 3, wc = grp;
    const int bid = blockIdx.x, idx = bid >> 3, xcd = bid & 7;
    const int pm = idx >> 2;
    const int j = xcd * 2 + ((idx >> 1) & 1);
    const int F0 = kvt ? 2048 + 96 * j : 128 * j;
    gu32* cnt = (gu32*)(ws + WS_CNT);
    const int G = l >> 4, q4 = (l >> 2) & 3, p4 = l & 3;
    const unsigned lbase = (unsigned)(uintptr_t)lds;
    const rsrc_t rw = mkrsrc(ws, (unsigned)WS_END);
    const char* bptr[3] = {bp0, bp1, bp2};
    const unsigned bstep[3] = {bs0, bs1, bs2};
    unsigned bvoff[3];
#pragma unroll
    for (int i = 0; i < 3; ++i) bvoff[i] = (l >> 2) * (bstep[i] >> 5) + ((((l >> 1) & 1) ^ ((l >> 5) & 1)) * 32) + (l & 1) * 16;
    const unsigned avoff = l * 16, adst = lbase + 2 * w * 1024;
    unsigned bdst[3];
#pragma unroll
    for (int i = 0; i < 3; ++i) bdst[i] = lbase + 16384 + (grp == 0 ? 3 * w + i : 12 + 2 * (w - 4) + (i < 2 ? i : 0)) * 1024;

    const unsigned lp0 = G * 512 + q4 * 64 + ((G & 1) * 32) + p4 * 8;
    const unsigned lp1 = lp0 ^ 32;
    unsigned rA = lbase + wr * 4096 + l * 16;
    unsigned rB0 = lbase + 16384 + 5 * wc * 2048 + lp0;
    unsigned rB1 = lbase + 16384 + 5 * wc * 2048 + lp1;
    unsigned rB8 = kvt ? lbase + 16384 + 2 * 2048 + (wc ? lp1 : lp0) : rB0 + 2 * 2048;
    unsigned rB9 = kvt ? lbase + 16384 + 7 * 2048 + (wc ? lp1 : lp0) : rB1 + 2 * 2048;
#define G1_MOVE(d) do { rA += (d); rB0 += (d); rB1 += (d); rB8 += (d); rB9 += (d); \
        OPAQUE(rA); OPAQUE(rB0); OPAQUE(rB1); OPAQUE(rB8); OPAQUE(rB9); } while (0)

    f32x4 acc[4][10];
#pragma unroll
    for (int m = 0; m < 4; ++m)
#pragma unroll
        for (int n = 0; n < 10; ++n) acc[m][n] = (f32x4){0.f, 0.f, 0.f, 0.f};
    bf16x8 fa[4], fb[10];

#define G1_READ(so) do { \
        _Pragma("unroll") for (int m = 0; m < 4; ++m) fa[m] = *(LAS const bf16x8*)(LP(rA) + (so) + m * 1024); \
        _Pragma("unroll") for (int u = 0; u < 5; ++u) if (u != 2) { \
            fb[2 * u] = tr_frag(LP(rB0) + (so) + u * 2048, LP(rB0) + (so) + u * 2048 + 256); \
            fb[2 * u + 1] = tr_frag(LP(rB1) + (so) + u * 2048, LP(rB1) + (so) + u * 2048 + 256); } \
        fb[4] = tr_frag(LP(rB8) + (so), LP(rB8) + (so) + 256); \
        fb[5] = tr_frag(LP(rB9) + (so), LP(rB9) + (so) + 256); } while (0)
#define G1_MMA() do { __builtin_amdgcn_s_setprio(1); \
        _Pragma("unroll") for (int m = 0; m < 4; ++m) _Pragma("unroll") for (int n_ = 0; n_ < 10; ++n_) { const int n = (m & 1) ? 9 - n_ : n_; \
            if (kvt && (n == 2 || n == 3)) MFMA16_ACC(acc[m][n], fa[m], fb[n]); else MFMA16_ACC(acc[m][n], fb[n], fa[m]); } \
        __builtin_amdgcn_s_setprio(0); } while (0)
#define G1_PHASE(so, STG, VW) do { G1_READ(so); STG; WAIT_L0(); VW; SCHED(); BAR(); SCHED(); G1_MMA(); SCHED(); BAR(); SCHED(); } while (0)

    G1_MOVE(0);
#pragma unroll
    for (int m = 0; m < 4; ++m)
#pragma unroll
        for (int n = 0; n < 10; ++n) OPAQUE(acc[m][n]);
    SCHED();
    wait_tags(cnt + 64 * (C_LN + pm * 8 + w) + (l & 3), tag - 1u, tag);
    STAMP(0, 3);
    G1_STAGE_A(0); G1_STAGE_A(1);
    WAIT_V(2);
    BAR();
    G1_STAGE(2);
    if (grp == 1) dma1<0>((const char*)wphi + (size_t)(2 * j) * 2048 + wr * 1024, avoff, lbase + G1_WPHI + wr * 1024);
    STAMP(1, 1);
    if (grp == 1) BAR();
    SCHED();
    for (int it = 0; it < 7; ++it) {
        if (w == 0) STAMP(4, it);
        if (w >= 6) {
            const int c = bid * 2 + w - 6;
            const char* g; int lim;
            if (it < 4) { lim = 480; g = c < 144 ? pw0 + (size_t)(192 + it * 144 + c) * 8192 : (c < 312 ? pw1 - 144 * 8192 : pw2 - 312 * 8192) + (size_t)(224 + it * 168 + c) * 8192; }
            else { const int u = (it - 4) * 384 + c; lim = 384; g = u < 896 ? (const char*)wdown + (size_t)u * 8192 : (const char*)wo + (size_t)(u - 896) * 8192; }
            if (c < lim) pf_lines(g, avoff * 8, lbase + G1_PF, ~0ull);
        }
        G1_PHASE(0, G1_STAGE(3), G1_WAIT());
        G1_PHASE(G1_SLOT, G1_STAGE(0), G1_WAIT());
        G1_MOVE(2 * G1_SLOT);
        G1_PHASE(0, G1_STAGE(1), G1_WAIT());
        G1_PHASE(G1_SLOT, G1_STAGE(2), G1_WAIT());
        G1_MOVE(-2 * G1_SLOT);
    }
    if (w == 0) STAMP(4, 7);
    G1_PHASE(0, G1_STAGE(3), G1_WAIT());
    G1_PHASE(G1_SLOT, (void)0, WAIT_V(4));
    G1_MOVE(2 * G1_SLOT);
    G1_PHASE(0, (void)0, WAIT_V(0));
    G1_READ(G1_SLOT); WAIT_L0(); SCHED(); BAR(); SCHED(); G1_MMA(); SCHED();
    if (grp == 0) BAR();
    asm volatile("s_nop 7\n\ts_nop 7" ::: "memory");
    SCHED();
    STAMP(1, 2);
    if (kvt) STAMP(2, 0);

    constexpr bool tb = !kvt;
    const int t16 = idx >> 1;
    const int pm2 = (xcd >> 1) * 4 + (t16 & 3), pn = (xcd & 1) * 4 + (t16 >> 2);
    const int tile = pm2 * 8 + pn;
    const int prep = 4 * (pm2 >> 1) + (xcd & 1) * 2 + (wr & 1);
    constexpr int k0 = tb ? 0 : G2_NBH;
    const unsigned lo16 = l * 16;
    const bool isb = wr >= 2;
    const char* gptr = isb ? (const char*)(wdown + (size_t)(64 * k0 + 32 * (wr - 2)) * D_ + pn * 128) : ws + WS_A2 + ((size_t)(2 * k0 + wr) * NMB + pm2 * 8) * 1024;
    const char* gptr_o = isb ? (const char*)(wo + (size_t)(32 * (wr & 1)) * D_ + pn * 128) : ws + WS_A2 + ((size_t)(112 + wr) * NMB + pm2 * 8) * 1024;
    const unsigned gstep = isb ? 64 * D_ * 2 : 2 * NMB * 1024;
    unsigned voff[4];
#pragma unroll
    for (int i = 0; i < 4; ++i) {
        const int r = 4 * i + G;
        voff[i] = isb ? r * (D_ * 2) + ((((l & 15) >> 1) ^ (r & 7)) * 32) + (l & 1) * 16 - 1024 * i : lo16;
    }
    const unsigned ghalf = isb ? 16 * D_ * 2 : 4096;
    unsigned dst = lbase + wr * 8192;
    int tld = 0, slot = 0;
#define G2_DMA4_(g, d, SC) asm volatile("s_mov_b32 m0, %5\n\ts_nop 0\n\tglobal_load_lds_dwordx4 %0, %4" SC "\n\tglobal_load_lds_dwordx4 %1, %4 offset:1024" SC "\n\t" \
            "global_load_lds_dwordx4 %2, %4 offset:2048" SC "\n\tglobal_load_lds_dwordx4 %3, %4 offset:3072" SC \
            :: "v"(voff[0]), "v"(voff[1]), "v"(voff[2]), "v"(voff[3]), "s"(g), "s"(d) : "memory")
#define G2_DMA4(g, d) G2_DMA4_(g, d, " sc1")
#define G2_NEXTSLOT() do { ++slot; const int wrap = slot == G2_NS; slot = wrap ? 0 : slot; dst += wrap ? (unsigned)(-(G2_NS - 1) * G2_SLOT) : (unsigned)G2_SLOT; } while (0)
    const char* gptr_odd = gptr + gstep;
#define G2_STAGE() do { \
        if (!isb) { G2_DMA4(gptr, dst); G2_DMA4(gptr + ghalf, dst + 4096); } else { G2_DMA4_(gptr, dst, ""); G2_DMA4_(gptr + ghalf, dst + 4096, ""); } \
        ++tld; gptr = !tb ? gptr + gstep : tld == G2_NBH / 2 ? gptr_odd : tld == G2_NBH ? gptr_o : gptr + (tld < G2_NBH ? 2 * gstep : gstep); G2_NEXTSLOT(); } while (0)
#define G2_PRUN(n) do { _Pragma("unroll 1") for (int t_ = 0; t_ < (n); ++t_) { WAIT_V(16); BAR(); G2_STAGE(); } } while (0)
#define POLL(dst_, p_) asm volatile("global_load_dword %0, %1, off sc1" : "=v"(dst_) : "v"(p_) : "memory")
    const size_t off0 = (size_t)(pm2 * 128 + (wr >> 1) * 64 + (l & 15)) * D_ + pn * 128 + (wr & 1) * 64 + (G & 1) * 16 + (G >> 1) * 8;

    const int fq = l >> 4, fr = l & 15;
    const int hh = 2 * j + wc;
    const int mb0 = pm * 16 + wr * 4;
    if (w >= 6) { G2_STAGE(); G2_STAGE(); }
    const f32x4 z4 = {0.f, 0.f, 0.f, 0.f};
    const unsigned hb = (unsigned)WS_A2 + (((kvt ? F0 + 32 * wc : F0 + 64 * wc + 32) >> 5) * NMB + mb0) * 1024;
    f32x4 pf[4][2];
#define POS(m) __builtin_bit_cast(bf16x8, pf[m][0])
#define NEG(m) __builtin_bit_cast(bf16x8, pf[m][1])
    u32x4 xv[4][2];
#define G1_LOGITS() do { bf16x8 wp[2]; \
        _Pragma("unroll") for (int c = 0; c < 2; ++c) { LAS const char* p = lds + G1_WPHI + wc * 2048 + (4 * G + q4) * 64 + c * 32 + p4 * 8; wp[c] = tr_frag(p, p + 1024); } \
        _Pragma("unroll") for (int m = 0; m < 4; ++m) { const bf16x8 qf = pack8(acc[m][0], acc[m][1]); pf[m][0] = MFMA16(wp[0], qf, z4); pf[m][1] = MFMA16(wp[1], qf, z4); } } while (0)
    __builtin_amdgcn_s_setprio(1);
    if constexpr (!kvt) {
#pragma unroll
        for (int m = 0; m < 4; ++m) {
            stw(glu8(acc[m][2], acc[m][4], acc[m][3], acc[m][5]), rw, lo16 + m * 1024, hb - NMB * 1024);
            stw(glu8(acc[m][6], acc[m][8], acc[m][7], acc[m][9]), rw, lo16 + m * 1024, hb);
        }
        SCHED();
        G1_LOGITS();
        SCHED();
        if (w < 4) { WAIT_V(0); arrive<4, 4>((LAS unsigned*)(lds + G1_ARR), cnt + 64 * (C_HQ + 4 * pm + (l & 3)) + j, tag); __builtin_amdgcn_s_setprio(0); }
        hedgehog<0, 2>(pf);
        SCHED();
        if (w >= 4) { WAIT_V(0); arrive<4, 4>((LAS unsigned*)(lds + G1_ARR + 4), cnt + 64 * (C_HQ + 4 * pm + (l & 3)) + 16 + j, tag); }
        SCHED();
        STAMP(1, 5);
        if (w == 4 || w == 5) { wait_tags(cnt + 64 * (C_HQ + prep) + (l & 15), tag - 1u, tag); STAMP(6, 0); G2_STAGE(); G2_STAGE(); }
        hedgehog<2, 4>(pf);
        __builtin_amdgcn_s_setprio(0);
    } else {
        G1_LOGITS();
        u32x4 i0 = {0u, 0u, 0u, 0u}, i1 = {0u, 0u, 0u, 0u};
        {
            const int d = fr - 4 * fq;
            const unsigned lo = (d == 0) ? 0x3f80u : (d == 1) ? 0x3f800000u : 0u;
            const unsigned hi = (d == 2) ? 0x3f80u : (d == 3) ? 0x3f800000u : 0u;
            i0[0] = lo; i0[1] = hi; i1[2] = lo; i1[3] = hi;
        }
        const bf16x8 I0 = __builtin_bit_cast(bf16x8, i0), I1 = __builtin_bit_cast(bf16x8, i1);
        constexpr int RED = 65536, ZRED = RED + 65536, HX = ZRED + 2048;
        static_assert(HX + 16384 <= G1_WPHI, "");
        bf16x8 V[2][2];
#pragma unroll
        for (int mp = 0; mp < 2; ++mp) { V[mp][0] = pack8(acc[2 * mp][2], acc[2 * mp + 1][2]); V[mp][1] = pack8(acc[2 * mp][3], acc[2 * mp + 1][3]); }
        SCHED();
        STAMP(2, 1);
        hedgehog<0, 4>(pf);
        SCHED();
        STAMP(2, 2);
        const u32x4 o4 = {0x3f803f80u, 0x3f803f80u, 0x3f803f80u, 0x3f803f80u};
        const bf16x8 ONES = __builtin_bit_cast(bf16x8, o4);
        f32x4 T[4][2][2];
#pragma unroll
        for (int m = 0; m < 4; ++m) {
            T[m][0][0] = MFMA16(POS(m), I0, z4); T[m][0][1] = MFMA16(POS(m), I1, z4);
            T[m][1][0] = MFMA16(NEG(m), I0, z4); T[m][1][1] = MFMA16(NEG(m), I1, z4);
        }
#pragma unroll
        for (int m = 0; m < 4; ++m) *(LAS u32x2*)(lds + HX + (wr * 4 + m) * 1024 + lo16 + 8 * wc) = glu4(acc[m][4], acc[m][5]);
        LAS char* red = lds + RED + (wc * 4 + wr) * 8192 + lo16;
#pragma unroll
        for (int s = 0; s < 2; ++s)
#pragma unroll
            for (int cc = 0; cc < 2; ++cc) {
                const bf16x8 P0 = pack8(T[0][s][cc], T[1][s][cc]), P1 = pack8(T[2][s][cc], T[3][s][cc]);
                const f32x4 k0_ = MFMA16(P1, V[1][0], MFMA16(P0, V[0][0], z4));
                const f32x4 k1_ = MFMA16(P1, V[1][1], MFMA16(P0, V[0][1], z4));
                const f32x4 zz = MFMA16(P1, ONES, MFMA16(P0, ONES, z4));
                stw(glu8(acc[2 * s + cc][6], acc[2 * s + cc][8], acc[2 * s + cc][7], acc[2 * s + cc][9]), rw, lo16 + (2 * s + cc) * 1024, hb);
                *(LAS f32x4*)(red + ((s * 2 + 0) * 2 + cc) * 1024) = k0_;
                *(LAS f32x4*)(red + ((s * 2 + 1) * 2 + cc) * 1024) = k1_;
                *(LAS f32x4*)(lds + ZRED + (wc * 4 + wr) * 256 + (s * 32 + cc * 16 + 4 * fq) * 4) = zz;
            }
        SCHED();
        if (w < 4) {
#pragma unroll
            for (int m = 0; m < 4; ++m) { xv[m][0] = *(const u32x4*)(x + off0 + m * 16 * D_); xv[m][1] = *(const u32x4*)(x + off0 + m * 16 * D_ + 32); }
        }
        gu32* fh = cnt + 64 * (C_HQ + prep) + (l & 31);
        unsigned sh;
        POLL(sh, fh);
        WAIT_L0(); SCHED();
        STAMP(2, 3);
        BAR(); SCHED();
        STAMP(2, 4);
        const unsigned kvo = (unsigned)WS_KVP + (pm * 32 + hh) * KVP_SZ;
        f32x4 sum[2];
#pragma unroll
        for (int i = 0; i < 2; ++i) {
            LAS const char* p = lds + RED + wc * 32768 + (2 * wr + i) * 1024 + lo16;
            sum[i] = (*(LAS const f32x4*)p + *(LAS const f32x4*)(p + 8192)) + (*(LAS const f32x4*)(p + 16384) + *(LAS const f32x4*)(p + 24576));
            stw(*(LAS const u32x4*)(lds + HX + (wr * 4 + 2 * wc + i) * 1024 + lo16), rw, lo16 + i * 1024, (unsigned)WS_A2 + (((F0 + 64) >> 5) * NMB + mb0 + 2 * wc) * 1024);
        }
        stw(pack8(sum[0], sum[1]), rw, lo16, kvo + wr * 1024);
        {
            LAS const char* p = lds + ZRED + wc * 1024 + (l & 15) * 16;
            const f32x4 zs = (*(LAS const f32x4*)p + *(LAS const f32x4*)(p + 256)) + (*(LAS const f32x4*)(p + 512) + *(LAS const f32x4*)(p + 768));
            stw(zs, rw, (wr == 0 && l < 16) ? lo16 : 0x80000000u, kvo + 4096);
        }
        __builtin_amdgcn_s_setprio(0);
        SCHED();
        asm volatile("s_waitcnt vmcnt(4)" : "+v"(sh) :: "memory");
        const bool early = __builtin_amdgcn_ballot_w64(sh != tag) == 0ull;
        if ((w == 4 || w == 5) && early) { G2_STAGE(); G2_STAGE(); WAIT_V(16); } else WAIT_V(0);
        arrive<8, 8>((LAS unsigned*)(lds + G1_ARR), l < 4 ? cnt + 64 * (C_KV + 4 * j + l) + pm : cnt + 64 * (C_HK + 4 * pm + (l & 3)) + j, tag);
        if ((w == 4 || w == 5) && !early) { wait_tags(fh, tag - 1u, tag); G2_STAGE(); G2_STAGE(); }
        WAIT_L0();
    }
    SCHED();
    STAMP(1, 6);

#define G2_OCOMP() do { \
        bf16x8 kf[2][2], zf[2]; \
        _Pragma("unroll") for (int s = 0; s < 2; ++s) { \
            kf[s][0] = *(LAS const bf16x8*)(lds + G2_KV + wc * 4096 + (s * 2) * 1024 + lo16); kf[s][1] = *(LAS const bf16x8*)(lds + G2_KV + wc * 4096 + (s * 2 + 1) * 1024 + lo16); \
            const u32x2 za = *(LAS const u32x2*)(lds + G2_KV + 8192 + wc * 128 + s * 64 + G * 8), zb = *(LAS const u32x2*)(lds + G2_KV + 8192 + wc * 128 + s * 64 + 32 + G * 8); \
            zf[s] = __builtin_bit_cast(bf16x8, (u32x4){za[0], za[1], zb[0], zb[1]}); } \
        _Pragma("unroll") for (int m = 0; m < 4; ++m) { \
            const f32x4 dn = MFMA16(zf[1], NEG(m), MFMA16(zf[0], POS(m), z4)); \
            const f32x4 n0 = MFMA16(kf[1][0], NEG(m), MFMA16(kf[0][0], POS(m), z4)), n1 = MFMA16(kf[1][1], NEG(m), MFMA16(kf[0][1], POS(m), z4)); \
            const float rd = __builtin_amdgcn_rcpf(dn[0] + 1e-6f); \
            stw(pack8(n0 * rd, n1 * rd), rw, lo16 + m * 1024, (unsigned)WS_A2 + ((112 + hh) * NMB + mb0) * 1024); } \
        WAIT_L0(); SCHED(); } while (0)

    if (w >= 4) {
        unsigned seen;
        WAIT_V(8);
        STAMP(5 + tb, 1);
        BAR(); G2_STAGE();
        WAIT_V(8); BAR(); G2_STAGE(); G2_STAGE();
        if constexpr (!tb) {
            constexpr int UK = 32 - G2_NBH - 4;
            G2_PRUN(UK - 3);
            POLL(seen, cnt + 64 * (C_HK + prep) + (l & 15));
            G2_PRUN(2);
            asm volatile("s_waitcnt vmcnt(16)" : "+v"(seen) :: "memory");
            if (!isb) wait_tags(cnt + 64 * (C_HK + prep) + (l & 15), seen, tag);
            BAR(); G2_STAGE();
            G2_PRUN(G2_NA - 5 - UK);
            WAIT_V(16); BAR(); WAIT_V(8); BAR(); WAIT_V(0); BAR();
        } else {
            static_assert(G2_NBH / 2 - 4 >= G2_U, "the odd h steps are first requested behind barrier NBH/2 - 4");
            G2_PRUN(G2_U - 3);
            gu32* fk = l < 32 ? cnt + 64 * (C_KV + 4 * j + wr) + (l & 7) : cnt + 64 * (C_HQ + prep) + 16 + (l & 15);
            POLL(seen, fk);
            G2_PRUN(2);
            asm volatile("s_waitcnt vmcnt(16)" : "+v"(seen) :: "memory");
            wait_tags(fk, seen, tag);
            u32x4 t[2][8]; float zt[8];
            const char* kvs = ws + WS_KVP + (size_t)(2 * j + (wr >> 1)) * KVP_SZ;
            const unsigned kvv = (wr & 1) * 2048 + lo16, zv = 4096 + l * 4;
#define G2_KVLD(i) do { _Pragma("unroll") for (int k = 0; k < 8; ++k) \
                asm volatile("global_load_dwordx4 %0, %1, %2 offset:%3 sc1" : "=v"(t[i][k]) : "v"(kvv), "s"(kvs + (size_t)k * 32 * KVP_SZ), "i"((i) * 1024) : "memory"); \
            _Pragma("unroll") for (int k = 4 * (i); k < 4 * (i) + 4; ++k) \
                asm volatile("global_load_dword %0, %1, %2 sc1" : "=v"(zt[k]) : "v"(zv), "s"(kvs + (size_t)k * 32 * KVP_SZ) : "memory"); } while (0)
            BAR(); G2_STAGE(); G2_KVLD(0);
            WAIT_V(28); BAR(); G2_STAGE(); G2_KVLD(1);
            WAIT_V(40); BAR(); G2_STAGE();
            WAIT_V(40); BAR(); G2_STAGE();
            WAIT_V(28); BAR(); G2_STAGE();
            WAIT_V(16); SCHED();
#pragma unroll
            for (int i = 0; i < 2; ++i) {
                asm volatile("" : "+v"(t[i][0]), "+v"(t[i][1]), "+v"(t[i][2]), "+v"(t[i][3]), "+v"(t[i][4]), "+v"(t[i][5]), "+v"(t[i][6]), "+v"(t[i][7]),
                             "+v"(zt[4 * i]), "+v"(zt[4 * i + 1]), "+v"(zt[4 * i + 2]), "+v"(zt[4 * i + 3]));
                u32x4 r;
#pragma unroll
                for (int e = 0; e < 4; ++e) {
                    float lo = 0.f, hi = 0.f;
#pragma unroll
                    for (int k = 0; k < 8; ++k) { lo += bflo(t[i][k][e]); hi += bfhi(t[i][k][e]); }
                    r[e] = pk2(lo, hi);
                }
                *(LAS u32x4*)(lds + G2_KV + (wr >> 1) * 4096 + (2 * (wr & 1) + i) * 1024 + lo16) = r;
            }
            const float z = ((zt[0] + zt[1]) + (zt[2] + zt[3])) + ((zt[4] + zt[5]) + (zt[6] + zt[7]));
            *(LAS unsigned short*)(lds + G2_KV + 8192 + (wr >> 1) * 128 + l * 2) = (unsigned short)pk2(z, z);
            WAIT_L0(); SCHED();
            BAR(); G2_STAGE();
            G2_OCOMP();
            G2_PRUN(3);
            if (w == 7 && l < 4) __hip_atomic_store(cnt + 64 * (C_O + 4 * pm + l) + j, tag, __ATOMIC_RELAXED, __HIP_MEMORY_SCOPE_AGENT);
            G2_PRUN(G2_NB - 31 - G2_U);
            gu32* f0 = cnt + 64 * (C_O + prep) + (l & 15);
            POLL(seen, f0);
            G2_PRUN(2);
            asm volatile("s_waitcnt vmcnt(16)" : "+v"(seen) :: "memory");
            STAMP(6, 2);
            if (!isb) wait_tags(f0, seen, tag);
            STAMP(6, 3);
            BAR(); G2_STAGE();
            G2_PRUN(15);
            gu32* f1 = cnt + 64 * (C_P + tile);
            WAIT_V(16); BAR(); WAIT_V(8); BAR();
            POLL(seen, f1);
            WAIT_V(1); BAR();
            asm volatile("s_waitcnt vmcnt(0)" : "+v"(seen) :: "memory");
            STAMP(6, 4);
            wait_tags(f1, seen, tag);
            STAMP(6, 5);
            char* part = ws + WS_PART + (size_t)tile * 32768;
#pragma unroll
            for (int i = 0; i < 4; ++i) voff[i] = lo16;
            G2_DMA4(part + wr * 8192, lbase + wr * 8192); G2_DMA4(part + wr * 8192 + 4096, lbase + wr * 8192 + 4096);
            WAIT_V(0);
            BAR();
        }
        STAMP(3, 7);
        return;
    }
    const int cr = w >> 1, cc2 = w & 1;
    f32x4 ac2[4][4];
#pragma unroll
    for (int m = 0; m < 4; ++m)
#pragma unroll
        for (int i = 0; i < 2; ++i) {
            if constexpr (tb) { ac2[m][2 * i] = z4; ac2[m][2 * i + 1] = z4; }
            else {
                unsigned d0 = xv[m][i][0], d1 = xv[m][i][1], d2 = xv[m][i][2], d3 = xv[m][i][3];
                swap16u(d0, d2); swap16u(d1, d3);
                ac2[m][2 * i] = (f32x4){bflo(d0), bfhi(d0), bflo(d1), bfhi(d1)}; ac2[m][2 * i + 1] = (f32x4){bflo(d2), bfhi(d2), bflo(d3), bfhi(d3)};
            }
        }
    const int r8 = 4 * (G & 1) + q4;
    unsigned sA = lbase + cr * 4096 + lo16;
    unsigned sB[4];
#pragma unroll
    for (int n = 0; n < 4; ++n) sB[n] = lbase + 16384 + (4 * G + q4) * 256 + (((4 * cc2 + n) ^ r8) * 32) + p4 * 8;
#define G2_MOVE(d) do { sA += (d); sB[0] += (d); sB[1] += (d); sB[2] += (d); sB[3] += (d); OPAQUE(sA); OPAQUE(sB[0]); OPAQUE(sB[1]); OPAQUE(sB[2]); OPAQUE(sB[3]); } while (0)
    bf16x8 ga[2][2][4], gb[2][2][4];
#define G2_RD_A(buf, so, kk, m) ga[buf][kk][m] = *(LAS const bf16x8*)(LP(sA) + (so) + (kk) * 8192 + (m) * 1024)
#define G2_RD_B(buf, so, kk, n) gb[buf][kk][n] = tr_frag(LP(sB[n]) + (so) + (kk) * 8192, LP(sB[n]) + (so) + (kk) * 8192 + 4096)
#define G2_READ(buf, so) do { _Pragma("unroll") for (int kk = 0; kk < 2; ++kk) _Pragma("unroll") for (int i = 0; i < 4; ++i) { G2_RD_B(buf, so, kk, i); G2_RD_A(buf, so, kk, i); } } while (0)
#define G2_STEP(cur, so, RD) do { if (RD) BAR(); SCHED(); \
        _Pragma("unroll") for (int kk = 0; kk < 2; ++kk) _Pragma("unroll") for (int m = 0; m < 4; ++m) _Pragma("unroll") for (int n_ = 0; n_ < 4; ++n_) { \
            const int n = (m & 1) ? 3 - n_ : n_; \
            MFMA16_ACC(ac2[m][n], gb[cur][kk][n], ga[cur][kk][m]); \
            if ((RD) && kk == 0) { const int i_ = m * 4 + n_; if (i_ & 1) G2_RD_A((cur) ^ 1, so, i_ >> 3, (i_ >> 1) & 3); else G2_RD_B((cur) ^ 1, so, i_ >> 3, (i_ >> 1) & 3); } \
            SCHED(); } \
        WAIT_L0(); SCHED(); } while (0)
    unsigned mv = 2 * G2_SLOT;
#define G2_RUN(n) do { _Pragma("unroll 1") for (int i_ = 0; i_ < (n); ++i_) { G2_STEP(0, G2_SLOT, 1); G2_MOVE(mv); mv = -mv; G2_STEP(1, 0, 1); } } while (0)

    G2_MOVE(0);
    BAR(); SCHED();
    G2_READ(0, 0);
    WAIT_L0(); SCHED();
    STAMP(3, 1);
    if constexpr (tb) {
        G2_RUN(G2_U / 2 + 3);
        G2_OCOMP();
        G2_RUN(1);
        WAIT_V(0); SCHED();
        G2_RUN(G2_NB / 2 - 5 - G2_U / 2);
    } else {
        G2_RUN(G2_NA / 2 - 1);
    }
    G2_STEP(0, G2_SLOT, 1);
    G2_STEP(1, 0, 0);
    asm volatile("s_nop 7\n\ts_nop 7" ::: "memory");
    SCHED();
    if constexpr (!tb) {
        STAMP(3, 2);
#pragma unroll
        for (int m = 0; m < 4; ++m)
#pragma unroll
            for (int i = 0; i < 2; ++i) stw(pack8(ac2[m][2 * i], ac2[m][2 * i + 1]), rw, lo16 + (m & 1) * 2048 + i * 1024, (unsigned)WS_PART + tile * 32768 + w * 8192 + (m >> 1) * 4096);
        WAIT_V(0); BAR();
        if (tid == 0) __hip_atomic_store(cnt + 64 * (C_P + tile), tag, __ATOMIC_RELAXED, __HIP_MEMORY_SCOPE_AGENT);
        STAMP(3, 6);
    } else {
        STAMP(3, 4);
        BAR(); SCHED();
#pragma unroll
        for (int m = 0; m < 4; ++m)
#pragma unroll
            for (int i = 0; i < 2; ++i) {
                const u32x4 pp = *(LAS const u32x4*)(lds + w * 8192 + (m * 2 + i) * 1024 + lo16);
                const f32x4 r0 = ac2[m][2 * i] + (f32x4){bflo(pp[0]), bfhi(pp[0]), bflo(pp[1]), bfhi(pp[1])};
                const f32x4 r1 = ac2[m][2 * i + 1] + (f32x4){bflo(pp[2]), bfhi(pp[2]), bflo(pp[3]), bfhi(pp[3])};
                unsigned d0 = pk2(r0[0], r0[1]), d1 = pk2(r0[2], r0[3]), d2 = pk2(r1[0], r1[1]), d3 = pk2(r1[2], r1[3]);
                swap16u(d0, d2); swap16u(d1, d3);
                *(u32x4*)(out + off0 + m * 16 * D_ + i * 32) = (u32x4){d0, d1, d2, d3};
            }
        if (bid == 0 && tid == 0) __hip_atomic_store(cnt + 64 * C_E, tag, __ATOMIC_RELAXED, __HIP_MEMORY_SCOPE_AGENT);
        STAMP(3, 5);
    }
}

__global__ __launch_bounds__(512, 2) void k_main(const uint16_t* __restrict__ x, const uint16_t* __restrict__ wqkv, const uint16_t* __restrict__ wphi,
                                                const uint16_t* __restrict__ wgate, const uint16_t* __restrict__ wup, const uint16_t* __restrict__ wdown,
                                                const uint16_t* __restrict__ wo, char* __restrict__ ws, uint16_t* __restrict__ out) {
    __shared__ __attribute__((aligned(16))) char smem_[G1_LDS];
    LAS char* lds = (LAS char*)smem_;
    const int tid = threadIdx.x, l = tid & 63;
    const int w = __builtin_amdgcn_readfirstlane(tid >> 6);
    const int grp = w >> 2;
    const int wr = w & 3, wc = grp;
    const int bid = blockIdx.x, idx = bid >> 3, xcd = bid & 7;
    const int pm = idx >> 2;
    const int j = xcd * 2 + ((idx >> 1) & 1);
    const bool kvt = (idx & 1) != 0;
    const int F0 = kvt ? 2048 + 96 * j : 128 * j;
    gu32* cnt = (gu32*)(ws + WS_CNT);
    STAMP(0, 0);
    const int lnslot = xcd * 4 + (idx & 3);
    u32x4 xa, xb; unsigned tag;
    {
        const char* xr = (const char*)x + (size_t)(pm * 256 + lnslot * 8 + w) * 2048;
        asm volatile("global_load_dwordx4 %0, %3, %4\n\tglobal_load_dwordx4 %1, %3, %4 offset:1024\n\tglobal_load_dword %2, %5, off sc1"
                     : "=&v"(xa), "=&v"(xb), "=&v"(tag) : "v"(l * 16), "s"(xr), "v"(cnt + 64 * C_E) : "memory");
    }

    const char* aptr = ws + WS_XN + (size_t)(pm * 16 + 2 * w) * 1024;
    const unsigned avoff = l * 16;
    const char* bptr[3]; unsigned bstep[3], bvoff[3], bdst[3];
#pragma unroll
    for (int i = 0; i < 3; ++i) {
        int t = grp == 0 ? 3 * w + i : 12 + 2 * (w - 4) + (i < 2 ? i : 0);
        const unsigned e = (unsigned)((kvt ? 0x491898502061204ull : 0xcb18a1412442040ull) >> (6 * (t >> 1))) & 63u;
        const unsigned mat = e & 3u, stride = mat == 0 ? 3 * D_ : F_;
        const uintptr_t base = mat == 0 ? (uintptr_t)wqkv : mat == 1 ? (uintptr_t)wgate : (uintptr_t)wup;
        bptr[i] = (const char*)(base + ((t & 1) * 16 * stride + (mat == 0 ? 64 * j : F0) + ((e >> 2) & 3u) * 1024 + (e >> 4) * 32) * 2u);
        bstep[i] = 64u * stride;
        bvoff[i] = (l >> 2) * stride * 2 + ((((l >> 1) & 1) ^ ((l >> 5) & 1)) * 32) + (l & 1) * 16;
        bdst[i] = (unsigned)(uintptr_t)lds + 16384 + t * 1024;
    }
    const unsigned lbase = (unsigned)(uintptr_t)lds;
    const unsigned adst = lbase + 2 * w * 1024;
    STAMP(1, 0);
    if (tid < 2) *(LAS unsigned*)(lds + G1_ARR + tid * 4) = 0u;
    G1_STAGE_B(0); G1_STAGE_B(1);
    if (grp == 0) WAIT_V(6); else WAIT_V(4);
    asm volatile("" : "+v"(xa), "+v"(xb), "+v"(tag) :: "memory");
    tag = __builtin_amdgcn_readfirstlane(tag) + 1u;
    const rsrc_t rw = mkrsrc(ws, (unsigned)WS_END);
    STAMP(0, 1);
    {
        float f[16];
#pragma unroll
        for (int i = 0; i < 4; ++i) { f[2 * i] = bflo(xa[i]); f[2 * i + 1] = bfhi(xa[i]); f[8 + 2 * i] = bflo(xb[i]); f[9 + 2 * i] = bfhi(xb[i]); }
        float sm = 0.f;
#pragma unroll
        for (int i = 0; i < 16; ++i) sm += f[i];
        const float mean = wsum(sm) * (1.0f / 1024.0f);
        float q = 0.f;
#pragma unroll
        for (int i = 0; i < 16; ++i) { const float d = f[i] - mean; q += d * d; }
        const float rs = __builtin_amdgcn_rcpf(bfr(__builtin_amdgcn_sqrtf(bfr(bfr(wsum(q) * (1.0f / 1024.0f)) + 1e-5f))));
        const float mb = bfr(mean);
#pragma unroll
        for (int i = 0; i < 8; ++i) { const unsigned d2 = pk2(f[2 * i] - mb, f[2 * i + 1] - mb); f[2 * i] = bflo(d2); f[2 * i + 1] = bfhi(d2); }
        LAS char* ex = lds + 3 * G1_SLOT;
        *(LAS u32x4*)(ex + l * 144 + w * 16) = (u32x4){pk2(f[0] * rs, f[1] * rs), pk2(f[2] * rs, f[3] * rs), pk2(f[4] * rs, f[5] * rs), pk2(f[6] * rs, f[7] * rs)};
        *(LAS u32x4*)(ex + (64 + l) * 144 + w * 16) = (u32x4){pk2(f[8] * rs, f[9] * rs), pk2(f[10] * rs, f[11] * rs), pk2(f[12] * rs, f[13] * rs), pk2(f[14] * rs, f[15] * rs)};
        WAIT_L0(); SCHED(); BAR(); SCHED();
#pragma unroll
        for (int i = 0; i < 2; ++i) {
            const int c = i * 64 + (tid >> 3), r = tid & 7;
            stw(*(LAS const u32x4*)(ex + c * 144 + r * 16), rw, ((c >> 2) * NMB * 1024) + ((c & 3) * 16 + r) * 16, (unsigned)WS_XN + (pm * 16 + (lnslot >> 1)) * 1024 + (lnslot & 1) * 128);
        }
        SCHED();
        WAIT_V(0);
        SCHED(); BAR(); SCHED();
        STAMP(0, 2);
        if (tid == 0) __hip_atomic_store(cnt + 64 * (C_LN + pm * 8 + (lnslot >> 2)) + (lnslot & 3), tag, __ATOMIC_RELAXED, __HIP_MEMORY_SCOPE_AGENT);
        SCHED();
    }
    if (kvt) tail<true>(x, wphi, wdown, wo, ws, out, lds, tag, aptr, bptr[0], bptr[1], bptr[2], bstep[0], bstep[1], bstep[2], (const char*)wqkv, (const char*)wgate, (const char*)wup);
    else tail<false>(x, wphi, wdown, wo, ws, out, lds, tag, aptr, bptr[0], bptr[1], bptr[2], bstep[0], bstep[1], bstep[2], (const char*)wqkv, (const char*)wgate, (const char*)wup);
}

extern "C" void kernel_launch(void* const* d_in, const int* in_sizes, int n_in, void* d_out, int out_size, void* d_ws, size_t ws_size,
                              hipStream_t stream) {
    const uint16_t* x = (const uint16_t*)d_in[0];
    const uint16_t* wqkv = (const uint16_t*)d_in[1];
    const uint16_t* wphi = (const uint16_t*)d_in[2];
    const uint16_t* wo = (const uint16_t*)d_in[3];
    const uint16_t* wgate = (const uint16_t*)d_in[5];
    const uint16_t* wup = (const uint16_t*)d_in[6];
    const uint16_t* wdown = (const uint16_t*)d_in[7];
    char* ws = (char*)d_ws;
    k_main<<<256, 512, 0, stream>>>(x, wqkv, wphi, wgate, wup, wdown, wo, ws, (uint16_t*)d_out);
}
```

```cpp
#include <hip/hip_runtime.h>
#include <hip/hip_bf16.h>
#include <stdint.h>

#define LAS __attribute__((address_space(3)))
#define GAS __attribute__((address_space(1)))
#define DI __device__ __forceinline__
typedef __bf16 bf16x8 __attribute__((ext_vector_type(8)));
typedef __bf16 bf16x2 __attribute__((ext_vector_type(2)));
typedef short s16x4 __attribute__((ext_vector_type(4)));
typedef short s16x8 __attribute__((ext_vector_type(8)));
typedef float f32x4 __attribute__((ext_vector_type(4)));
typedef float f32x2 __attribute__((ext_vector_type(2)));
typedef unsigned u32x4 __attribute__((ext_vector_type(4)));
typedef unsigned u32x2 __attribute__((ext_vector_type(2)));

constexpr int S_ = 2048, D_ = 1024, F_ = 3584, H_ = 32;
constexpr int NMB = S_ / 16;
constexpr size_t WS_XN = 0;
constexpr size_t WS_A2 = WS_XN + (size_t)32 * NMB * 1024;
constexpr size_t WS_KVP = WS_A2 + (size_t)144 * NMB * 1024;
constexpr int KVP_SZ = 4352;
constexpr size_t WS_PART = WS_KVP + (size_t)8 * 32 * KVP_SZ;
constexpr size_t WS_CNT = WS_PART + (size_t)128 * 32768;
constexpr int C_LN = 0, C_HQ = 64, C_HK = 96, C_O = 128, C_KV = 160, C_P = 224, C_E = 352, C_N = 353;
constexpr size_t WS_END = WS_CNT + C_N * 256;

#define STAMP(kid, i) do {} while (0)
#define MFMA16(a, b, c) __builtin_amdgcn_mfma_f32_16x16x32_bf16((a), (b), (c), 0, 0, 0)
#define WAIT_V(n) asm volatile("s_waitcnt vmcnt(" #n ")" ::: "memory")
#define WAIT_L(n) asm volatile("s_waitcnt lgkmcnt(" #n ")" ::: "memory")
#define WAIT_L0() __builtin_amdgcn_s_waitcnt(0xC07F)
#define MFMA16_ACC(acc, a, b) asm volatile("v_mfma_f32_16x16x32_bf16 %0, %1, %2, %0" : "+v"(acc) : "v"(a), "v"(b))
#define BAR() __builtin_amdgcn_s_barrier()
#define SCHED() __builtin_amdgcn_sched_barrier(0)

DI unsigned pk2(float lo, float hi) {
    f32x2 v = {lo, hi};
    return __builtin_bit_cast(unsigned, __builtin_convertvector(v, bf16x2));
}
DI float bflo(unsigned u) { return __uint_as_float(u << 16); }
DI float bfhi(unsigned u) { return __uint_as_float(u & 0xffff0000u); }
DI float bfr(float x) { return bflo(pk2(x, x)); }
DI bf16x8 pack8(const f32x4& a, const f32x4& b) {
    u32x4 r = {pk2(a[0], a[1]), pk2(a[2], a[3]), pk2(b[0], b[1]), pk2(b[2], b[3])};
    return __builtin_bit_cast(bf16x8, r);
}
DI bf16x8 tr_frag(LAS const char* p0, LAS const char* p1) {
    s16x4 a = __builtin_amdgcn_ds_read_tr16_b64_v4i16((LAS s16x4*)p0);
    s16x4 b = __builtin_amdgcn_ds_read_tr16_b64_v4i16((LAS s16x4*)p1);
    s16x8 c = __builtin_shufflevector(a, b, 0, 1, 2, 3, 4, 5, 6, 7);
    return __builtin_bit_cast(bf16x8, c);
}
template <int OFF> DI void dma1(const char* g, unsigned voff, unsigned ldsdst) {
    asm volatile("s_mov_b32 m0, %2\n\ts_nop 0\n\tglobal_load_lds_dwordx4 %0, %1 offset:%3" :: "v"(voff), "s"(g), "s"(ldsdst), "i"(OFF) : "memory");
}
template <int ADD> DI void dma2s(const char* g, unsigned voff, unsigned ldsdst) {
    asm volatile("s_add_u32 m0, %2, %3\n\ts_nop 0\n\tglobal_load_lds_dwordx4 %0, %1 sc1\n\tglobal_load_lds_dwordx4 %0, %1 offset:1024 sc1"
                 :: "v"(voff), "s"(g), "s"(ldsdst), "i"(ADD) : "memory", "scc");
}
template <int ADD> DI void dma1a(const char* g, unsigned voff, unsigned ldsdst) {
    asm volatile("s_add_u32 m0, %2, %3\n\ts_nop 0\n\tglobal_load_lds_dwordx4 %0, %1" :: "v"(voff), "s"(g), "s"(ldsdst), "i"(ADD) : "memory", "scc");
}
DI void pf_lines(const char* g, unsigned voff, unsigned ldspad, unsigned long long mask) {
    asm volatile("s_mov_b32 m0, %2\n\ts_mov_b64 exec, %3\n\tglobal_load_lds_dword %0, %1\n\ts_mov_b64 exec, -1" :: "v"(voff), "s"(g), "s"(ldspad), "s"(mask) : "memory");
}
typedef __amdgpu_buffer_rsrc_t rsrc_t;
DI rsrc_t mkrsrc(const void* p, unsigned bytes) { return __builtin_amdgcn_make_buffer_rsrc((void*)p, 0, bytes, 0x00020000); }
template <class T> DI void stw(const T& v, rsrc_t r, unsigned voff, unsigned soff) { __builtin_amdgcn_raw_buffer_store_b128(__builtin_bit_cast(u32x4, v), r, voff + soff, 0, 16); }
#define OPAQUE(x) asm volatile("" : "+v"(x))
#define OPAQUE_S(x) asm volatile("" : "+s"(x))
DI float fmx(float a, float b) { return __builtin_elementwise_maximum(a, b); }
DI void swap16(float& a, float& b) { auto r = __builtin_amdgcn_permlane16_swap(__float_as_uint(a), __float_as_uint(b), false, false); a = __uint_as_float(r[0]); b = __uint_as_float(r[1]); }
DI void swap32(float& a, float& b) { auto r = __builtin_amdgcn_permlane32_swap(__float_as_uint(a), __float_as_uint(b), false, false); a = __uint_as_float(r[0]); b = __uint_as_float(r[1]); }
DI void swap16u(unsigned& a, unsigned& b) { auto r = __builtin_amdgcn_permlane16_swap(a, b, false, false); a = r[0]; b = r[1]; }
template <bool MAX> DI void red2(float& a, float& b) {
    swap16(a, b); float v = MAX ? fmx(a, b) : a + b;
    float u = v; swap32(v, u); v = MAX ? fmx(v, u) : v + u;
    a = v; b = v; swap16(a, b);
}

constexpr int G1_SLOT = 36864;
constexpr int G1_WPHI = 149504;
constexpr int G1_PF = G1_WPHI + 4096;
constexpr int G1_ARR = G1_PF + 256;
constexpr int G1_LDS = G1_ARR + 16;
DI void hedgehog4(const f32x4 (&p)[4][2], bf16x8 (&pos)[4], bf16x8 (&neg)[4]) {
    const float L2E = 1.44269504088896340736f;
    float mx[4], nm[4];
#pragma unroll
    for (int m = 0; m < 4; ++m) {
        mx[m] = fmx(fmx(fmx(fmx(p[m][0][0], p[m][0][1]), p[m][0][2]), fmx(fmx(p[m][0][3], p[m][1][0]), p[m][1][1])), fmx(p[m][1][2], p[m][1][3]));
        nm[m] = fmx(fmx(fmx(fmx(-p[m][0][0], -p[m][0][1]), -p[m][0][2]), fmx(fmx(-p[m][0][3], -p[m][1][0]), -p[m][1][1])), fmx(-p[m][1][2], -p[m][1][3]));
    }
#pragma unroll
    for (int m = 0; m < 4; ++m) red2<true>(mx[m], nm[m]);
    f32x2 ep[4][4], en[4][4];
    float sp[4], sn[4];
#pragma unroll
    for (int m = 0; m < 4; ++m) {
        const float cp = -mx[m] * L2E, cn = -nm[m] * L2E;
#pragma unroll
        for (int i = 0; i < 4; ++i) {
            const f32x2 x = {p[m][i >> 1][2 * (i & 1)], p[m][i >> 1][2 * (i & 1) + 1]};
            const f32x2 a = __builtin_elementwise_fma(x, (f32x2){L2E, L2E}, (f32x2){cp, cp}), b = __builtin_elementwise_fma(x, (f32x2){-L2E, -L2E}, (f32x2){cn, cn});
            ep[m][i] = (f32x2){__builtin_amdgcn_exp2f(a[0]), __builtin_amdgcn_exp2f(a[1])};
            en[m][i] = (f32x2){__builtin_amdgcn_exp2f(b[0]), __builtin_amdgcn_exp2f(b[1])};
        }
        const f32x2 tp = (ep[m][0] + ep[m][1]) + (ep[m][2] + ep[m][3]), tn = (en[m][0] + en[m][1]) + (en[m][2] + en[m][3]);
        float t0 = tp[0], t1 = tn[0];
        OPAQUE(t0); OPAQUE(t1);
        sp[m] = t0 + tp[1]; sn[m] = t1 + tn[1];
    }
#pragma unroll
    for (int m = 0; m < 4; ++m) red2<false>(sp[m], sn[m]);
#pragma unroll
    for (int m = 0; m < 4; ++m) {
        const float rp = __builtin_amdgcn_rcpf(sp[m]), rn = __builtin_amdgcn_rcpf(sn[m]);
        u32x4 a, b;
#pragma unroll
        for (int i = 0; i < 4; ++i) { const f32x2 x = ep[m][i] * rp, y = en[m][i] * rn; a[i] = pk2(x[0], x[1]); b[i] = pk2(y[0], y[1]); }
        pos[m] = __builtin_bit_cast(bf16x8, a); neg[m] = __builtin_bit_cast(bf16x8, b);
    }
}
DI u32x2 glu4(const f32x4& g, const f32x4& u) {
    u32x2 r;
#pragma unroll
    for (int i = 0; i < 2; ++i) {
        const f32x2 t = (f32x2){g[2 * i], g[2 * i + 1]} * -1.44269504088896340736f;
        const f32x2 d = (f32x2){__builtin_amdgcn_exp2f(t[0]), __builtin_amdgcn_exp2f(t[1])} + 1.0f;
        const f32x2 h = (f32x2){u[2 * i], u[2 * i + 1]} * (f32x2){__builtin_amdgcn_rcpf(d[0]), __builtin_amdgcn_rcpf(d[1])};
        r[i] = pk2(h[0], h[1]);
    }
    return r;
}
DI u32x4 glu8(const f32x4& g0, const f32x4& u0, const f32x4& g1, const f32x4& u1) { const u32x2 a = glu4(g0, u0), b = glu4(g1, u1); return (u32x4){a[0], a[1], b[0], b[1]}; }

constexpr int G2_SLOT = 32768;
constexpr int G2_NS = 4;
constexpr int G2_KV = G2_NS * G2_SLOT;
static_assert(G2_KV + 8192 + 256 <= G1_WPHI, "");
#define G2_NA 36
constexpr int G2_NB = 72 - G2_NA, G2_NBH = G2_NB - 16;
static_assert(G2_NA % 4 == 0 && G2_NA >= 32 && G2_NA <= 36, "");
typedef GAS unsigned gu32;
DI void wait_tags(gu32* p, unsigned seen, unsigned tag) {
    if (__builtin_amdgcn_ballot_w64(seen != tag) != 0ull) {
        unsigned spins = 0;
        while (__builtin_amdgcn_ballot_w64(__hip_atomic_load(p, __ATOMIC_RELAXED, __HIP_MEMORY_SCOPE_AGENT) != tag) != 0ull && ++spins < (1u << 18))
            __builtin_amdgcn_s_sleep(1);
    }
}
DI void arrive(LAS unsigned* arr, gu32* word, unsigned tag) {
    const int l = threadIdx.x & 63;
    unsigned old = 0;
    if (l == 0) old = __hip_atomic_fetch_add(arr, 1u, __ATOMIC_RELAXED, __HIP_MEMORY_SCOPE_WORKGROUP);
    if (__builtin_amdgcn_readfirstlane(old) == 7u && l < 4) __hip_atomic_store(word + 64 * l, tag, __ATOMIC_RELAXED, __HIP_MEMORY_SCOPE_AGENT);
}
DI float wsum(float v) {
    v += __builtin_amdgcn_update_dpp(0.f, v, 0xb1, 0xf, 0xf, true);
    v += __builtin_amdgcn_update_dpp(0.f, v, 0x4e, 0xf, 0xf, true);
    v += __builtin_amdgcn_update_dpp(0.f, v, 0x141, 0xf, 0xf, true);
    v += __builtin_amdgcn_update_dpp(0.f, v, 0x140, 0xf, 0xf, true);
    float u = v; swap16(v, u); v += u;
    u = v; swap32(v, u); return v + u;
}

#define LP(a) ((LAS const char*)(uintptr_t)(a))
#define G1_STAGE_A(slot) do { dma2s<(slot) * G1_SLOT>(aptr, avoff, adst); aptr += NMB * 1024; OPAQUE_S(aptr); } while (0)
#define G1_STAGE_B(slot) do { \
        dma1a<(slot) * G1_SLOT>(bptr[0], bvoff[0], bdst); dma1a<(slot) * G1_SLOT + 1024>(bptr[1], bvoff[1], bdst); \
        if (grp == 0) dma1a<(slot) * G1_SLOT + 2048>(bptr[2], bvoff[2], bdst); \
        bptr[0] += bstep[0]; bptr[1] += bstep[1]; bptr[2] += bstep[2]; \
        OPAQUE_S(bptr[0]); OPAQUE_S(bptr[1]); OPAQUE_S(bptr[2]); } while (0)
#define G1_STAGE(slot) do { G1_STAGE_A(slot); G1_STAGE_B(slot); } while (0)
#define G1_WAIT() WAIT_V(8)

template <bool kvt>
DI void tail(const uint16_t* __restrict__ x, const uint16_t* __restrict__ wphi, const uint16_t* __restrict__ wdown, const uint16_t* __restrict__ wo, char* __restrict__ ws,
             uint16_t* __restrict__ out, LAS char* lds, const unsigned tag, const char* aptr, const char* bp0, const char* bp1, const char* bp2,
             unsigned bs0, unsigned bs1, unsigned bs2, const char* pw0, const char* pw1, const char* pw2) {
    int tid = threadIdx.x;
    OPAQUE(tid);
    const int l = tid & 63;
    const int w = __builtin_amdgcn_readfirstlane(tid >> 6);
    const int grp = w >> 2, wr = w & 3, wc = grp;
    const int bid = blockIdx.x, idx = bid >> 3, xcd = bid & 7;
    const int pm = idx >> 2;
    const int j = xcd * 2 + ((idx >> 1) & 1);
    const int F0 = kvt ? 2048 + 96 * j : 128 * j;
    gu32* cnt = (gu32*)(ws + WS_CNT);
    const int G = l >> 4, q4 = (l >> 2) & 3, p4 = l & 3;
    const unsigned lbase = (unsigned)(uintptr_t)lds;
    const rsrc_t rw = mkrsrc(ws, (unsigned)WS_END);
    const char* bptr[3] = {bp0, bp1, bp2};
    const unsigned bstep[3] = {bs0, bs1, bs2};
    unsigned bvoff[3];
#pragma unroll
    for (int i = 0; i < 3; ++i) bvoff[i] = (l >> 2) * (bstep[i] >> 5) + ((((l >> 1) & 1) ^ ((l >> 5) & 1)) * 32) + (l & 1) * 16;
    const unsigned avoff = l * 16, adst = lbase + 2 * w * 1024;
    const unsigned bdst = lbase + 16384 + (grp == 0 ? 3 * w : 12 + 2 * (w - 4)) * 1024;

    const unsigned lp0 = G * 512 + q4 * 64 + ((G & 1) * 32) + p4 * 8;
    const unsigned lp1 = lp0 ^ 32;
    unsigned rA = lbase + wr * 4096 + l * 16;
    unsigned rB0 = lbase + 16384 + 5 * wc * 2048 + lp0;
    unsigned rB1 = lbase + 16384 + 5 * wc * 2048 + lp1;
    unsigned rB8 = kvt ? lbase + 16384 + 2 * 2048 + (wc ? lp1 : lp0) : rB0 + 2 * 2048;
    unsigned rB9 = kvt ? lbase + 16384 + 7 * 2048 + (wc ? lp1 : lp0) : rB1 + 2 * 2048;
#define G1_MOVE(d) do { rA += (d); rB0 += (d); rB1 += (d); rB8 += (d); rB9 += (d); \
        OPAQUE(rA); OPAQUE(rB0); OPAQUE(rB1); OPAQUE(rB8); OPAQUE(rB9); } while (0)

    f32x4 acc[4][10];
#pragma unroll
    for (int m = 0; m < 4; ++m)
#pragma unroll
        for (int n = 0; n < 10; ++n) acc[m][n] = (f32x4){0.f, 0.f, 0.f, 0.f};
    bf16x8 fa[4], fb[10];

#define G1_READ(so) do { \
        _Pragma("unroll") for (int m = 0; m < 4; ++m) fa[m] = *(LAS const bf16x8*)(LP(rA) + (so) + m * 1024); \
        _Pragma("unroll") for (int u = 0; u < 5; ++u) if (u != 2) { \
            fb[2 * u] = tr_frag(LP(rB0) + (so) + u * 2048, LP(rB0) + (so) + u * 2048 + 256); \
            fb[2 * u + 1] = tr_frag(LP(rB1) + (so) + u * 2048, LP(rB1) + (so) + u * 2048 + 256); } \
        fb[4] = tr_frag(LP(rB8) + (so), LP(rB8) + (so) + 256); \
        fb[5] = tr_frag(LP(rB9) + (so), LP(rB9) + (so) + 256); } while (0)
#define G1_MMA() do { __builtin_amdgcn_s_setprio(1); \
        _Pragma("unroll") for (int m = 0; m < 4; ++m) _Pragma("unroll") for (int n_ = 0; n_ < 10; ++n_) { const int n = (m & 1) ? 9 - n_ : n_; \
            if (kvt && (n == 2 || n == 3)) MFMA16_ACC(acc[m][n], fa[m], fb[n]); else MFMA16_ACC(acc[m][n], fb[n], fa[m]); } \
        __builtin_amdgcn_s_setprio(0); } while (0)
#define G1_PHASE(so, STG, VW) do { G1_READ(so); STG; WAIT_L0(); VW; SCHED(); BAR(); SCHED(); G1_MMA(); SCHED(); BAR(); SCHED(); } while (0)

    G1_MOVE(0);
#pragma unroll
    for (int m = 0; m < 4; ++m)
#pragma unroll
        for (int n = 0; n < 10; ++n) OPAQUE(acc[m][n]);
    SCHED();
    wait_tags(cnt + 64 * (C_LN + pm * 8 + w) + (l & 3), tag - 1u, tag);
    STAMP(0, 3);
    G1_STAGE_A(0); G1_STAGE_A(1);
    WAIT_V(2);
    BAR();
    G1_STAGE(2);
    if (grp == 1) dma1<0>((const char*)wphi + (size_t)(2 * j) * 2048 + wr * 1024, avoff, lbase + G1_WPHI + wr * 1024);
    STAMP(1, 1);
    if (grp == 1) BAR();
    SCHED();
    for (int it = 0; it < 7; ++it) {
        if (w == 0) STAMP(4, it);
        if (w >= 6) {
            const int c = bid * 2 + w - 6;
            const char* g; int lim;
            if (it < 4) { lim = 480; g = c < 144 ? pw0 + (size_t)(192 + it * 144 + c) * 8192 : (c < 312 ? pw1 - 144 * 8192 : pw2 - 312 * 8192) + (size_t)(224 + it * 168 + c) * 8192; }
            else { const int u = (it - 4) * 384 + c; lim = 384; g = u < 896 ? (const char*)wdown + (size_t)u * 8192 : (const char*)wo + (size_t)(u - 896) * 8192; }
            if (c < lim) pf_lines(g, avoff * 8, lbase + G1_PF, ~0ull);
        }
        G1_PHASE(0, G1_STAGE(3), G1_WAIT());
        G1_PHASE(G1_SLOT, G1_STAGE(0), G1_WAIT());
        G1_MOVE(2 * G1_SLOT);
        G1_PHASE(0, G1_STAGE(1), G1_WAIT());
        G1_PHASE(G1_SLOT, G1_STAGE(2), G1_WAIT());
        G1_MOVE(-2 * G1_SLOT);
    }
    if (w == 0) STAMP(4, 7);
    G1_PHASE(0, G1_STAGE(3), G1_WAIT());
    G1_PHASE(G1_SLOT, (void)0, WAIT_V(4));
    G1_MOVE(2 * G1_SLOT);
    G1_PHASE(0, (void)0, WAIT_V(0));
    G1_READ(G1_SLOT); WAIT_L0(); SCHED(); BAR(); SCHED(); G1_MMA(); SCHED();
    if (grp == 0) BAR();
    asm volatile("s_nop 7\n\ts_nop 7" ::: "memory");
    SCHED();
    STAMP(1, 2);
    if (kvt) STAMP(2, 0);

    constexpr bool tb = !kvt;
    const int t16 = idx >> 1;
    const int pm2 = (xcd >> 1) * 4 + (t16 & 3), pn = (xcd & 1) * 4 + (t16 >> 2);
    const int tile = pm2 * 8 + pn;
    const int prep = 4 * (pm2 >> 1) + (xcd & 1) * 2 + (wr & 1);
    constexpr int k0 = tb ? 0 : G2_NBH;
    const int col0 = pn * 128;
    const unsigned lo16 = l * 16;
    const bool isb = wr >= 2;
    const char* gptr = isb ? (const char*)(wdown + (size_t)(64 * k0 + 32 * (wr - 2)) * D_ + col0) : ws + WS_A2 + ((size_t)(2 * k0 + wr) * NMB + pm2 * 8) * 1024;
    const char* gptr_o = isb ? (const char*)(wo + (size_t)(32 * (wr & 1)) * D_ + col0) : ws + WS_A2 + ((size_t)(112 + wr) * NMB + pm2 * 8) * 1024;
    const unsigned gstep = isb ? 64 * D_ * 2 : 2 * NMB * 1024;
    unsigned voff[4];
#pragma unroll
    for (int i = 0; i < 4; ++i) {
        const int r = 4 * i + G;
        voff[i] = isb ? r * (D_ * 2) + ((((l & 15) >> 1) ^ (r & 7)) * 32) + (l & 1) * 16 - 1024 * i : lo16;
    }
    const unsigned ghalf = isb ? 16 * D_ * 2 : 4096;
    unsigned dst = lbase + wr * 8192;
    int tld = 0, slot = 0;
#define G2_DMA4_(g, d, SC) asm volatile("s_mov_b32 m0, %5\n\ts_nop 0\n\tglobal_load_lds_dwordx4 %0, %4" SC "\n\tglobal_load_lds_dwordx4 %1, %4 offset:1024" SC "\n\t" \
            "global_load_lds_dwordx4 %2, %4 offset:2048" SC "\n\tglobal_load_lds_dwordx4 %3, %4 offset:3072" SC \
            :: "v"(voff[0]), "v"(voff[1]), "v"(voff[2]), "v"(voff[3]), "s"(g), "s"(d) : "memory")
#define G2_DMA4(g, d) G2_DMA4_(g, d, " sc1")
#define G2_NEXTSLOT() do { ++slot; const int wrap = slot == G2_NS; slot = wrap ? 0 : slot; dst += wrap ? (unsigned)(-(G2_NS - 1) * G2_SLOT) : (unsigned)G2_SLOT; } while (0)
#define G2_W 4
    const int rot = G2_W == 1 ? 0 : ((t16 & 3) + (t16 >> 2)) * (G2_W / 4);
#define G2_STAGE() do { const char* g_ = gptr + (size_t)((tld + rot) & (G2_W - 1)) * gstep; \
        if (!isb) { G2_DMA4(g_, dst); G2_DMA4(g_ + ghalf, dst + 4096); } else { G2_DMA4_(g_, dst, ""); G2_DMA4_(g_ + ghalf, dst + 4096, ""); } \
        ++tld; gptr = (tb && tld == G2_NBH) ? gptr_o : (tld & (G2_W - 1)) == 0 ? gptr + G2_W * gstep : gptr; G2_NEXTSLOT(); } while (0)
#define G2_PRUN(n) do { _Pragma("unroll 1") for (int t_ = 0; t_ < (n); ++t_) { WAIT_V(16); BAR(); G2_STAGE(); } } while (0)
#define POLL(dst_, p_) asm volatile("global_load_dword %0, %1, off sc1" : "=v"(dst_) : "v"(p_) : "memory")
    const size_t off0 = (size_t)(pm2 * 128 + (wr >> 1) * 64 + (l & 15)) * D_ + col0 + (wr & 1) * 64 + (G & 1) * 16 + (G >> 1) * 8;

    const int fq = l >> 4, fr = l & 15;
    const int hh = 2 * j + wc;
    const int mb0 = pm * 16 + wr * 4;
    if (w >= 6) { G2_STAGE(); G2_STAGE(); }
    const f32x4 z4 = {0.f, 0.f, 0.f, 0.f};
    const unsigned hb = (unsigned)WS_A2 + (((kvt ? F0 + 32 * wc : F0 + 64 * wc + 32) >> 5) * NMB + mb0) * 1024;
    f32x4 pf[4][2];
    bf16x8 pos[4], neg[4];
    u32x4 xv[4][2];
#define G1_LOGITS() do { bf16x8 wp[2]; \
        _Pragma("unroll") for (int c = 0; c < 2; ++c) { LAS const char* p = lds + G1_WPHI + wc * 2048 + (4 * G + q4) * 64 + c * 32 + p4 * 8; wp[c] = tr_frag(p, p + 1024); } \
        _Pragma("unroll") for (int m = 0; m < 4; ++m) { const bf16x8 qf = pack8(acc[m][0], acc[m][1]); pf[m][0] = MFMA16(wp[0], qf, z4); pf[m][1] = MFMA16(wp[1], qf, z4); } } while (0)
    __builtin_amdgcn_s_setprio(1);
    if constexpr (!kvt) {
#pragma unroll
        for (int m = 0; m < 4; ++m) {
            stw(glu8(acc[m][2], acc[m][4], acc[m][3], acc[m][5]), rw, lo16 + m * 1024, hb - NMB * 1024);
            stw(glu8(acc[m][6], acc[m][8], acc[m][7], acc[m][9]), rw, lo16 + m * 1024, hb);
        }
        SCHED();
        G1_LOGITS();
        SCHED();
        WAIT_V(0); arrive((LAS unsigned*)(lds + G1_ARR), cnt + 64 * (C_HQ + 4 * pm) + j, tag);
        __builtin_amdgcn_s_setprio(0);
        SCHED();
        STAMP(1, 5);
        if (w == 4 || w == 5) { wait_tags(cnt + 64 * (C_HQ + prep) + (l & 15), tag - 1u, tag); STAMP(6, 0); G2_STAGE(); G2_STAGE(); }
        hedgehog4(pf, pos, neg);
    } else {
        G1_LOGITS();
        u32x4 i0 = {0u, 0u, 0u, 0u}, i1 = {0u, 0u, 0u, 0u};
        {
            const int d = fr - 4 * fq;
            const unsigned lo = (d == 0) ? 0x3f80u : (d == 1) ? 0x3f800000u : 0u;
            const unsigned hi = (d == 2) ? 0x3f80u : (d == 3) ? 0x3f800000u : 0u;
            i0[0] = lo; i0[1] = hi; i1[2] = lo; i1[3] = hi;
        }
        const bf16x8 I0 = __builtin_bit_cast(bf16x8, i0), I1 = __builtin_bit_cast(bf16x8, i1);
        constexpr int RED = 65536, ZRED = RED + 65536, HX = ZRED + 2048;
        static_assert(HX + 16384 <= G1_WPHI, "");
        bf16x8 V[2][2];
#pragma unroll
        for (int mp = 0; mp < 2; ++mp) { V[mp][0] = pack8(acc[2 * mp][2], acc[2 * mp + 1][2]); V[mp][1] = pack8(acc[2 * mp][3], acc[2 * mp + 1][3]); }
        SCHED();
        STAMP(2, 1);
        hedgehog4(pf, pos, neg);
        SCHED();
        STAMP(2, 2);
        const u32x4 o4 = {0x3f803f80u, 0x3f803f80u, 0x3f803f80u, 0x3f803f80u};
        const bf16x8 ONES = __builtin_bit_cast(bf16x8, o4);
        f32x4 T[4][2][2];
#pragma unroll
        for (int m = 0; m < 4; ++m) {
            T[m][0][0] = MFMA16(pos[m], I0, z4); T[m][0][1] = MFMA16(pos[m], I1, z4);
            T[m][1][0] = MFMA16(neg[m], I0, z4); T[m][1][1] = MFMA16(neg[m], I1, z4);
        }
#pragma unroll
        for (int m = 0; m < 4; ++m) *(LAS u32x2*)(lds + HX + (wr * 4 + m) * 1024 + lo16 + 8 * wc) = glu4(acc[m][4], acc[m][5]);
        LAS char* red = lds + RED + (wc * 4 + wr) * 8192 + lo16;
#pragma unroll
        for (int s = 0; s < 2; ++s)
#pragma unroll
            for (int cc = 0; cc < 2; ++cc) {
                const bf16x8 P0 = pack8(T[0][s][cc], T[1][s][cc]), P1 = pack8(T[2][s][cc], T[3][s][cc]);
                const f32x4 k0_ = MFMA16(P1, V[1][0], MFMA16(P0, V[0][0], z4));
                const f32x4 k1_ = MFMA16(P1, V[1][1], MFMA16(P0, V[0][1], z4));
                const f32x4 zz = MFMA16(P1, ONES, MFMA16(P0, ONES, z4));
                stw(glu8(acc[2 * s + cc][6], acc[2 * s + cc][8], acc[2 * s + cc][7], acc[2 * s + cc][9]), rw, lo16 + (2 * s + cc) * 1024, hb);
                *(LAS f32x4*)(red + ((s * 2 + 0) * 2 + cc) * 1024) = k0_;
                *(LAS f32x4*)(red + ((s * 2 + 1) * 2 + cc) * 1024) = k1_;
                *(LAS f32x4*)(lds + ZRED + (wc * 4 + wr) * 256 + (s * 32 + cc * 16 + 4 * fq) * 4) = zz;
            }
        SCHED();
        if (w < 4) {
#pragma unroll
            for (int m = 0; m < 4; ++m) { xv[m][0] = *(const u32x4*)(x + off0 + m * 16 * D_); xv[m][1] = *(const u32x4*)(x + off0 + m * 16 * D_ + 32); }
        } else if (w < 6) { wait_tags(cnt + 64 * (C_HQ + prep) + (l & 15), tag - 1u, tag); G2_STAGE(); G2_STAGE(); }
        WAIT_L0(); SCHED();
        STAMP(2, 3);
        BAR(); SCHED();
        STAMP(2, 4);
        const unsigned kvo = (unsigned)WS_KVP + (pm * 32 + hh) * KVP_SZ;
        f32x4 sum[2];
#pragma unroll
        for (int i = 0; i < 2; ++i) {
            LAS const char* p = lds + RED + wc * 32768 + (2 * wr + i) * 1024 + lo16;
            sum[i] = (*(LAS const f32x4*)p + *(LAS const f32x4*)(p + 8192)) + (*(LAS const f32x4*)(p + 16384) + *(LAS const f32x4*)(p + 24576));
            stw(*(LAS const u32x4*)(lds + HX + (wr * 4 + 2 * wc + i) * 1024 + lo16), rw, lo16 + i * 1024, (unsigned)WS_A2 + (((F0 + 64) >> 5) * NMB + mb0 + 2 * wc) * 1024);
        }
        stw(pack8(sum[0], sum[1]), rw, lo16, kvo + wr * 1024);
        {
            LAS const char* p = lds + ZRED + wc * 1024 + (l & 15) * 16;
            const f32x4 zs = (*(LAS const f32x4*)p + *(LAS const f32x4*)(p + 256)) + (*(LAS const f32x4*)(p + 512) + *(LAS const f32x4*)(p + 768));
            stw(zs, rw, (wr == 0 && l < 16) ? lo16 : 0x80000000u, kvo + 4096);
        }
        __builtin_amdgcn_s_setprio(0);
        WAIT_L0();
    }
    SCHED();
    STAMP(1, 6);

#define G2_OCOMP() do { \
        bf16x8 kf[2][2], zf[2]; \
        _Pragma("unroll") for (int s = 0; s < 2; ++s) { \
            kf[s][0] = *(LAS const bf16x8*)(lds + G2_KV + wc * 4096 + (s * 2) * 1024 + lo16); kf[s][1] = *(LAS const bf16x8*)(lds + G2_KV + wc * 4096 + (s * 2 + 1) * 1024 + lo16); \
            const u32x2 za = *(LAS const u32x2*)(lds + G2_KV + 8192 + wc * 128 + s * 64 + G * 8), zb = *(LAS const u32x2*)(lds + G2_KV + 8192 + wc * 128 + s * 64 + 32 + G * 8); \
            zf[s] = __builtin_bit_cast(bf16x8, (u32x4){za[0], za[1], zb[0], zb[1]}); } \
        _Pragma("unroll") for (int m = 0; m < 4; ++m) { \
            const f32x4 dn = MFMA16(zf[1], neg[m], MFMA16(zf[0], pos[m], z4)); \
            const f32x4 n0 = MFMA16(kf[1][0], neg[m], MFMA16(kf[0][0], pos[m], z4)), n1 = MFMA16(kf[1][1], neg[m], MFMA16(kf[0][1], pos[m], z4)); \
            const float rd = __builtin_amdgcn_rcpf(dn[0] + 1e-6f); \
            stw(pack8(n0 * rd, n1 * rd), rw, lo16 + m * 1024, (unsigned)WS_A2 + ((112 + hh) * NMB + mb0) * 1024); } \
        WAIT_L0(); SCHED(); } while (0)

    if (w >= 4) {
        unsigned seen;
        WAIT_V(8);
        STAMP(5 + tb, 1);
        BAR(); G2_STAGE();
        WAIT_V(8); BAR();
        if (!tb && w == 7 && l < 8) __hip_atomic_store(l < 4 ? cnt + 64 * (C_KV + 4 * j + l) + pm : cnt + 64 * (C_HK + 4 * pm + l - 4) + j, tag, __ATOMIC_RELAXED, __HIP_MEMORY_SCOPE_AGENT);
        G2_STAGE(); G2_STAGE();
        if constexpr (!tb) {
            constexpr int UK = 32 - G2_NBH - 4;
            G2_PRUN(UK - 3);
            POLL(seen, cnt + 64 * (C_HK + prep) + (l & 15));
            G2_PRUN(2);
            asm volatile("s_waitcnt vmcnt(16)" : "+v"(seen) :: "memory");
            if (!isb) wait_tags(cnt + 64 * (C_HK + prep) + (l & 15), seen, tag);
            BAR(); G2_STAGE();
            G2_PRUN(G2_NA - 5 - UK);
            WAIT_V(16); BAR(); WAIT_V(8); BAR(); WAIT_V(0); BAR();
        } else {
            G2_PRUN(1);
            POLL(seen, cnt + 64 * (C_KV + 4 * j + wr) + (l & 7));
            G2_PRUN(2);
            asm volatile("s_waitcnt vmcnt(16)" : "+v"(seen) :: "memory");
            wait_tags(cnt + 64 * (C_KV + 4 * j + wr) + (l & 7), seen, tag);
            u32x4 t[2][8]; float zt[8];
            const char* kvs = ws + WS_KVP + (size_t)(2 * j + (wr >> 1)) * KVP_SZ;
            const unsigned kvv = (wr & 1) * 2048 + lo16, zv = 4096 + l * 4;
#define G2_KVLD(i) do { _Pragma("unroll") for (int k = 0; k < 8; ++k) \
                asm volatile("global_load_dwordx4 %0, %1, %2 offset:%3 sc1" : "=v"(t[i][k]) : "v"(kvv), "s"(kvs + (size_t)k * 32 * KVP_SZ), "i"((i) * 1024) : "memory"); \
            _Pragma("unroll") for (int k = 4 * (i); k < 4 * (i) + 4; ++k) \
                asm volatile("global_load_dword %0, %1, %2 sc1" : "=v"(zt[k]) : "v"(zv), "s"(kvs + (size_t)k * 32 * KVP_SZ) : "memory"); } while (0)
            BAR(); G2_STAGE(); G2_KVLD(0);
            WAIT_V(28); BAR(); G2_STAGE(); G2_KVLD(1);
            WAIT_V(40); BAR(); G2_STAGE();
            WAIT_V(40); BAR(); G2_STAGE();
            WAIT_V(28); BAR(); G2_STAGE();
            WAIT_V(16); SCHED();
#pragma unroll
            for (int i = 0; i < 2; ++i) {
                asm volatile("" : "+v"(t[i][0]), "+v"(t[i][1]), "+v"(t[i][2]), "+v"(t[i][3]), "+v"(t[i][4]), "+v"(t[i][5]), "+v"(t[i][6]), "+v"(t[i][7]),
                             "+v"(zt[4 * i]), "+v"(zt[4 * i + 1]), "+v"(zt[4 * i + 2]), "+v"(zt[4 * i + 3]));
                u32x4 r;
#pragma unroll
                for (int e = 0; e < 4; ++e) {
                    float lo = 0.f, hi = 0.f;
#pragma unroll
                    for (int k = 0; k < 8; ++k) { lo += bflo(t[i][k][e]); hi += bfhi(t[i][k][e]); }
                    r[e] = pk2(lo, hi);
                }
                *(LAS u32x4*)(lds + G2_KV + (wr >> 1) * 4096 + (2 * (wr & 1) + i) * 1024 + lo16) = r;
            }
            const float z = ((zt[0] + zt[1]) + (zt[2] + zt[3])) + ((zt[4] + zt[5]) + (zt[6] + zt[7]));
            *(LAS unsigned short*)(lds + G2_KV + 8192 + (wr >> 1) * 128 + l * 2) = (unsigned short)pk2(z, z);
            WAIT_L0(); SCHED();
            BAR(); G2_STAGE();
            G2_OCOMP();
            G2_PRUN(3);
            if (w == 7 && l < 4) __hip_atomic_store(cnt + 64 * (C_O + 4 * pm + l) + j, tag, __ATOMIC_RELAXED, __HIP_MEMORY_SCOPE_AGENT);
            G2_PRUN(G2_NB - 35);
            gu32* f0 = cnt + 64 * (C_O + prep) + (l & 15);
            POLL(seen, f0);
            G2_PRUN(2);
            asm volatile("s_waitcnt vmcnt(16)" : "+v"(seen) :: "memory");
            STAMP(6, 2);
            if (!isb) wait_tags(f0, seen, tag);
            STAMP(6, 3);
            BAR(); G2_STAGE();
            G2_PRUN(15);
            gu32* f1 = cnt + 64 * (C_P + tile);
            WAIT_V(16); BAR(); WAIT_V(8); BAR();
            POLL(seen, f1);
            WAIT_V(1); BAR();
            asm volatile("s_waitcnt vmcnt(0)" : "+v"(seen) :: "memory");
            STAMP(6, 4);
            wait_tags(f1, seen, tag);
            STAMP(6, 5);
            char* part = ws + WS_PART + (size_t)tile * 32768;
#pragma unroll
            for (int i = 0; i < 4; ++i) voff[i] = lo16;
            G2_DMA4(part + wr * 8192, lbase + wr * 8192); G2_DMA4(part + wr * 8192 + 4096, lbase + wr * 8192 + 4096);
            WAIT_V(0);
            BAR();
        }
        STAMP(3, 7);
        return;
    }
    const int cr = w >> 1, cc2 = w & 1;
    f32x4 ac2[4][4];
#pragma unroll
    for (int m = 0; m < 4; ++m)
#pragma unroll
        for (int i = 0; i < 2; ++i) {
            if constexpr (tb) { ac2[m][2 * i] = z4; ac2[m][2 * i + 1] = z4; }
            else {
                unsigned d0 = xv[m][i][0], d1 = xv[m][i][1], d2 = xv[m][i][2], d3 = xv[m][i][3];
                swap16u(d0, d2); swap16u(d1, d3);
                ac2[m][2 * i] = (f32x4){bflo(d0), bfhi(d0), bflo(d1), bfhi(d1)}; ac2[m][2 * i + 1] = (f32x4){bflo(d2), bfhi(d2), bflo(d3), bfhi(d3)};
            }
        }
    const int r8 = 4 * (G & 1) + q4;
    unsigned sA = lbase + cr * 4096 + lo16;
    unsigned sB[4];
#pragma unroll
    for (int n = 0; n < 4; ++n) sB[n] = lbase + 16384 + (4 * G + q4) * 256 + (((4 * cc2 + n) ^ r8) * 32) + p4 * 8;
#define G2_MOVE(d) do { sA += (d); sB[0] += (d); sB[1] += (d); sB[2] += (d); sB[3] += (d); OPAQUE(sA); OPAQUE(sB[0]); OPAQUE(sB[1]); OPAQUE(sB[2]); OPAQUE(sB[3]); } while (0)
    bf16x8 ga[2][2][4], gb[2][2][4];
#define G2_RD_A(buf, so, kk, m) ga[buf][kk][m] = *(LAS const bf16x8*)(LP(sA) + (so) + (kk) * 8192 + (m) * 1024)
#define G2_RD_B(buf, so, kk, n) gb[buf][kk][n] = tr_frag(LP(sB[n]) + (so) + (kk) * 8192, LP(sB[n]) + (so) + (kk) * 8192 + 4096)
#define G2_READ(buf, so) do { _Pragma("unroll") for (int kk = 0; kk < 2; ++kk) _Pragma("unroll") for (int i = 0; i < 4; ++i) { G2_RD_B(buf, so, kk, i); G2_RD_A(buf, so, kk, i); } } while (0)
#define G2_STEP(cur, so, RD) do { if (RD) BAR(); SCHED(); \
        _Pragma("unroll") for (int kk = 0; kk < 2; ++kk) _Pragma("unroll") for (int m = 0; m < 4; ++m) _Pragma("unroll") for (int n_ = 0; n_ < 4; ++n_) { \
            const int n = (m & 1) ? 3 - n_ : n_; \
            MFMA16_ACC(ac2[m][n], gb[cur][kk][n], ga[cur][kk][m]); \
            if ((RD) && kk == 0) { const int i_ = m * 4 + n_; if (i_ & 1) G2_RD_A((cur) ^ 1, so, i_ >> 3, (i_ >> 1) & 3); else G2_RD_B((cur) ^ 1, so, i_ >> 3, (i_ >> 1) & 3); } \
            SCHED(); } \
        WAIT_L0(); SCHED(); } while (0)
    unsigned mv = 2 * G2_SLOT;
#define G2_RUN(n) do { _Pragma("unroll 1") for (int i_ = 0; i_ < (n); ++i_) { G2_STEP(0, G2_SLOT, 1); G2_MOVE(mv); mv = -mv; G2_STEP(1, 0, 1); } } while (0)

    G2_MOVE(0);
    BAR(); SCHED();
    G2_READ(0, 0);
    WAIT_L0(); SCHED();
    STAMP(3, 0); STAMP(3, 1);
    if constexpr (!tb) WAIT_V(0);
    if constexpr (tb) {
        G2_RUN(5);
        G2_OCOMP();
        G2_RUN(1);
        WAIT_V(0); SCHED();
        G2_RUN(G2_NB / 2 - 7);
    } else {
        G2_RUN(G2_NA / 2 - 1);
    }
    G2_STEP(0, G2_SLOT, 1);
    G2_STEP(1, 0, 0);
    asm volatile("s_nop 7\n\ts_nop 7" ::: "memory");
    SCHED();
    if constexpr (!tb) {
        STAMP(3, 2);
#pragma unroll
        for (int m = 0; m < 4; ++m)
#pragma unroll
            for (int i = 0; i < 2; ++i) stw(pack8(ac2[m][2 * i], ac2[m][2 * i + 1]), rw, lo16 + (m & 1) * 2048 + i * 1024, (unsigned)WS_PART + tile * 32768 + w * 8192 + (m >> 1) * 4096);
        WAIT_V(0); BAR();
        if (tid == 0) __hip_atomic_store(cnt + 64 * (C_P + tile), tag, __ATOMIC_RELAXED, __HIP_MEMORY_SCOPE_AGENT);
        STAMP(3, 6);
    } else {
        STAMP(3, 4);
        BAR(); SCHED();
#pragma unroll
        for (int m = 0; m < 4; ++m)
#pragma unroll
            for (int i = 0; i < 2; ++i) {
                const u32x4 pp = *(LAS const u32x4*)(lds + w * 8192 + (m * 2 + i) * 1024 + lo16);
                const f32x4 r0 = ac2[m][2 * i] + (f32x4){bflo(pp[0]), bfhi(pp[0]), bflo(pp[1]), bfhi(pp[1])};
                const f32x4 r1 = ac2[m][2 * i + 1] + (f32x4){bflo(pp[2]), bfhi(pp[2]), bflo(pp[3]), bfhi(pp[3])};
                unsigned d0 = pk2(r0[0], r0[1]), d1 = pk2(r0[2], r0[3]), d2 = pk2(r1[0], r1[1]), d3 = pk2(r1[2], r1[3]);
                swap16u(d0, d2); swap16u(d1, d3);
                *(u32x4*)(out + off0 + m * 16 * D_ + i * 32) = (u32x4){d0, d1, d2, d3};
            }
        if (bid == 0 && tid == 0) __hip_atomic_store(cnt + 64 * C_E, tag, __ATOMIC_RELAXED, __HIP_MEMORY_SCOPE_AGENT);
        STAMP(3, 5);
    }
}

__global__ __launch_bounds__(512, 2) void k_main(const uint16_t* __restrict__ x, const uint16_t* __restrict__ wqkv, const uint16_t* __restrict__ wphi,
                                                const uint16_t* __restrict__ wgate, const uint16_t* __restrict__ wup, const uint16_t* __restrict__ wdown,
                                                const uint16_t* __restrict__ wo, char* __restrict__ ws, uint16_t* __restrict__ out) {
    __shared__ __attribute__((aligned(16))) char smem_[G1_LDS];
    LAS char* lds = (LAS char*)smem_;
    const int tid = threadIdx.x, l = tid & 63;
    const int w = __builtin_amdgcn_readfirstlane(tid >> 6);
    const int grp = w >> 2;
    const int wr = w & 3, wc = grp;
    const int bid = blockIdx.x, idx = bid >> 3, xcd = bid & 7;
    const int pm = idx >> 2;
    const int j = xcd * 2 + ((idx >> 1) & 1);
    const bool kvt = (idx & 1) != 0;
    const int F0 = kvt ? 2048 + 96 * j : 128 * j;
    gu32* cnt = (gu32*)(ws + WS_CNT);
    STAMP(0, 0);
    const int lnslot = xcd * 4 + (idx & 3);
    u32x4 xa, xb; unsigned tag;
    {
        const char* xr = (const char*)x + (size_t)(pm * 256 + lnslot * 8 + w) * 2048;
        asm volatile("global_load_dwordx4 %0, %3, %4\n\tglobal_load_dwordx4 %1, %3, %4 offset:1024\n\tglobal_load_dword %2, %5, off sc1"
                     : "=&v"(xa), "=&v"(xb), "=&v"(tag) : "v"(l * 16), "s"(xr), "v"(cnt + 64 * C_E) : "memory");
    }

    const char* aptr = ws + WS_XN + (size_t)(pm * 16 + 2 * w) * 1024;
    const unsigned avoff = l * 16;
    const char* bptr[3]; unsigned bstep[3], bvoff[3];
    const unsigned bdst = (unsigned)(uintptr_t)lds + 16384 + (grp == 0 ? 3 * w : 12 + 2 * (w - 4)) * 1024;
#pragma unroll
    for (int i = 0; i < 3; ++i) {
        int t = grp == 0 ? 3 * w + i : 12 + 2 * (w - 4) + (i < 2 ? i : 0);
        const unsigned e = (unsigned)((kvt ? 0x491898502061204ull : 0xcb18a1412442040ull) >> (6 * (t >> 1))) & 63u;
        const unsigned mat = e & 3u, stride = mat == 0 ? 3 * D_ : F_;
        const uintptr_t base = mat == 0 ? (uintptr_t)wqkv : mat == 1 ? (uintptr_t)wgate : (uintptr_t)wup;
        bptr[i] = (const char*)(base + ((t & 1) * 16 * stride + (mat == 0 ? 64 * j : F0) + ((e >> 2) & 3u) * 1024 + (e >> 4) * 32) * 2u);
        bstep[i] = 64u * stride;
        bvoff[i] = (l >> 2) * stride * 2 + ((((l >> 1) & 1) ^ ((l >> 5) & 1)) * 32) + (l & 1) * 16;
    }
    const unsigned lbase = (unsigned)(uintptr_t)lds;
    const unsigned adst = lbase + 2 * w * 1024;
    STAMP(1, 0);
    if (tid < 2) *(LAS unsigned*)(lds + G1_ARR + tid * 4) = 0u;
    G1_STAGE_B(0); G1_STAGE_B(1);
    if (grp == 0) WAIT_V(6); else WAIT_V(4);
    asm volatile("" : "+v"(xa), "+v"(xb), "+v"(tag) :: "memory");
    tag = __builtin_amdgcn_readfirstlane(tag) + 1u;
    const rsrc_t rw = mkrsrc(ws, (unsigned)WS_END);
    STAMP(0, 1);
    {
        float f[16];
#pragma unroll
        for (int i = 0; i < 4; ++i) { f[2 * i] = bflo(xa[i]); f[2 * i + 1] = bfhi(xa[i]); f[8 + 2 * i] = bflo(xb[i]); f[9 + 2 * i] = bfhi(xb[i]); }
        float sm = 0.f;
#pragma unroll
        for (int i = 0; i < 16; ++i) sm += f[i];
        const float mean = wsum(sm) * (1.0f / 1024.0f);
        float q = 0.f;
#pragma unroll
        for (int i = 0; i < 16; ++i) { const float d = f[i] - mean; q += d * d; }
        const float rs = __builtin_amdgcn_rcpf(bfr(__builtin_amdgcn_sqrtf(bfr(bfr(wsum(q) * (1.0f / 1024.0f)) + 1e-5f))));
        const float mb = bfr(mean);
#pragma unroll
        for (int i = 0; i < 8; ++i) { const unsigned d2 = pk2(f[2 * i] - mb, f[2 * i + 1] - mb); f[2 * i] = bflo(d2); f[2 * i + 1] = bfhi(d2); }
        LAS char* ex = lds + 3 * G1_SLOT;
        *(LAS u32x4*)(ex + l * 144 + w * 16) = (u32x4){pk2(f[0] * rs, f[1] * rs), pk2(f[2] * rs, f[3] * rs), pk2(f[4] * rs, f[5] * rs), pk2(f[6] * rs, f[7] * rs)};
        *(LAS u32x4*)(ex + (64 + l) * 144 + w * 16) = (u32x4){pk2(f[8] * rs, f[9] * rs), pk2(f[10] * rs, f[11] * rs), pk2(f[12] * rs, f[13] * rs), pk2(f[14] * rs, f[15] * rs)};
        WAIT_L0(); SCHED(); BAR(); SCHED();
#pragma unroll
        for (int i = 0; i < 2; ++i) {
            const int c = i * 64 + (tid >> 3), r = tid & 7;
            stw(*(LAS const u32x4*)(ex + c * 144 + r * 16), rw, ((c >> 2) * NMB * 1024) + ((c & 3) * 16 + r) * 16, (unsigned)WS_XN + (pm * 16 + (lnslot >> 1)) * 1024 + (lnslot & 1) * 128);
        }
        SCHED();
        WAIT_V(0);
        SCHED(); BAR(); SCHED();
        STAMP(0, 2);
        if (tid == 0) __hip_atomic_store(cnt + 64 * (C_LN + pm * 8 + (lnslot >> 2)) + (lnslot & 3), tag, __ATOMIC_RELAXED, __HIP_MEMORY_SCOPE_AGENT);
        SCHED();
    }
    if (kvt) tail<true>(x, wphi, wdown, wo, ws, out, lds, tag, aptr, bptr[0], bptr[1], bptr[2], bstep[0], bstep[1], bstep[2], (const char*)wqkv, (const char*)wgate, (const char*)wup);
    else tail<false>(x, wphi, wdown, wo, ws, out, lds, tag, aptr, bptr[0], bptr[1], bptr[2], bstep[0], bstep[1], bstep[2], (const char*)wqkv, (const char*)wgate, (const char*)wup);
}

extern "C" void kernel_launch(void* const* d_in, const int* in_sizes, int n_in, void* d_out, int out_size, void* d_ws, size_t ws_size,
                              hipStream_t stream) {
    const uint16_t* x = (const uint16_t*)d_in[0];
    const uint16_t* wqkv = (const uint16_t*)d_in[1];
    const uint16_t* wphi = (const uint16_t*)d_in[2];
    const uint16_t* wo = (const uint16_t*)d_in[3];
    const uint16_t* wgate = (const uint16_t*)d_in[5];
    const uint16_t* wup = (const uint16_t*)d_in[6];
    const uint16_t* wdown = (const uint16_t*)d_in[7];
    char* ws = (char*)d_ws;
    k_main<<<256, 512, 0, stream>>>(x, wqkv, wphi, wgate, wup, wdown, wo, ws, (uint16_t*)d_out);
}
```

```cpp
#include <hip/hip_runtime.h>
#include <hip/hip_bf16.h>
#include <stdint.h>

#define LAS __attribute__((address_space(3)))
#define GAS __attribute__((address_space(1)))
#define DI __device__ __forceinline__
typedef __bf16 bf16x8 __attribute__((ext_vector_type(8)));
typedef __bf16 bf16x2 __attribute__((ext_vector_type(2)));
typedef short s16x4 __attribute__((ext_vector_type(4)));
typedef short s16x8 __attribute__((ext_vector_type(8)));
typedef float f32x4 __attribute__((ext_vector_type(4)));
typedef float f32x2 __attribute__((ext_vector_type(2)));
typedef unsigned u32x4 __attribute__((ext_vector_type(4)));
typedef unsigned u32x2 __attribute__((ext_vector_type(2)));

constexpr int S_ = 2048, D_ = 1024, F_ = 3584;
constexpr int NMB = S_ / 16;
constexpr size_t WS_XN = 0;
constexpr size_t WS_A2 = WS_XN + (size_t)32 * NMB * 1024;
constexpr size_t WS_KVP = WS_A2 + (size_t)144 * NMB * 1024;
constexpr int KVP_SZ = 4352;
constexpr size_t WS_PART = WS_KVP + (size_t)8 * 32 * KVP_SZ;
constexpr size_t WS_CNT = WS_PART + (size_t)128 * 32768;
constexpr int C_LN = 0, C_HQ = 64, C_HK = 96, C_O = 128, C_KV = 160, C_P = 224, C_E = 352, C_N = 353;
constexpr size_t WS_END = WS_CNT + C_N * 256;

#define MFMA16(a, b, c) __builtin_amdgcn_mfma_f32_16x16x32_bf16((a), (b), (c), 0, 0, 0)
#define WAIT_V(n) asm volatile("s_waitcnt vmcnt(" #n ")" ::: "memory")
#define WAIT_L0() __builtin_amdgcn_s_waitcnt(0xC07F)
#define MFMA16_ACC(acc, a, b) asm volatile("v_mfma_f32_16x16x32_bf16 %0, %1, %2, %0" : "+v"(acc) : "v"(a), "v"(b))
#define BAR() __builtin_amdgcn_s_barrier()
#define SCHED() __builtin_amdgcn_sched_barrier(0)

DI unsigned pk2(float lo, float hi) {
    f32x2 v = {lo, hi};
    return __builtin_bit_cast(unsigned, __builtin_convertvector(v, bf16x2));
}
DI float bflo(unsigned u) { return __uint_as_float(u << 16); }
DI float bfhi(unsigned u) { return __uint_as_float(u & 0xffff0000u); }
DI float bfr(float x) { return bflo(pk2(x, x)); }
DI bf16x8 pack8(const f32x4& a, const f32x4& b) {
    u32x4 r = {pk2(a[0], a[1]), pk2(a[2], a[3]), pk2(b[0], b[1]), pk2(b[2], b[3])};
    return __builtin_bit_cast(bf16x8, r);
}
DI bf16x8 tr_frag(LAS const char* p0, LAS const char* p1) {
    s16x4 a = __builtin_amdgcn_ds_read_tr16_b64_v4i16((LAS s16x4*)p0);
    s16x4 b = __builtin_amdgcn_ds_read_tr16_b64_v4i16((LAS s16x4*)p1);
    s16x8 c = __builtin_shufflevector(a, b, 0, 1, 2, 3, 4, 5, 6, 7);
    return __builtin_bit_cast(bf16x8, c);
}
template <int OFF> DI void dma1(const char* g, unsigned voff, unsigned ldsdst) {
    asm volatile("s_mov_b32 m0, %2\n\ts_nop 0\n\tglobal_load_lds_dwordx4 %0, %1 offset:%3" :: "v"(voff), "s"(g), "s"(ldsdst), "i"(OFF) : "memory");
}
template <int ADD> DI void dma2s(const char* g, unsigned voff, unsigned ldsdst) {
    asm volatile("s_add_u32 m0, %2, %3\n\ts_nop 0\n\tglobal_load_lds_dwordx4 %0, %1 sc1\n\tglobal_load_lds_dwordx4 %0, %1 offset:1024 sc1"
                 :: "v"(voff), "s"(g), "s"(ldsdst), "i"(ADD) : "memory", "scc");
}
template <int ADD> DI void dma1a(const char* g, unsigned voff, unsigned ldsdst) {
    asm volatile("s_add_u32 m0, %2, %3\n\ts_nop 0\n\tglobal_load_lds_dwordx4 %0, %1" :: "v"(voff), "s"(g), "s"(ldsdst), "i"(ADD) : "memory", "scc");
}
DI void pf_lines(const char* g, unsigned voff, unsigned ldspad, unsigned long long mask) {
    asm volatile("s_mov_b32 m0, %2\n\ts_mov_b64 exec, %3\n\tglobal_load_lds_dword %0, %1\n\ts_mov_b64 exec, -1" :: "v"(voff), "s"(g), "s"(ldspad), "s"(mask) : "memory");
}
typedef __amdgpu_buffer_rsrc_t rsrc_t;
DI rsrc_t mkrsrc(const void* p, unsigned bytes) { return __builtin_amdgcn_make_buffer_rsrc((void*)p, 0, bytes, 0x00020000); }
template <class T> DI void stw(const T& v, rsrc_t r, unsigned voff, unsigned soff) { __builtin_amdgcn_raw_buffer_store_b128(__builtin_bit_cast(u32x4, v), r, voff + soff, 0, 16); }
#define OPAQUE(x) asm volatile("" : "+v"(x))
#define OPAQUE_S(x) asm volatile("" : "+s"(x))
DI float fmx(float a, float b) { return __builtin_elementwise_maximum(a, b); }
DI void swap16(float& a, float& b) { auto r = __builtin_amdgcn_permlane16_swap(__float_as_uint(a), __float_as_uint(b), false, false); a = __uint_as_float(r[0]); b = __uint_as_float(r[1]); }
DI void swap32(float& a, float& b) { auto r = __builtin_amdgcn_permlane32_swap(__float_as_uint(a), __float_as_uint(b), false, false); a = __uint_as_float(r[0]); b = __uint_as_float(r[1]); }
DI void swap16u(unsigned& a, unsigned& b) { auto r = __builtin_amdgcn_permlane16_swap(a, b, false, false); a = r[0]; b = r[1]; }
template <bool MAX> DI void red2(float& a, float& b) {
    swap16(a, b); float v = MAX ? fmx(a, b) : a + b;
    float u = v; swap32(v, u); v = MAX ? fmx(v, u) : v + u;
    a = v; b = v; swap16(a, b);
}

constexpr int G1_SLOT = 36864;
constexpr int G1_WPHI = 149504;
constexpr int G1_PF = G1_WPHI + 4096;
constexpr int G1_ARR = G1_PF + 256;
constexpr int G1_LDS = G1_ARR + 16;
DI void hedgehog4(const f32x4 (&p)[4][2], bf16x8 (&pos)[4], bf16x8 (&neg)[4]) {
    const float L2E = 1.44269504088896340736f;
    float mx[4], nm[4];
#pragma unroll
    for (int m = 0; m < 4; ++m) {
        mx[m] = fmx(fmx(fmx(fmx(p[m][0][0], p[m][0][1]), p[m][0][2]), fmx(fmx(p[m][0][3], p[m][1][0]), p[m][1][1])), fmx(p[m][1][2], p[m][1][3]));
        nm[m] = fmx(fmx(fmx(fmx(-p[m][0][0], -p[m][0][1]), -p[m][0][2]), fmx(fmx(-p[m][0][3], -p[m][1][0]), -p[m][1][1])), fmx(-p[m][1][2], -p[m][1][3]));
    }
#pragma unroll
    for (int m = 0; m < 4; ++m) red2<true>(mx[m], nm[m]);
    f32x2 ep[4][4], en[4][4];
    float sp[4], sn[4];
#pragma unroll
    for (int m = 0; m < 4; ++m) {
        const float cp = -mx[m] * L2E, cn = -nm[m] * L2E;
#pragma unroll
        for (int i = 0; i < 4; ++i) {
            const f32x2 x = {p[m][i >> 1][2 * (i & 1)], p[m][i >> 1][2 * (i & 1) + 1]};
            const f32x2 a = __builtin_elementwise_fma(x, (f32x2){L2E, L2E}, (f32x2){cp, cp}), b = __builtin_elementwise_fma(x, (f32x2){-L2E, -L2E}, (f32x2){cn, cn});
            ep[m][i] = (f32x2){__builtin_amdgcn_exp2f(a[0]), __builtin_amdgcn_exp2f(a[1])};
            en[m][i] = (f32x2){__builtin_amdgcn_exp2f(b[0]), __builtin_amdgcn_exp2f(b[1])};
        }
        const f32x2 tp = (ep[m][0] + ep[m][1]) + (ep[m][2] + ep[m][3]), tn = (en[m][0] + en[m][1]) + (en[m][2] + en[m][3]);
        float t0 = tp[0], t1 = tn[0];
        OPAQUE(t0); OPAQUE(t1);
        sp[m] = t0 + tp[1]; sn[m] = t1 + tn[1];
    }
#pragma unroll
    for (int m = 0; m < 4; ++m) red2<false>(sp[m], sn[m]);
#pragma unroll
    for (int m = 0; m < 4; ++m) {
        const float rp = __builtin_amdgcn_rcpf(sp[m]), rn = __builtin_amdgcn_rcpf(sn[m]);
        u32x4 a, b;
#pragma unroll
        for (int i = 0; i < 4; ++i) { const f32x2 x = ep[m][i] * rp, y = en[m][i] * rn; a[i] = pk2(x[0], x[1]); b[i] = pk2(y[0], y[1]); }
        pos[m] = __builtin_bit_cast(bf16x8, a); neg[m] = __builtin_bit_cast(bf16x8, b);
    }
}
DI u32x2 glu4(const f32x4& g, const f32x4& u) {
    u32x2 r;
#pragma unroll
    for (int i = 0; i < 2; ++i) {
        const f32x2 t = (f32x2){g[2 * i], g[2 * i + 1]} * -1.44269504088896340736f;
        const f32x2 d = (f32x2){__builtin_amdgcn_exp2f(t[0]), __builtin_amdgcn_exp2f(t[1])} + 1.0f;
        const f32x2 h = (f32x2){u[2 * i], u[2 * i + 1]} * (f32x2){__builtin_amdgcn_rcpf(d[0]), __builtin_amdgcn_rcpf(d[1])};
        r[i] = pk2(h[0], h[1]);
    }
    return r;
}
DI u32x4 glu8(const f32x4& g0, const f32x4& u0, const f32x4& g1, const f32x4& u1) { const u32x2 a = glu4(g0, u0), b = glu4(g1, u1); return (u32x4){a[0], a[1], b[0], b[1]}; }

constexpr int G2_SLOT = 32768;
constexpr int G2_NS = 4;
constexpr int G2_KV = G2_NS * G2_SLOT;
static_assert(G2_KV + 8192 + 256 <= G1_WPHI, "");
constexpr int G2_NA = 36;
constexpr int G2_NB = 72 - G2_NA, G2_NBH = G2_NB - 16;
static_assert(G2_NA % 4 == 0 && G2_NA >= 32 && G2_NA <= 36, "");
typedef GAS unsigned gu32;
DI void wait_tags(gu32* p, unsigned seen, unsigned tag) {
    if (__builtin_amdgcn_ballot_w64(seen != tag) != 0ull) {
        unsigned spins = 0;
        while (__builtin_amdgcn_ballot_w64(__hip_atomic_load(p, __ATOMIC_RELAXED, __HIP_MEMORY_SCOPE_AGENT) != tag) != 0ull && ++spins < (1u << 18))
            __builtin_amdgcn_s_sleep(1);
    }
    asm volatile("" ::: "memory");
}
DI void arrive(LAS unsigned* arr, gu32* word, unsigned tag) {
    const int l = threadIdx.x & 63;
    unsigned old = 0;
    if (l == 0) old = __hip_atomic_fetch_add(arr, 1u, __ATOMIC_RELAXED, __HIP_MEMORY_SCOPE_WORKGROUP);
    if (__builtin_amdgcn_readfirstlane(old) == 7u && l < 4) __hip_atomic_store(word + 64 * l, tag, __ATOMIC_RELAXED, __HIP_MEMORY_SCOPE_AGENT);
}
DI float wsum(float v) {
    v += __builtin_amdgcn_update_dpp(0.f, v, 0xb1, 0xf, 0xf, true);
    v += __builtin_amdgcn_update_dpp(0.f, v, 0x4e, 0xf, 0xf, true);
    v += __builtin_amdgcn_update_dpp(0.f, v, 0x141, 0xf, 0xf, true);
    v += __builtin_amdgcn_update_dpp(0.f, v, 0x140, 0xf, 0xf, true);
    float u = v; swap16(v, u); v += u;
    u = v; swap32(v, u); return v + u;
}

#define LP(a) ((LAS const char*)(uintptr_t)(a))
#define G1_STAGE_A(slot) do { dma2s<(slot) * G1_SLOT>(aptr, avoff, adst); aptr += NMB * 1024; OPAQUE_S(aptr); } while (0)
#define G1_STAGE_B(slot) do { \
        dma1a<(slot) * G1_SLOT>(bptr[0], bvoff[0], bdst); dma1a<(slot) * G1_SLOT + 1024>(bptr[1], bvoff[1], bdst); \
        if (grp == 0) dma1a<(slot) * G1_SLOT + 2048>(bptr[2], bvoff[2], bdst); \
        bptr[0] += bstep[0]; bptr[1] += bstep[1]; bptr[2] += bstep[2]; \
        OPAQUE_S(bptr[0]); OPAQUE_S(bptr[1]); OPAQUE_S(bptr[2]); } while (0)
#define G1_STAGE(slot) do { G1_STAGE_A(slot); G1_STAGE_B(slot); } while (0)
#define G1_WAIT() WAIT_V(8)

template <bool kvt>
DI void tail(const uint16_t* __restrict__ x, const uint16_t* __restrict__ wphi, const uint16_t* __restrict__ wdown, const uint16_t* __restrict__ wo, char* __restrict__ ws,
             uint16_t* __restrict__ out, LAS char* lds, const unsigned tag, const char* aptr, const char* bp0, const char* bp1, const char* bp2,
             unsigned bs0, unsigned bs1, unsigned bs2, const char* pw0, const char* pw1, const char* pw2) {
    int tid = threadIdx.x;
    OPAQUE(tid);
    const int l = tid & 63;
    const int w = __builtin_amdgcn_readfirstlane(tid >> 6);
    const int grp = w >> 2, wr = w & 3, wc = grp;
    const int bid = blockIdx.x, idx = bid >> 3, xcd = bid & 7;
    const int pm = idx >> 2;
    const int j = xcd * 2 + ((idx >> 1) & 1);
    const int F0 = kvt ? 2048 + 96 * j : 128 * j;
    gu32* cnt = (gu32*)(ws + WS_CNT);
    const int G = l >> 4, q4 = (l >> 2) & 3, p4 = l & 3;
    const unsigned lbase = (unsigned)(uintptr_t)lds;
    const rsrc_t rw = mkrsrc(ws, (unsigned)WS_END);
    const char* bptr[3] = {bp0, bp1, bp2};
    const unsigned bstep[3] = {bs0, bs1, bs2};
    unsigned bvoff[3];
#pragma unroll
    for (int i = 0; i < 3; ++i) bvoff[i] = (l >> 2) * (bstep[i] >> 5) + ((((l >> 1) & 1) ^ ((l >> 5) & 1)) * 32) + (l & 1) * 16;
    const unsigned avoff = l * 16, adst = lbase + 2 * w * 1024;
    const unsigned bdst = lbase + 16384 + (grp == 0 ? 3 * w : 12 + 2 * (w - 4)) * 1024;

    const unsigned lp0 = G * 512 + q4 * 64 + ((G & 1) * 32) + p4 * 8;
    const unsigned lp1 = lp0 ^ 32;
    unsigned rA = lbase + wr * 4096 + l * 16;
    unsigned rB0 = lbase + 16384 + 5 * wc * 2048 + lp0;
    unsigned rB1 = lbase + 16384 + 5 * wc * 2048 + lp1;
    unsigned rB8 = kvt ? lbase + 16384 + 2 * 2048 + (wc ? lp1 : lp0) : rB0 + 2 * 2048;
    unsigned rB9 = kvt ? lbase + 16384 + 7 * 2048 + (wc ? lp1 : lp0) : rB1 + 2 * 2048;
#define G1_MOVE(d) do { rA += (d); rB0 += (d); rB1 += (d); rB8 += (d); rB9 += (d); \
        OPAQUE(rA); OPAQUE(rB0); OPAQUE(rB1); OPAQUE(rB8); OPAQUE(rB9); } while (0)

    f32x4 acc[4][10];
#pragma unroll
    for (int m = 0; m < 4; ++m)
#pragma unroll
        for (int n = 0; n < 10; ++n) acc[m][n] = (f32x4){0.f, 0.f, 0.f, 0.f};
    bf16x8 fa[4], fb[10];

#define G1_READ(so) do { \
        _Pragma("unroll") for (int m = 0; m < 4; ++m) fa[m] = *(LAS const bf16x8*)(LP(rA) + (so) + m * 1024); \
        _Pragma("unroll") for (int u = 0; u < 5; ++u) if (u != 2) { \
            fb[2 * u] = tr_frag(LP(rB0) + (so) + u * 2048, LP(rB0) + (so) + u * 2048 + 256); \
            fb[2 * u + 1] = tr_frag(LP(rB1) + (so) + u * 2048, LP(rB1) + (so) + u * 2048 + 256); } \
        fb[4] = tr_frag(LP(rB8) + (so), LP(rB8) + (so) + 256); \
        fb[5] = tr_frag(LP(rB9) + (so), LP(rB9) + (so) + 256); } while (0)
#define G1_MMA() do { __builtin_amdgcn_s_setprio(1); \
        _Pragma("unroll") for (int m = 0; m < 4; ++m) _Pragma("unroll") for (int n_ = 0; n_ < 10; ++n_) { const int n = (m & 1) ? 9 - n_ : n_; \
            if (kvt && (n == 2 || n == 3)) MFMA16_ACC(acc[m][n], fa[m], fb[n]); else MFMA16_ACC(acc[m][n], fb[n], fa[m]); } \
        __builtin_amdgcn_s_setprio(0); } while (0)
#define G1_PHASE(so, STG, VW) do { G1_READ(so); STG; WAIT_L0(); VW; SCHED(); BAR(); SCHED(); G1_MMA(); SCHED(); BAR(); SCHED(); } while (0)

    G1_MOVE(0);
#pragma unroll
    for (int m = 0; m < 4; ++m)
#pragma unroll
        for (int n = 0; n < 10; ++n) OPAQUE(acc[m][n]);
    SCHED();
    wait_tags(cnt + 64 * (C_LN + pm * 8 + w) + (l & 3), tag - 1u, tag);
    G1_STAGE_A(0); G1_STAGE_A(1);
    WAIT_V(2);
    BAR();
    G1_STAGE(2);
    if (grp == 1) dma1<0>((const char*)wphi + (size_t)(2 * j) * 2048 + wr * 1024, avoff, lbase + G1_WPHI + wr * 1024);
    if (grp == 1) BAR();
    SCHED();
    for (int it = 0; it < 7; ++it) {
        if (w >= 6) {
            const int c = bid * 2 + w - 6;
            const char* g; int lim;
            if (it < 4) { lim = 480; g = c < 144 ? pw0 + (size_t)(192 + it * 144 + c) * 8192 : (c < 312 ? pw1 - 144 * 8192 : pw2 - 312 * 8192) + (size_t)(224 + it * 168 + c) * 8192; }
            else { const int u = (it - 4) * 384 + c; lim = 384; g = u < 896 ? (const char*)wdown + (size_t)u * 8192 : (const char*)wo + (size_t)(u - 896) * 8192; }
            if (c < lim) pf_lines(g, avoff * 8, lbase + G1_PF, ~0ull);
        }
        G1_PHASE(0, G1_STAGE(3), G1_WAIT());
        G1_PHASE(G1_SLOT, G1_STAGE(0), G1_WAIT());
        G1_MOVE(2 * G1_SLOT);
        G1_PHASE(0, G1_STAGE(1), G1_WAIT());
        G1_PHASE(G1_SLOT, G1_STAGE(2), G1_WAIT());
        G1_MOVE(-2 * G1_SLOT);
    }
    G1_PHASE(0, G1_STAGE(3), G1_WAIT());
    G1_PHASE(G1_SLOT, (void)0, WAIT_V(4));
    G1_MOVE(2 * G1_SLOT);
    G1_PHASE(0, (void)0, WAIT_V(0));
    G1_READ(G1_SLOT); WAIT_L0(); SCHED(); BAR(); SCHED(); G1_MMA(); SCHED();
    if (grp == 0) BAR();
    asm volatile("s_nop 7\n\ts_nop 7" ::: "memory");
    SCHED();
    constexpr bool tb = !kvt;
    const int t16 = idx >> 1;
    const int pm2 = (xcd >> 1) * 4 + (t16 & 3), pn = (xcd & 1) * 4 + (t16 >> 2);
    const int tile = pm2 * 8 + pn;
    const int prep = 4 * (pm2 >> 1) + (xcd & 1) * 2 + (wr & 1);
    constexpr int k0 = tb ? 0 : G2_NBH;
    const int col0 = pn * 128;
    const unsigned lo16 = l * 16;
    const bool isb = wr >= 2;
    const char* gptr = isb ? (const char*)(wdown + (size_t)(64 * k0 + 32 * (wr - 2)) * D_ + col0) : ws + WS_A2 + ((size_t)(2 * k0 + wr) * NMB + pm2 * 8) * 1024;
    const char* gptr_o = isb ? (const char*)(wo + (size_t)(32 * (wr & 1)) * D_ + col0) : ws + WS_A2 + ((size_t)(112 + wr) * NMB + pm2 * 8) * 1024;
    const unsigned gstep = isb ? 64 * D_ * 2 : 2 * NMB * 1024;
    unsigned voff[4];
#pragma unroll
    for (int i = 0; i < 4; ++i) {
        const int r = 4 * i + G;
        voff[i] = isb ? r * (D_ * 2) + ((((l & 15) >> 1) ^ (r & 7)) * 32) + (l & 1) * 16 - 1024 * i : lo16;
    }
    const unsigned ghalf = isb ? 16 * D_ * 2 : 4096;
    unsigned dst = lbase + wr * 8192;
    int tld = 0, slot = 0;
#define G2_DMA4_(g, d, SC) asm volatile("s_mov_b32 m0, %5\n\ts_nop 0\n\tglobal_load_lds_dwordx4 %0, %4" SC "\n\tglobal_load_lds_dwordx4 %1, %4 offset:1024" SC "\n\t" \
            "global_load_lds_dwordx4 %2, %4 offset:2048" SC "\n\tglobal_load_lds_dwordx4 %3, %4 offset:3072" SC \
            :: "v"(voff[0]), "v"(voff[1]), "v"(voff[2]), "v"(voff[3]), "s"(g), "s"(d) : "memory")
#define G2_DMA4(g, d) G2_DMA4_(g, d, " sc1")
#define G2_NEXTSLOT() do { ++slot; const int wrap = slot == G2_NS; slot = wrap ? 0 : slot; dst += wrap ? (unsigned)(-(G2_NS - 1) * G2_SLOT) : (unsigned)G2_SLOT; } while (0)
    constexpr int G2_W = 4;
    const int rot = (t16 & 3) + (t16 >> 2);
#define G2_STAGE() do { const char* g_ = gptr + (size_t)((tld + rot) & (G2_W - 1)) * gstep; \
        if (!isb) { G2_DMA4(g_, dst); G2_DMA4(g_ + ghalf, dst + 4096); } else { G2_DMA4_(g_, dst, ""); G2_DMA4_(g_ + ghalf, dst + 4096, ""); } \
        ++tld; gptr = (tb && tld == G2_NBH) ? gptr_o : (tld & (G2_W - 1)) == 0 ? gptr + G2_W * gstep : gptr; G2_NEXTSLOT(); } while (0)
#define G2_PRUN(n) do { _Pragma("unroll 1") for (int t_ = 0; t_ < (n); ++t_) { WAIT_V(16); BAR(); G2_STAGE(); } } while (0)
#define POLL(dst_, p_) asm volatile("global_load_dword %0, %1, off sc1" : "=v"(dst_) : "v"(p_) : "memory")
    const size_t off0 = (size_t)(pm2 * 128 + (wr >> 1) * 64 + (l & 15)) * D_ + col0 + (wr & 1) * 64 + (G & 1) * 16 + (G >> 1) * 8;

    const int fq = l >> 4, fr = l & 15;
    const int hh = 2 * j + wc;
    const int mb0 = pm * 16 + wr * 4;
    if (w >= 6) { G2_STAGE(); G2_STAGE(); }
    const f32x4 z4 = {0.f, 0.f, 0.f, 0.f};
    const unsigned hb = (unsigned)WS_A2 + (((kvt ? F0 + 32 * wc : F0 + 64 * wc + 32) >> 5) * NMB + mb0) * 1024;
    f32x4 pf[4][2];
    bf16x8 pos[4], neg[4];
    u32x4 xv[4][2];
#define G1_LOGITS() do { bf16x8 wp[2]; \
        _Pragma("unroll") for (int c = 0; c < 2; ++c) { LAS const char* p = lds + G1_WPHI + wc * 2048 + (4 * G + q4) * 64 + c * 32 + p4 * 8; wp[c] = tr_frag(p, p + 1024); } \
        _Pragma("unroll") for (int m = 0; m < 4; ++m) { const bf16x8 qf = pack8(acc[m][0], acc[m][1]); pf[m][0] = MFMA16(wp[0], qf, z4); pf[m][1] = MFMA16(wp[1], qf, z4); } } while (0)
    __builtin_amdgcn_s_setprio(1);
    if constexpr (!kvt) {
#pragma unroll
        for (int m = 0; m < 4; ++m) {
            stw(glu8(acc[m][2], acc[m][4], acc[m][3], acc[m][5]), rw, lo16 + m * 1024, hb - NMB * 1024);
            stw(glu8(acc[m][6], acc[m][8], acc[m][7], acc[m][9]), rw, lo16 + m * 1024, hb);
        }
        SCHED();
        G1_LOGITS();
        SCHED();
        WAIT_V(0); arrive((LAS unsigned*)(lds + G1_ARR), cnt + 64 * (C_HQ + 4 * pm) + j, tag);
        __builtin_amdgcn_s_setprio(0);
        SCHED();
        if (w == 4 || w == 5) { wait_tags(cnt + 64 * (C_HQ + prep) + (l & 15), tag - 1u, tag); G2_STAGE(); G2_STAGE(); }
        hedgehog4(pf, pos, neg);
    } else {
        G1_LOGITS();
        u32x4 i0 = {0u, 0u, 0u, 0u}, i1 = {0u, 0u, 0u, 0u};
        {
            const int d = fr - 4 * fq;
            const unsigned lo = (d == 0) ? 0x3f80u : (d == 1) ? 0x3f800000u : 0u;
            const unsigned hi = (d == 2) ? 0x3f80u : (d == 3) ? 0x3f800000u : 0u;
            i0[0] = lo; i0[1] = hi; i1[2] = lo; i1[3] = hi;
        }
        const bf16x8 I0 = __builtin_bit_cast(bf16x8, i0), I1 = __builtin_bit_cast(bf16x8, i1);
        constexpr int RED = 65536, ZRED = RED + 65536, HX = ZRED + 2048;
        static_assert(HX + 16384 <= G1_WPHI, "");
        bf16x8 V[2][2];
#pragma unroll
        for (int mp = 0; mp < 2; ++mp) { V[mp][0] = pack8(acc[2 * mp][2], acc[2 * mp + 1][2]); V[mp][1] = pack8(acc[2 * mp][3], acc[2 * mp + 1][3]); }
        SCHED();
        hedgehog4(pf, pos, neg);
        SCHED();
        const u32x4 o4 = {0x3f803f80u, 0x3f803f80u, 0x3f803f80u, 0x3f803f80u};
        const bf16x8 ONES = __builtin_bit_cast(bf16x8, o4);
        f32x4 T[4][2][2];
#pragma unroll
        for (int m = 0; m < 4; ++m) {
            T[m][0][0] = MFMA16(pos[m], I0, z4); T[m][0][1] = MFMA16(pos[m], I1, z4);
            T[m][1][0] = MFMA16(neg[m], I0, z4); T[m][1][1] = MFMA16(neg[m], I1, z4);
        }
#pragma unroll
        for (int m = 0; m < 4; ++m) *(LAS u32x2*)(lds + HX + (wr * 4 + m) * 1024 + lo16 + 8 * wc) = glu4(acc[m][4], acc[m][5]);
        LAS char* red = lds + RED + (wc * 4 + wr) * 8192 + lo16;
#pragma unroll
        for (int s = 0; s < 2; ++s)
#pragma unroll
            for (int cc = 0; cc < 2; ++cc) {
                const bf16x8 P0 = pack8(T[0][s][cc], T[1][s][cc]), P1 = pack8(T[2][s][cc], T[3][s][cc]);
                const f32x4 k0_ = MFMA16(P1, V[1][0], MFMA16(P0, V[0][0], z4));
                const f32x4 k1_ = MFMA16(P1, V[1][1], MFMA16(P0, V[0][1], z4));
                const f32x4 zz = MFMA16(P1, ONES, MFMA16(P0, ONES, z4));
                stw(glu8(acc[2 * s + cc][6], acc[2 * s + cc][8], acc[2 * s + cc][7], acc[2 * s + cc][9]), rw, lo16 + (2 * s + cc) * 1024, hb);
                *(LAS f32x4*)(red + ((s * 2 + 0) * 2 + cc) * 1024) = k0_;
                *(LAS f32x4*)(red + ((s * 2 + 1) * 2 + cc) * 1024) = k1_;
                *(LAS f32x4*)(lds + ZRED + (wc * 4 + wr) * 256 + (s * 32 + cc * 16 + 4 * fq) * 4) = zz;
            }
        SCHED();
        if (w < 4) {
#pragma unroll
            for (int m = 0; m < 4; ++m) { xv[m][0] = *(const u32x4*)(x + off0 + m * 16 * D_); xv[m][1] = *(const u32x4*)(x + off0 + m * 16 * D_ + 32); }
        } else if (w < 6) { wait_tags(cnt + 64 * (C_HQ + prep) + (l & 15), tag - 1u, tag); G2_STAGE(); G2_STAGE(); }
        WAIT_L0(); SCHED();
        BAR(); SCHED();
        const unsigned kvo = (unsigned)WS_KVP + (pm * 32 + hh) * KVP_SZ;
        f32x4 sum[2];
#pragma unroll
        for (int i = 0; i < 2; ++i) {
            LAS const char* p = lds + RED + wc * 32768 + (2 * wr + i) * 1024 + lo16;
            sum[i] = (*(LAS const f32x4*)p + *(LAS const f32x4*)(p + 8192)) + (*(LAS const f32x4*)(p + 16384) + *(LAS const f32x4*)(p + 24576));
            stw(*(LAS const u32x4*)(lds + HX + (wr * 4 + 2 * wc + i) * 1024 + lo16), rw, lo16 + i * 1024, (unsigned)WS_A2 + (((F0 + 64) >> 5) * NMB + mb0 + 2 * wc) * 1024);
        }
        stw(pack8(sum[0], sum[1]), rw, lo16, kvo + wr * 1024);
        {
            LAS const char* p = lds + ZRED + wc * 1024 + (l & 15) * 16;
            const f32x4 zs = (*(LAS const f32x4*)p + *(LAS const f32x4*)(p + 256)) + (*(LAS const f32x4*)(p + 512) + *(LAS const f32x4*)(p + 768));
            stw(zs, rw, (wr == 0 && l < 16) ? lo16 : 0x80000000u, kvo + 4096);
        }
        __builtin_amdgcn_s_setprio(0);
        WAIT_L0();
    }
    SCHED();
#define G2_OCOMP() do { \
        bf16x8 kf[2][2], zf[2]; \
        _Pragma("unroll") for (int s = 0; s < 2; ++s) { \
            kf[s][0] = *(LAS const bf16x8*)(lds + G2_KV + wc * 4096 + (s * 2) * 1024 + lo16); kf[s][1] = *(LAS const bf16x8*)(lds + G2_KV + wc * 4096 + (s * 2 + 1) * 1024 + lo16); \
            const u32x2 za = *(LAS const u32x2*)(lds + G2_KV + 8192 + wc * 128 + s * 64 + G * 8), zb = *(LAS const u32x2*)(lds + G2_KV + 8192 + wc * 128 + s * 64 + 32 + G * 8); \
            zf[s] = __builtin_bit_cast(bf16x8, (u32x4){za[0], za[1], zb[0], zb[1]}); } \
        _Pragma("unroll") for (int m = 0; m < 4; ++m) { \
            const f32x4 dn = MFMA16(zf[1], neg[m], MFMA16(zf[0], pos[m], z4)); \
            const f32x4 n0 = MFMA16(kf[1][0], neg[m], MFMA16(kf[0][0], pos[m], z4)), n1 = MFMA16(kf[1][1], neg[m], MFMA16(kf[0][1], pos[m], z4)); \
            const float rd = __builtin_amdgcn_rcpf(dn[0] + 1e-6f); \
            stw(pack8(n0 * rd, n1 * rd), rw, lo16 + m * 1024, (unsigned)WS_A2 + ((112 + hh) * NMB + mb0) * 1024); } \
        WAIT_L0(); SCHED(); } while (0)

    if (w >= 4) {
        unsigned seen;
        WAIT_V(8);
        BAR(); G2_STAGE();
        WAIT_V(8); BAR();
        if (!tb && w == 7 && l < 8) __hip_atomic_store(l < 4 ? cnt + 64 * (C_KV + 4 * j + l) + pm : cnt + 64 * (C_HK + 4 * pm + l - 4) + j, tag, __ATOMIC_RELAXED, __HIP_MEMORY_SCOPE_AGENT);
        G2_STAGE(); G2_STAGE();
        if constexpr (!tb) {
            constexpr int UK = 32 - G2_NBH - 4;
            G2_PRUN(UK - 3);
            POLL(seen, cnt + 64 * (C_HK + prep) + (l & 15));
            G2_PRUN(2);
            asm volatile("s_waitcnt vmcnt(16)" : "+v"(seen) :: "memory");
            if (!isb) wait_tags(cnt + 64 * (C_HK + prep) + (l & 15), seen, tag);
            BAR(); G2_STAGE();
            G2_PRUN(G2_NA - 5 - UK);
            WAIT_V(16); BAR(); WAIT_V(8); BAR(); WAIT_V(0); BAR();
        } else {
            G2_PRUN(1);
            POLL(seen, cnt + 64 * (C_KV + 4 * j + wr) + (l & 7));
            G2_PRUN(2);
            asm volatile("s_waitcnt vmcnt(16)" : "+v"(seen) :: "memory");
            wait_tags(cnt + 64 * (C_KV + 4 * j + wr) + (l & 7), seen, tag);
            u32x4 t[2][8]; float zt[8];
            const char* kvs = ws + WS_KVP + (size_t)(2 * j + (wr >> 1)) * KVP_SZ;
            const unsigned kvv = (wr & 1) * 2048 + lo16, zv = 4096 + l * 4;
#define G2_KVLD(i) do { _Pragma("unroll") for (int k = 0; k < 8; ++k) \
                asm volatile("global_load_dwordx4 %0, %1, %2 offset:%3 sc1" : "=v"(t[i][k]) : "v"(kvv), "s"(kvs + (size_t)k * 32 * KVP_SZ), "i"((i) * 1024) : "memory"); \
            _Pragma("unroll") for (int k = 4 * (i); k < 4 * (i) + 4; ++k) \
                asm volatile("global_load_dword %0, %1, %2 sc1" : "=v"(zt[k]) : "v"(zv), "s"(kvs + (size_t)k * 32 * KVP_SZ) : "memory"); } while (0)
            BAR(); G2_STAGE(); G2_KVLD(0);
            WAIT_V(28); BAR(); G2_STAGE(); G2_KVLD(1);
            WAIT_V(40); BAR(); G2_STAGE();
            WAIT_V(40); BAR(); G2_STAGE();
            WAIT_V(28); BAR(); G2_STAGE();
            WAIT_V(16); SCHED();
#pragma unroll
            for (int i = 0; i < 2; ++i) {
                asm volatile("" : "+v"(t[i][0]), "+v"(t[i][1]), "+v"(t[i][2]), "+v"(t[i][3]), "+v"(t[i][4]), "+v"(t[i][5]), "+v"(t[i][6]), "+v"(t[i][7]),
                             "+v"(zt[4 * i]), "+v"(zt[4 * i + 1]), "+v"(zt[4 * i + 2]), "+v"(zt[4 * i + 3]));
                u32x4 r;
#pragma unroll
                for (int e = 0; e < 4; ++e) {
                    float lo = 0.f, hi = 0.f;
#pragma unroll
                    for (int k = 0; k < 8; ++k) { lo += bflo(t[i][k][e]); hi += bfhi(t[i][k][e]); }
                    r[e] = pk2(lo, hi);
                }
                *(LAS u32x4*)(lds + G2_KV + (wr >> 1) * 4096 + (2 * (wr & 1) + i) * 1024 + lo16) = r;
            }
            const float z = ((zt[0] + zt[1]) + (zt[2] + zt[3])) + ((zt[4] + zt[5]) + (zt[6] + zt[7]));
            *(LAS unsigned short*)(lds + G2_KV + 8192 + (wr >> 1) * 128 + l * 2) = (unsigned short)pk2(z, z);
            WAIT_L0(); SCHED();
            BAR(); G2_STAGE();
            G2_OCOMP();
            G2_PRUN(3);
            if (w == 7 && l < 4) __hip_atomic_store(cnt + 64 * (C_O + 4 * pm + l) + j, tag, __ATOMIC_RELAXED, __HIP_MEMORY_SCOPE_AGENT);
            G2_PRUN(G2_NB - 35);
            gu32* f0 = cnt + 64 * (C_O + prep) + (l & 15);
            POLL(seen, f0);
            G2_PRUN(2);
            asm volatile("s_waitcnt vmcnt(16)" : "+v"(seen) :: "memory");
            if (!isb) wait_tags(f0, seen, tag);
            BAR(); G2_STAGE();
            G2_PRUN(15);
            gu32* f1 = cnt + 64 * (C_P + tile);
            WAIT_V(16); BAR(); WAIT_V(8); BAR();
            POLL(seen, f1);
            WAIT_V(1); BAR();
            asm volatile("s_waitcnt vmcnt(0)" : "+v"(seen) :: "memory");
            wait_tags(f1, seen, tag);
            char* part = ws + WS_PART + (size_t)tile * 32768;
#pragma unroll
            for (int i = 0; i < 4; ++i) voff[i] = lo16;
            G2_DMA4(part + wr * 8192, lbase + wr * 8192); G2_DMA4(part + wr * 8192 + 4096, lbase + wr * 8192 + 4096);
            WAIT_V(0);
            BAR();
        }
        return;
    }
    const int cr = w >> 1, cc2 = w & 1;
    f32x4 ac2[4][4];
#pragma unroll
    for (int m = 0; m < 4; ++m)
#pragma unroll
        for (int i = 0; i < 2; ++i) {
            if constexpr (tb) { ac2[m][2 * i] = z4; ac2[m][2 * i + 1] = z4; }
            else {
                unsigned d0 = xv[m][i][0], d1 = xv[m][i][1], d2 = xv[m][i][2], d3 = xv[m][i][3];
                swap16u(d0, d2); swap16u(d1, d3);
                ac2[m][2 * i] = (f32x4){bflo(d0), bfhi(d0), bflo(d1), bfhi(d1)}; ac2[m][2 * i + 1] = (f32x4){bflo(d2), bfhi(d2), bflo(d3), bfhi(d3)};
            }
        }
    const int r8 = 4 * (G & 1) + q4;
    unsigned sA = lbase + cr * 4096 + lo16;
    unsigned sB[4];
#pragma unroll
    for (int n = 0; n < 4; ++n) sB[n] = lbase + 16384 + (4 * G + q4) * 256 + (((4 * cc2 + n) ^ r8) * 32) + p4 * 8;
#define G2_MOVE(d) do { sA += (d); sB[0] += (d); sB[1] += (d); sB[2] += (d); sB[3] += (d); OPAQUE(sA); OPAQUE(sB[0]); OPAQUE(sB[1]); OPAQUE(sB[2]); OPAQUE(sB[3]); } while (0)
    bf16x8 ga[2][2][4], gb[2][2][4];
#define G2_RD_A(buf, so, kk, m) ga[buf][kk][m] = *(LAS const bf16x8*)(LP(sA) + (so) + (kk) * 8192 + (m) * 1024)
#define G2_RD_B(buf, so, kk, n) gb[buf][kk][n] = tr_frag(LP(sB[n]) + (so) + (kk) * 8192, LP(sB[n]) + (so) + (kk) * 8192 + 4096)
#define G2_READ(buf, so) do { _Pragma("unroll") for (int kk = 0; kk < 2; ++kk) _Pragma("unroll") for (int i = 0; i < 4; ++i) { G2_RD_B(buf, so, kk, i); G2_RD_A(buf, so, kk, i); } } while (0)
#define G2_STEP(cur, so, RD) do { if (RD) BAR(); SCHED(); \
        _Pragma("unroll") for (int kk = 0; kk < 2; ++kk) _Pragma("unroll") for (int m = 0; m < 4; ++m) _Pragma("unroll") for (int n_ = 0; n_ < 4; ++n_) { \
            const int n = (m & 1) ? 3 - n_ : n_; \
            MFMA16_ACC(ac2[m][n], gb[cur][kk][n], ga[cur][kk][m]); \
            if ((RD) && kk == 0) { const int i_ = m * 4 + n_; if (i_ & 1) G2_RD_A((cur) ^ 1, so, i_ >> 3, (i_ >> 1) & 3); else G2_RD_B((cur) ^ 1, so, i_ >> 3, (i_ >> 1) & 3); } \
            SCHED(); } \
        WAIT_L0(); SCHED(); } while (0)
    unsigned mv = 2 * G2_SLOT;
#define G2_RUN(n) do { _Pragma("unroll 1") for (int i_ = 0; i_ < (n); ++i_) { G2_STEP(0, G2_SLOT, 1); G2_MOVE(mv); mv = -mv; G2_STEP(1, 0, 1); } } while (0)

    G2_MOVE(0);
#pragma unroll
    for (int m = 0; m < 4; ++m)
#pragma unroll
        for (int n = 0; n < 4; ++n) OPAQUE(ac2[m][n]);
    SCHED(); BAR(); SCHED();
    G2_READ(0, 0);
    WAIT_L0(); SCHED();
    if constexpr (!tb) WAIT_V(0);
    if constexpr (tb) {
        G2_RUN(5);
        G2_OCOMP();
        G2_RUN(1);
        WAIT_V(0); SCHED();
        G2_RUN(G2_NB / 2 - 7);
    } else {
        G2_RUN(G2_NA / 2 - 1);
    }
    G2_STEP(0, G2_SLOT, 1);
    G2_STEP(1, 0, 0);
    asm volatile("s_nop 7\n\ts_nop 7" ::: "memory");
    SCHED();
    if constexpr (!tb) {
#pragma unroll
        for (int m = 0; m < 4; ++m)
#pragma unroll
            for (int i = 0; i < 2; ++i) stw(pack8(ac2[m][2 * i], ac2[m][2 * i + 1]), rw, lo16 + (m & 1) * 2048 + i * 1024, (unsigned)WS_PART + tile * 32768 + w * 8192 + (m >> 1) * 4096);
        WAIT_V(0); BAR();
        if (tid == 0) __hip_atomic_store(cnt + 64 * (C_P + tile), tag, __ATOMIC_RELAXED, __HIP_MEMORY_SCOPE_AGENT);
    } else {
        BAR(); SCHED();
#pragma unroll
        for (int m = 0; m < 4; ++m)
#pragma unroll
            for (int i = 0; i < 2; ++i) {
                const u32x4 pp = *(LAS const u32x4*)(lds + w * 8192 + (m * 2 + i) * 1024 + lo16);
                const f32x4 r0 = ac2[m][2 * i] + (f32x4){bflo(pp[0]), bfhi(pp[0]), bflo(pp[1]), bfhi(pp[1])};
                const f32x4 r1 = ac2[m][2 * i + 1] + (f32x4){bflo(pp[2]), bfhi(pp[2]), bflo(pp[3]), bfhi(pp[3])};
                unsigned d0 = pk2(r0[0], r0[1]), d1 = pk2(r0[2], r0[3]), d2 = pk2(r1[0], r1[1]), d3 = pk2(r1[2], r1[3]);
                swap16u(d0, d2); swap16u(d1, d3);
                *(u32x4*)(out + off0 + m * 16 * D_ + i * 32) = (u32x4){d0, d1, d2, d3};
            }
        if (bid == 0 && tid == 0) __hip_atomic_store(cnt + 64 * C_E, tag, __ATOMIC_RELAXED, __HIP_MEMORY_SCOPE_AGENT);
    }
}

__global__ __launch_bounds__(512, 2) void k_main(const uint16_t* __restrict__ x, const uint16_t* __restrict__ wqkv, const uint16_t* __restrict__ wphi,
                                                const uint16_t* __restrict__ wgate, const uint16_t* __restrict__ wup, const uint16_t* __restrict__ wdown,
                                                const uint16_t* __restrict__ wo, char* __restrict__ ws, uint16_t* __restrict__ out) {
    __shared__ __attribute__((aligned(16))) char smem_[G1_LDS];
    LAS char* lds = (LAS char*)smem_;
    const int tid = threadIdx.x, l = tid & 63;
    const int w = __builtin_amdgcn_readfirstlane(tid >> 6);
    const int grp = w >> 2;
    const int bid = blockIdx.x, idx = bid >> 3, xcd = bid & 7;
    const int pm = idx >> 2;
    const int j = xcd * 2 + ((idx >> 1) & 1);
    const bool kvt = (idx & 1) != 0;
    const int F0 = kvt ? 2048 + 96 * j : 128 * j;
    gu32* cnt = (gu32*)(ws + WS_CNT);
    const int lnslot = xcd * 4 + (idx & 3);
    u32x4 xa, xb; unsigned tag;
    {
        const char* xr = (const char*)x + (size_t)(pm * 256 + lnslot * 8 + w) * 2048;
        asm volatile("global_load_dwordx4 %0, %3, %4\n\tglobal_load_dwordx4 %1, %3, %4 offset:1024\n\tglobal_load_dword %2, %5, off sc1"
                     : "=&v"(xa), "=&v"(xb), "=&v"(tag) : "v"(l * 16), "s"(xr), "v"(cnt + 64 * C_E) : "memory");
    }

    const char* aptr = ws + WS_XN + (size_t)(pm * 16 + 2 * w) * 1024;
    const char* bptr[3]; unsigned bstep[3], bvoff[3];
    const unsigned bdst = (unsigned)(uintptr_t)lds + 16384 + (grp == 0 ? 3 * w : 12 + 2 * (w - 4)) * 1024;
#pragma unroll
    for (int i = 0; i < 3; ++i) {
        int t = grp == 0 ? 3 * w + i : 12 + 2 * (w - 4) + (i < 2 ? i : 0);
        const unsigned e = (unsigned)((kvt ? 0x491898502061204ull : 0xcb18a1412442040ull) >> (6 * (t >> 1))) & 63u;
        const unsigned mat = e & 3u, stride = mat == 0 ? 3 * D_ : F_;
        const uintptr_t base = mat == 0 ? (uintptr_t)wqkv : mat == 1 ? (uintptr_t)wgate : (uintptr_t)wup;
        bptr[i] = (const char*)(base + ((t & 1) * 16 * stride + (mat == 0 ? 64 * j : F0) + ((e >> 2) & 3u) * 1024 + (e >> 4) * 32) * 2u);
        bstep[i] = 64u * stride;
        bvoff[i] = (l >> 2) * stride * 2 + ((((l >> 1) & 1) ^ ((l >> 5) & 1)) * 32) + (l & 1) * 16;
    }
    if (tid < 2) *(LAS unsigned*)(lds + G1_ARR + tid * 4) = 0u;
    G1_STAGE_B(0); G1_STAGE_B(1);
    if (grp == 0) WAIT_V(6); else WAIT_V(4);
    asm volatile("" : "+v"(xa), "+v"(xb), "+v"(tag) :: "memory");
    tag = __builtin_amdgcn_readfirstlane(tag) + 1u;
    const rsrc_t rw = mkrsrc(ws, (unsigned)WS_END);
    {
        float f[16];
#pragma unroll
        for (int i = 0; i < 4; ++i) { f[2 * i] = bflo(xa[i]); f[2 * i + 1] = bfhi(xa[i]); f[8 + 2 * i] = bflo(xb[i]); f[9 + 2 * i] = bfhi(xb[i]); }
        float sm = 0.f;
#pragma unroll
        for (int i = 0; i < 16; ++i) sm += f[i];
        const float mean = wsum(sm) * (1.0f / 1024.0f);
        float q = 0.f;
#pragma unroll
        for (int i = 0; i < 16; ++i) { const float d = f[i] - mean; q += d * d; }
        const float rs = __builtin_amdgcn_rcpf(bfr(__builtin_amdgcn_sqrtf(bfr(bfr(wsum(q) * (1.0f / 1024.0f)) + 1e-5f))));
        const float mb = bfr(mean);
#pragma unroll
        for (int i = 0; i < 8; ++i) { const unsigned d2 = pk2(f[2 * i] - mb, f[2 * i + 1] - mb); f[2 * i] = bflo(d2); f[2 * i + 1] = bfhi(d2); }
        LAS char* ex = lds + 3 * G1_SLOT;
        *(LAS u32x4*)(ex + l * 144 + w * 16) = (u32x4){pk2(f[0] * rs, f[1] * rs), pk2(f[2] * rs, f[3] * rs), pk2(f[4] * rs, f[5] * rs), pk2(f[6] * rs, f[7] * rs)};
        *(LAS u32x4*)(ex + (64 + l) * 144 + w * 16) = (u32x4){pk2(f[8] * rs, f[9] * rs), pk2(f[10] * rs, f[11] * rs), pk2(f[12] * rs, f[13] * rs), pk2(f[14] * rs, f[15] * rs)};
        WAIT_L0(); SCHED(); BAR(); SCHED();
#pragma unroll
        for (int i = 0; i < 2; ++i) {
            const int c = i * 64 + (tid >> 3), r = tid & 7;
            stw(*(LAS const u32x4*)(ex + c * 144 + r * 16), rw, ((c >> 2) * NMB * 1024) + ((c & 3) * 16 + r) * 16, (unsigned)WS_XN + (pm * 16 + (lnslot >> 1)) * 1024 + (lnslot & 1) * 128);
        }
        SCHED();
        WAIT_V(0);
        SCHED(); BAR(); SCHED();
        if (tid == 0) __hip_atomic_store(cnt + 64 * (C_LN + pm * 8 + (lnslot >> 2)) + (lnslot & 3), tag, __ATOMIC_RELAXED, __HIP_MEMORY_SCOPE_AGENT);
        SCHED();
    }
    if (kvt) tail<true>(x, wphi, wdown, wo, ws, out, lds, tag, aptr, bptr[0], bptr[1], bptr[2], bstep[0], bstep[1], bstep[2], (const char*)wqkv, (const char*)wgate, (const char*)wup);
    else tail<false>(x, wphi, wdown, wo, ws, out, lds, tag, aptr, bptr[0], bptr[1], bptr[2], bstep[0], bstep[1], bstep[2], (const char*)wqkv, (const char*)wgate, (const char*)wup);
}

extern "C" void kernel_launch(void* const* d_in, const int* in_sizes, int n_in, void* d_out, int out_size, void* d_ws, size_t ws_size,
                              hipStream_t stream) {
    const uint16_t* x = (const uint16_t*)d_in[0];
    const uint16_t* wqkv = (const uint16_t*)d_in[1];
    const uint16_t* wphi = (const uint16_t*)d_in[2];
    const uint16_t* wo = (const uint16_t*)d_in[3];
    const uint16_t* wgate = (const uint16_t*)d_in[5];
    const uint16_t* wup = (const uint16_t*)d_in[6];
    const uint16_t* wdown = (const uint16_t*)d_in[7];
    char* ws = (char*)d_ws;
    k_main<<<256, 512, 0, stream>>>(x, wqkv, wphi, wgate, wup, wdown, wo, ws, (uint16_t*)d_out);
}
```
